```python
import torch

def _softmax(x, axis=-1):
    return torch.softmax(x, dim=axis)
def _gelu(x):
    return torch.nn.functional.gelu(x, approximate="tanh")
def _silu(x):
    return torch.nn.functional.silu(x)
def _sigmoid(x):
    return torch.sigmoid(x)
def _layernorm(x, axis=-1, eps=1e-5):
    m = x.mean(dim=axis, keepdim=True)
    v = x.var(dim=axis, keepdim=True, unbiased=False)
    return (x - m) / torch.sqrt(v + eps)
def _scatter_add(tgt, idx, src, axis=0):
    return tgt.clone().index_add_(axis, idx, src)
def _segment_sum(src, seg, n):
    out = torch.zeros((n,) + src.shape[1:], dtype=src.dtype)
    return out.index_add_(0, seg, src)
def _pad(x, pw):
    flat = tuple(v for pair in reversed(pw) for v in pair)
    return torch.nn.functional.pad(x, flat)

kernel_name = 'concat_heads__mha_block__sliding_local_dilated__prompt'

def setup_inputs():

    _w = (lambda a: (lambda t: t.to(torch.bfloat16) if t.is_floating_point() else t)(torch.as_tensor(a)))
    def _A_setup():
        B, S, D, H = 1, 2048, 1024, 32
        return {
            "x": _w((lambda *s: torch.randn(*s, dtype=torch.bfloat16) * 0.1)(B, S, D)),
            "wqkv": _w((lambda *s: torch.randn(*s, dtype=torch.bfloat16) * 0.4)(D, 3 * D)),
            "wo": _w((lambda *s: torch.randn(*s, dtype=torch.bfloat16) * 0.8)(D, D)),
        }
    _A_in = _A_setup()

    def _B_setup():
        B, S, D = 1, 2048, 1024
        return {
            "x": _w((lambda *s: torch.randn(*s, dtype=torch.bfloat16) * 0.1)(B, S, D)),
            "wqkv": _w((lambda *s: torch.randn(*s, dtype=torch.bfloat16) * 0.4)(D, 3 * D)),
            "wo": _w((lambda *s: torch.randn(*s, dtype=torch.bfloat16) * 0.8)(D, D)),
        }
    _B_in = _B_setup()

    return {
        "A_x": _A_in["x"],
        "A_wqkv": _A_in["wqkv"],
        "A_wo": _A_in["wo"],
        "B_x": _B_in["x"],
        "B_wqkv": _B_in["wqkv"],
        "B_wo": _B_in["wo"],
    }

def reference(A_x, A_wqkv, A_wo, B_x, B_wqkv, B_wo):
    def _A_ref(x, wqkv, wo):
        B, S, D = x.shape
        H = 32
        Dh = D // H
        qkv = (lambda x, *d: x.permute(*d))((x @ wqkv).reshape(B, S, 3, H, Dh), 2, 0, 3, 1, 4)
        q, k, v = qkv[0], qkv[1], qkv[2]
        scores = torch.einsum("bhqd,bhkd->bhqk", q, k) * (Dh ** -0.5)
        p = _softmax(scores, axis=-1)
        o = torch.einsum("bhqk,bhkd->bhqd", p, v)
        return (lambda x, *d: x.permute(*d))(o, 0, 2, 1, 3).reshape(B, S, D) @ wo

    def _B_ref(x, wqkv, wo):
        B, S, D = x.shape
        H = 32; Dh = D // H
        W, dil = 8, 2
        qkv = (lambda x, *d: x.permute(*d))((x @ wqkv).reshape(B, S, 3, H, Dh), 2, 0, 3, 1, 4)
        q, k, v = qkv[0], qkv[1], qkv[2]
        scale = Dh ** -0.5
        pos = torch.arange(S)
        off = torch.arange(W) * dil
        idx = pos[:, None] - off[None, :]
        mask = idx >= 0
        idx_c = torch.where(mask, idx, 0).reshape(-1)
        kg = k[:, :, idx_c, :].reshape(B, H, S, W, Dh)
        vg = v[:, :, idx_c, :].reshape(B, H, S, W, Dh)
        logits = torch.einsum("bhsd,bhswd->bhsw", q, kg) * scale
        logits = torch.where(mask[None, None, :, :], logits, -1e9)
        attn = _softmax(logits, axis=-1)
        o = torch.einsum("bhsw,bhswd->bhsd", attn, vg)
        return (lambda x, *d: x.permute(*d))(o, 0, 2, 1, 3).reshape(B, S, D) @ wo

    a = _A_ref(A_x, A_wqkv, A_wo)
    b = _B_ref(B_x, B_wqkv, B_wo)
    return torch.concatenate([a, b], axis=-1)
```

```cpp
#include <hip/hip_runtime.h>
#include <hip/hip_bf16.h>
#define DI __device__ __forceinline__
typedef unsigned u32x4 __attribute__((ext_vector_type(4)));
typedef unsigned u32x2 __attribute__((ext_vector_type(2)));
typedef unsigned u32x16 __attribute__((ext_vector_type(16)));
typedef __attribute__((address_space(1))) unsigned gu32;
constexpr unsigned WS_Q = 0u, WS_F = 0x400000u, WS_M = 0x480000u, WS_KV = 0x800000u, WS_O = 0x1800000u, WS_END = 0x2000000u;
constexpr unsigned F_KV = 0x10000u, F_Q = 0x20000u, F_OB = 0x4000u, F_OA = 0x30000u;
constexpr unsigned FS1 = 0u, FS2 = 1u;
DI unsigned spread(unsigned i, unsigned sh) { return ((i >> sh) << 7) + ((i & ((1u << sh) - 1u)) << 2); }
constexpr int LDS_BYTES = 160 * 1024;

DI u32x4 make_srd(const void* p, unsigned bytes) {
  const unsigned long long a = (unsigned long long)p;
  return (u32x4){(unsigned)a, (unsigned)(a >> 32) & 0xffffu, bytes, 0x20000u};
}

__global__ __launch_bounds__(256) void fwd_kernel(const char* __restrict__ xa, const char* __restrict__ wa, const char* __restrict__ woa,
                                                  const char* __restrict__ xb, const char* __restrict__ wb, const char* __restrict__ wob,
                                                  char* __restrict__ out, char* __restrict__ ws) {
  __shared__ __attribute__((aligned(1024))) char lds[LDS_BYTES];
  const unsigned l = threadIdx.x & 63u, wid = __builtin_amdgcn_readfirstlane(threadIdx.x >> 6);
  const unsigned xcd = blockIdx.x & 7u, idx = blockIdx.x >> 3, role = idx >> 4, jx = idx & 15u;
  const unsigned mb = 4u * (xcd & 1u) + (jx & 3u), hp = 4u * (xcd >> 1) + (jx >> 2), cu = xcd * 16u + jx;
  const unsigned cpo = (idx * 4u + wid) * 1024u + l * 16u;
  asm volatile(
      ".Lfwd_begin:\n"
      "s_getpc_b64 s[62:63]\n\t"
      "v_min_u32 v54, .Lfwd_end-.Lfwd_begin-16, %[cpo]\n\t"
      "s_mov_b32 m0, 153600\n\t"
      "s_nop 0\n\t"
      "global_load_lds_dwordx4 v54, s[62:63]\n\t"
    :
    : [cpo] "v"(cpo)
    : "v54", "s62", "s63", "scc", "vcc", "memory");
  __builtin_amdgcn_sched_barrier(0);
  const unsigned g = l >> 4, s = l & 15u, q4 = s >> 2;
  const unsigned wr = wid >> 1, wc = wid & 1u;
  const char* px = role ? xb : xa;
  const char* pw = role ? wb : wa;
  const unsigned xrow = (256u * mb + 64u * wid + (l >> 3)) * 2048u;
  const unsigned vx0 = xrow + (((l & 7u) ^ g) << 4);
  const unsigned wrow = (16u * wid + (l >> 3)) * 6144u + hp * 128u;
  const unsigned f0 = g & 1u;
  const unsigned vw0 = wrow + ((((((l & 7u) >> 1) ^ f0) << 1) | (l & 1u)) << 4);
  const unsigned hrow = (mb ? 256u * mb - 16u : 0u) + 8u * wc + (l >> 3);
  const unsigned vxh = hrow * 2048u + (((l & 7u) ^ (4u * wc + g)) << 4);
  const unsigned hd = 147456u + wc * 1024u;
  asm volatile(
      "s_mov_b64 s[40:41], %[px]\n\t"
      "s_mov_b32 s42, 0x400000\n\t"
      "s_mov_b32 s43, 0x20000\n\t"
      "s_mov_b64 s[44:45], %[pw]\n\t"
      "s_mov_b32 s46, 0x600000\n\t"
      "s_mov_b32 s47, 0x20000\n\t"
      "s_lshl_b32 s51, %[wid], 11\n\t"
      "s_add_u32 s51, s51, 98304\n\t"
      "s_mov_b32 s53, 0\n\t"
      "s_lshl_b32 s49, %[wid], 13\n\t"
      "s_mov_b32 s52, 0\n\t"
      "s_mov_b32 s56, %[hd]\n\t"
      "v_mov_b32 v32, %[vx0]\n\t"
      "v_xor_b32 v33, 64, %[vx0]\n\t"
      "v_add_u32 v33, 16384, v33\n\t"
      "v_mov_b32 v40, %[vxh]\n\t"
      "s_add_u32 m0, s49, 0\n\t"
      "s_nop 0\n\t"
      "buffer_load_dwordx4 v32, s[40:43], s52 offen lds\n\t"
      "s_add_u32 m0, s49, 1024\n\t"
      "s_nop 0\n\t"
      "buffer_load_dwordx4 v33, s[40:43], s52 offen lds\n\t"
      "v_add_u32 v34, 32768, v32\n\t"
      "s_add_u32 m0, s49, 2048\n\t"
      "s_nop 0\n\t"
      "buffer_load_dwordx4 v34, s[40:43], s52 offen lds\n\t"
      "v_add_u32 v35, 32768, v33\n\t"
      "s_add_u32 m0, s49, 3072\n\t"
      "s_nop 0\n\t"
      "buffer_load_dwordx4 v35, s[40:43], s52 offen lds\n\t"
      "v_add_u32 v36, 65536, v32\n\t"
      "s_add_u32 m0, s49, 4096\n\t"
      "s_nop 0\n\t"
      "buffer_load_dwordx4 v36, s[40:43], s52 offen lds\n\t"
      "v_add_u32 v37, 65536, v33\n\t"
      "s_add_u32 m0, s49, 5120\n\t"
      "s_nop 0\n\t"
      "buffer_load_dwordx4 v37, s[40:43], s52 offen lds\n\t"
      "v_add_u32 v38, 98304, v32\n\t"
      "s_add_u32 m0, s49, 6144\n\t"
      "s_nop 0\n\t"
      "buffer_load_dwordx4 v38, s[40:43], s52 offen lds\n\t"
      "v_add_u32 v39, 98304, v33\n\t"
      "s_add_u32 m0, s49, 7168\n\t"
      "s_nop 0\n\t"
      "buffer_load_dwordx4 v39, s[40:43], s52 offen lds\n\t"
      "s_mov_b32 m0, s56\n\t"
      "s_nop 0\n\t"
      "buffer_load_dwordx4 v40, s[40:43], s52 offen lds\n\t"
      "v_mov_b32 v41, %[vw0]\n\t"
      "v_xor_b32 v42, 64, %[vw0]\n\t"
      "v_add_u32 v42, 49152, v42\n\t"
      "s_add_u32 m0, s51, 0\n\t"
      "s_nop 0\n\t"
      "buffer_load_dwordx4 v41, s[44:47], s53 offen lds\n\t"
      "s_add_u32 m0, s51, 1024\n\t"
      "s_nop 0\n\t"
      "buffer_load_dwordx4 v42, s[44:47], s53 offen lds\n\t"
      "v_add_u32 v43, 2048, v41\n\t"
      "s_add_u32 m0, s51, 8192\n\t"
      "s_nop 0\n\t"
      "buffer_load_dwordx4 v43, s[44:47], s53 offen lds\n\t"
      "v_add_u32 v44, 2048, v42\n\t"
      "s_add_u32 m0, s51, 9216\n\t"
      "s_nop 0\n\t"
      "buffer_load_dwordx4 v44, s[44:47], s53 offen lds\n\t"
      "v_add_u32 v45, 4096, v41\n\t"
      "s_add_u32 m0, s51, 16384\n\t"
      "s_nop 0\n\t"
      "buffer_load_dwordx4 v45, s[44:47], s53 offen lds\n\t"
      "v_add_u32 v46, 4096, v42\n\t"
      "s_add_u32 m0, s51, 17408\n\t"
      "s_nop 0\n\t"
      "buffer_load_dwordx4 v46, s[44:47], s53 offen lds\n\t"
    :
    : [px] "s"(px), [pw] "s"(pw), [wid] "s"(wid), [vx0] "v"(vx0), [vw0] "v"(vw0), [vxh] "v"(vxh), [hd] "s"(hd),
      "s"(woa), "s"(wob), "s"(out), "s"(ws)
    : "v32", "v33", "v34", "v35", "v36", "v37", "v38", "v39", "v40", "v41", "v42", "v43", "v44", "v45", "v46", "s40", "s41", "s42", "s43", "s44", "s45", "s46", "s47", "s48", "s49", "s50", "s51", "s52", "s53", "s54", "s55", "s56", "s57", "scc", "vcc", "memory");
  __builtin_amdgcn_sched_barrier(0);
  const unsigned base = (unsigned)(size_t)lds;
  const char* pf = ws + WS_F;
  const u32x4 sws = make_srd(ws, WS_END);
  unsigned tag;
  u32x16 qfr; u32x2 ref;
  const unsigned ahead = 2u * hp + role, q = l & 31u, r8 = l >> 3;
  const unsigned mt = 8u * (xcd & 1u) + (xcd & 6u) + (idx & 1u), nh = idx >> 1;
  const char* pkv = ws + WS_KV + ahead * 262144u;
  const unsigned vos = WS_O + (16u * mb + 4u * wid + (q >> 4)) * 32768u + ahead * 1024u + (l >> 5) * 256u + s * 16u;
  const unsigned wv1k = (cu * 4u + wid) * 1024u;
  const unsigned krot = mb * 32768u;
  const unsigned vqo = WS_Q + ahead * 131072u + (8u * mb + 2u * wid) * 2048u + l * 16u;
  const unsigned vmo = WS_M + ahead * 8192u + (256u * mb + 64u * wid + q) * 4u;
  const unsigned vg = wid * 4096u + l * 16u;
  const unsigned vw = (32u * wid + r8) * 2048u + nh * 128u + ((((((l & 7u) >> 1) ^ (r8 >> 1)) << 1) | (l & 1u)) << 4);
  const unsigned fq = F_Q + ((2u * hp + wc) * 8u + mb) * 128u + wr * 4u, fo = F_OB + (2u * mb + wr) * 256u + (2u * hp + wc) * 4u;
  const unsigned vf1 = fq + role * (fo - fq);

  unsigned head = ahead, rb = base + l * 16u;
  unsigned one0 = (l & 7u) == 0u ? 0x3f803f80u : 0u, one1 = (l & 7u) == 4u ? 0x3f803f80u : 0u;
  unsigned vf2 = F_OA + mb * 4096u + spread(head, FS2);
  unsigned vp2 = F_OA + (mt >> 1) * 4096u + spread(q, FS2);
  unsigned wn = wid & 1u, wk = wid >> 1;
  unsigned kr = 4u * g + q4;
  unsigned wrd = base + 65536u + (64u * wk + kr) * 128u + (((2u * wn) ^ ((kr >> 1) & 3u)) << 5) + (l & 3u) * 8u;
  unsigned vo = (8u * mt + 4u * (wk ^ 1u) + 2u * wn) * 32768u + wk * 2048u + l * 16u;
  unsigned ord = base + wk * 16384u + l * 16u;
  unsigned vout = (128u * mt + 64u * wk + s) * 4096u + (64u * nh + 32u * wn + (g & 1u) * 16u + (g >> 1) * 8u) * 2u;
  unsigned vp3 = F_KV + head * 2048u + spread(s, FS1);
  unsigned vpb = F_OB + mt * 256u + q * 4u;
  unsigned vos2 = vos;

  {
    const unsigned swz = s * 128u + ((g ^ (s >> 1)) << 4);
    const unsigned xr = base + 128u * wr * 128u + swz;
    const unsigned fw = (q4 >> 1) | ((g & 1u) << 1);
    const unsigned wrd = base + (8u * g + q4) * 128u + (((2u * wc) ^ fw) << 5) + (l & 3u) * 8u;
    const unsigned head = 2u * hp + wc;
    if (role == 0u) {
      const unsigned blk = 8u * mb + 4u * wr, th = (g >> 1) * 1024u + (g & 1u) * 512u;
      const unsigned vq = WS_Q + head * 131072u + blk * 2048u + th + s * 16u;
      const unsigned kvb = WS_KV + head * 262144u + blk * 4096u + th;
      const unsigned vk = kvb + (((s >> 2) << 3) | (s & 3u)) * 16u, vv = kvb + s * 16u;
      const unsigned vm = WS_M + head * 8192u + (32u * blk + l) * 4u;
      const unsigned vfk = F_KV + head * 2048u + spread(2u * mb + wr, FS1);
      asm volatile(
      "s_mov_b64 s[40:41], %[px]\n\t"
      "s_mov_b32 s42, 0x400000\n\t"
      "s_mov_b32 s43, 0x20000\n\t"
      "s_mov_b64 s[44:45], %[pw]\n\t"
      "s_mov_b32 s46, 0x600000\n\t"
      "s_mov_b32 s47, 0x20000\n\t"
      "v_mov_b32 v41, %[vw0]\n\t"
      "v_xor_b32 v42, 64, %[vw0]\n\t"
      "v_add_u32 v42, 49152, v42\n\t"
      "v_add_u32 v43, 2048, v41\n\t"
      "v_add_u32 v44, 2048, v42\n\t"
      "v_add_u32 v45, 4096, v41\n\t"
      "v_add_u32 v46, 4096, v42\n\t"
      "v_mov_b32 v32, %[vx0]\n\t"
      "v_xor_b32 v33, 64, %[vx0]\n\t"
      "v_add_u32 v33, 16384, v33\n\t"
      "v_add_u32 v34, 32768, v32\n\t"
      "v_add_u32 v35, 32768, v33\n\t"
      "v_add_u32 v36, 65536, v32\n\t"
      "v_add_u32 v37, 65536, v33\n\t"
      "v_add_u32 v38, 98304, v32\n\t"
      "v_add_u32 v39, 98304, v33\n\t"
      "v_mov_b32 v53, 0\n\t"
      "global_load_dword v53, v53, %[pf] sc1\n\t"
      "s_getpc_b64 s[62:63]\n\t"
      ".Lpc0_%=:\n"
      "s_sub_u32 s62, s62, .Lpc0_%=-.Lfwd_begin\n\t"
      "s_subb_u32 s63, s63, 0\n\t"
      "v_min_u32 v54, .Lfwd_end-.Lfwd_begin-16, %[cpo]\n\t"
      "s_mov_b64 s[96:97], %[pw]\n\t"
      "s_mov_b32 s98, 0x600000\n\t"
      "s_mov_b32 s99, 0x20000\n\t"
      "v_and_b32 v55, 1023, %[vg]\n\t"
      "v_add_u32 v62, %[wv1k], v55\n\t"
      "v_add_u32 v55, 786432, v62\n\t"
      "s_lshl_b32 s51, %[wid], 11\n\t"
      "s_add_u32 s51, s51, 122880\n\t"
      "s_mov_b32 s53, 393216\n\t"
      "s_add_u32 m0, s51, 0\n\t"
      "s_nop 0\n\t"
      "buffer_load_dwordx4 v41, s[44:47], s53 offen lds\n\t"
      "s_add_u32 m0, s51, 1024\n\t"
      "s_nop 0\n\t"
      "buffer_load_dwordx4 v42, s[44:47], s53 offen lds\n\t"
      "s_add_u32 m0, s51, 8192\n\t"
      "s_nop 0\n\t"
      "buffer_load_dwordx4 v43, s[44:47], s53 offen lds\n\t"
      "s_add_u32 m0, s51, 9216\n\t"
      "s_nop 0\n\t"
      "buffer_load_dwordx4 v44, s[44:47], s53 offen lds\n\t"
      "s_add_u32 m0, s51, 16384\n\t"
      "s_nop 0\n\t"
      "buffer_load_dwordx4 v45, s[44:47], s53 offen lds\n\t"
      "s_add_u32 m0, s51, 17408\n\t"
      "s_nop 0\n\t"
      "buffer_load_dwordx4 v46, s[44:47], s53 offen lds\n\t"
      "s_lshl_b32 s49, %[wid], 13\n\t"
      "s_add_u32 s49, s49, 32768\n\t"
      "s_mov_b32 s52, 128\n\t"
      "s_add_u32 m0, s49, 0\n\t"
      "s_nop 0\n\t"
      "buffer_load_dwordx4 v32, s[40:43], s52 offen lds\n\t"
      "s_add_u32 m0, s49, 1024\n\t"
      "s_nop 0\n\t"
      "buffer_load_dwordx4 v33, s[40:43], s52 offen lds\n\t"
      "s_add_u32 m0, s49, 2048\n\t"
      "s_nop 0\n\t"
      "buffer_load_dwordx4 v34, s[40:43], s52 offen lds\n\t"
      "s_add_u32 m0, s49, 3072\n\t"
      "s_nop 0\n\t"
      "buffer_load_dwordx4 v35, s[40:43], s52 offen lds\n\t"
      "s_add_u32 m0, s49, 4096\n\t"
      "s_nop 0\n\t"
      "buffer_load_dwordx4 v36, s[40:43], s52 offen lds\n\t"
      "s_add_u32 m0, s49, 5120\n\t"
      "s_nop 0\n\t"
      "buffer_load_dwordx4 v37, s[40:43], s52 offen lds\n\t"
      "s_add_u32 m0, s49, 6144\n\t"
      "s_nop 0\n\t"
      "buffer_load_dwordx4 v38, s[40:43], s52 offen lds\n\t"
      "s_add_u32 m0, s49, 7168\n\t"
      "s_nop 0\n\t"
      "buffer_load_dwordx4 v39, s[40:43], s52 offen lds\n\t"
      "s_lshl_b32 s49, %[wid], 13\n\t"
      "s_add_u32 s49, s49, 65536\n\t"
      "s_mov_b32 s52, 256\n\t"
      "s_add_u32 m0, s49, 0\n\t"
      "s_nop 0\n\t"
      "buffer_load_dwordx4 v32, s[40:43], s52 offen lds\n\t"
      "s_add_u32 m0, s49, 1024\n\t"
      "s_nop 0\n\t"
      "buffer_load_dwordx4 v33, s[40:43], s52 offen lds\n\t"
      "s_add_u32 m0, s49, 2048\n\t"
      "s_nop 0\n\t"
      "buffer_load_dwordx4 v34, s[40:43], s52 offen lds\n\t"
      "s_add_u32 m0, s49, 3072\n\t"
      "s_nop 0\n\t"
      "buffer_load_dwordx4 v35, s[40:43], s52 offen lds\n\t"
      "s_mov_b32 m0, 153600\n\t"
      "s_nop 0\n\t"
      "buffer_load_dwordx4 v55, s[96:99], 0 offen lds\n\t"
      "v_add_u32 v55, 0x80000, v55\n\t"
      "s_mov_b32 m0, 153600\n\t"
      "s_nop 0\n\t"
      "buffer_load_dwordx4 v55, s[96:99], 0 offen lds\n\t"
      "v_add_u32 v55, 0x80000, v55\n\t"
      "v_mov_b32 v64, 0\n\t"
      "v_mov_b32 v65, 0\n\t"
      "v_mov_b32 v66, 0\n\t"
      "v_mov_b32 v67, 0\n\t"
      "s_nop 1\n\t"
      "v_mfma_f32_32x32x16_bf16 v[240:255], v[64:67], v[64:67], 0\n\t"
      "v_mfma_f32_32x32x16_bf16 v[224:239], v[64:67], v[64:67], 0\n\t"
      "v_mfma_f32_32x32x16_bf16 v[208:223], v[64:67], v[64:67], 0\n\t"
      "v_mfma_f32_32x32x16_bf16 v[192:207], v[64:67], v[64:67], 0\n\t"
      "v_mfma_f32_32x32x16_bf16 v[176:191], v[64:67], v[64:67], 0\n\t"
      "v_mfma_f32_32x32x16_bf16 v[160:175], v[64:67], v[64:67], 0\n\t"
      "v_mfma_f32_32x32x16_bf16 v[144:159], v[64:67], v[64:67], 0\n\t"
      "v_mfma_f32_32x32x16_bf16 v[128:143], v[64:67], v[64:67], 0\n\t"
      "v_mfma_f32_32x32x16_bf16 v[112:127], v[64:67], v[64:67], 0\n\t"
      "v_mfma_f32_32x32x16_bf16 v[96:111], v[64:67], v[64:67], 0\n\t"
      "v_mfma_f32_32x32x16_bf16 v[80:95], v[64:67], v[64:67], 0\n\t"
      "v_mfma_f32_32x32x16_bf16 v[64:79], v[64:67], v[64:67], 0\n\t"
      "s_mov_b32 s48, 0\n\t"
      "s_mov_b32 s50, 98304\n\t"
      "s_mov_b32 s54, 0\n\t"
      "v_mov_b32 v47, %[xr]\n\t"
      "v_xor_b32 v48, 64, %[xr]\n\t"
      "v_add_u32 v49, 98304, %[wr]\n\t"
      "v_xor_b32 v50, 32, v49\n\t"
      "s_waitcnt vmcnt(21)\n\t"
      "s_barrier\n\t"
      "ds_read_b64_tr_b16 a[32:33], v49 offset:0\n\t"
      "ds_read_b64_tr_b16 a[34:35], v49 offset:512\n\t"
      "ds_read_b64_tr_b16 a[36:37], v50 offset:0\n\t"
      "ds_read_b64_tr_b16 a[38:39], v50 offset:512\n\t"
      "ds_read_b64_tr_b16 a[40:41], v49 offset:8192\n\t"
      "ds_read_b64_tr_b16 a[42:43], v49 offset:8704\n\t"
      "ds_read_b64_tr_b16 a[44:45], v50 offset:8192\n\t"
      "ds_read_b64_tr_b16 a[46:47], v50 offset:8704\n\t"
      "ds_read_b64_tr_b16 a[48:49], v49 offset:16384\n\t"
      "ds_read_b64_tr_b16 a[50:51], v49 offset:16896\n\t"
      "ds_read_b64_tr_b16 a[52:53], v50 offset:16384\n\t"
      "ds_read_b64_tr_b16 a[54:55], v50 offset:16896\n\t"
      "ds_read_b128 a[0:3], v47 offset:0\n\t"
      "ds_read_b128 a[4:7], v47 offset:2048\n\t"
      "ds_read_b128 a[8:11], v47 offset:4096\n\t"
      "ds_read_b128 a[12:15], v47 offset:6144\n\t"
      "ds_read_b128 a[16:19], v47 offset:8192\n\t"
      "ds_read_b128 a[20:23], v47 offset:10240\n\t"
      "ds_read_b128 a[24:27], v47 offset:12288\n\t"
      "ds_read_b128 a[28:31], v47 offset:14336\n\t"
      "s_waitcnt lgkmcnt(0)\n\t"
      "G1LOOP0_%=:\n"
      "v_mfma_f32_16x16x32_bf16 v[64:67], a[32:35], a[0:3], v[64:67]\n\t"
      "ds_read_b64_tr_b16 a[88:89], v49 offset:4096\n\t"
      "v_mfma_f32_16x16x32_bf16 v[68:71], a[36:39], a[0:3], v[68:71]\n\t"
      "ds_read_b64_tr_b16 a[90:91], v49 offset:4608\n\t"
      "v_mfma_f32_16x16x32_bf16 v[72:75], a[40:43], a[0:3], v[72:75]\n\t"
      "ds_read_b64_tr_b16 a[92:93], v50 offset:4096\n\t"
      "v_mfma_f32_16x16x32_bf16 v[76:79], a[44:47], a[0:3], v[76:79]\n\t"
      "ds_read_b64_tr_b16 a[94:95], v50 offset:4608\n\t"
      "v_mfma_f32_16x16x32_bf16 v[80:83], a[0:3], a[48:51], v[80:83]\n\t"
      "ds_read_b64_tr_b16 a[96:97], v49 offset:12288\n\t"
      "v_mfma_f32_16x16x32_bf16 v[84:87], a[0:3], a[52:55], v[84:87]\n\t"
      "ds_read_b64_tr_b16 a[98:99], v49 offset:12800\n\t"
      "v_mfma_f32_16x16x32_bf16 v[88:91], a[32:35], a[4:7], v[88:91]\n\t"
      "ds_read_b64_tr_b16 a[100:101], v50 offset:12288\n\t"
      "v_mfma_f32_16x16x32_bf16 v[92:95], a[36:39], a[4:7], v[92:95]\n\t"
      "ds_read_b64_tr_b16 a[102:103], v50 offset:12800\n\t"
      "v_mfma_f32_16x16x32_bf16 v[96:99], a[40:43], a[4:7], v[96:99]\n\t"
      "ds_read_b64_tr_b16 a[104:105], v49 offset:20480\n\t"
      "v_mfma_f32_16x16x32_bf16 v[100:103], a[44:47], a[4:7], v[100:103]\n\t"
      "ds_read_b64_tr_b16 a[106:107], v49 offset:20992\n\t"
      "v_mfma_f32_16x16x32_bf16 v[104:107], a[4:7], a[48:51], v[104:107]\n\t"
      "ds_read_b64_tr_b16 a[108:109], v50 offset:20480\n\t"
      "v_mfma_f32_16x16x32_bf16 v[108:111], a[4:7], a[52:55], v[108:111]\n\t"
      "ds_read_b64_tr_b16 a[110:111], v50 offset:20992\n\t"
      "v_mfma_f32_16x16x32_bf16 v[112:115], a[32:35], a[8:11], v[112:115]\n\t"
      "ds_read_b128 a[56:59], v48 offset:0\n\t"
      "v_mfma_f32_16x16x32_bf16 v[116:119], a[36:39], a[8:11], v[116:119]\n\t"
      "ds_read_b128 a[60:63], v48 offset:2048\n\t"
      "v_mfma_f32_16x16x32_bf16 v[120:123], a[40:43], a[8:11], v[120:123]\n\t"
      "ds_read_b128 a[64:67], v48 offset:4096\n\t"
      "v_mfma_f32_16x16x32_bf16 v[124:127], a[44:47], a[8:11], v[124:127]\n\t"
      "ds_read_b128 a[68:71], v48 offset:6144\n\t"
      "v_mfma_f32_16x16x32_bf16 v[128:131], a[8:11], a[48:51], v[128:131]\n\t"
      "ds_read_b128 a[72:75], v48 offset:8192\n\t"
      "v_mfma_f32_16x16x32_bf16 v[132:135], a[8:11], a[52:55], v[132:135]\n\t"
      "ds_read_b128 a[76:79], v48 offset:10240\n\t"
      "v_mfma_f32_16x16x32_bf16 v[136:139], a[32:35], a[12:15], v[136:139]\n\t"
      "ds_read_b128 a[80:83], v48 offset:12288\n\t"
      "v_mfma_f32_16x16x32_bf16 v[140:143], a[36:39], a[12:15], v[140:143]\n\t"
      "ds_read_b128 a[84:87], v48 offset:14336\n\t"
      "v_mfma_f32_16x16x32_bf16 v[144:147], a[40:43], a[12:15], v[144:147]\n\t"
      "v_mfma_f32_16x16x32_bf16 v[148:151], a[44:47], a[12:15], v[148:151]\n\t"
      "s_add_u32 m0, s49, 4096\n\t"
      "v_mfma_f32_16x16x32_bf16 v[152:155], a[12:15], a[48:51], v[152:155]\n\t"
      "buffer_load_dwordx4 v36, s[40:43], s52 offen lds\n\t"
      "v_mfma_f32_16x16x32_bf16 v[156:159], a[12:15], a[52:55], v[156:159]\n\t"
      "s_add_u32 m0, s49, 5120\n\t"
      "v_mfma_f32_16x16x32_bf16 v[160:163], a[32:35], a[16:19], v[160:163]\n\t"
      "buffer_load_dwordx4 v37, s[40:43], s52 offen lds\n\t"
      "v_mfma_f32_16x16x32_bf16 v[164:167], a[36:39], a[16:19], v[164:167]\n\t"
      "s_add_u32 m0, s49, 6144\n\t"
      "v_mfma_f32_16x16x32_bf16 v[168:171], a[40:43], a[16:19], v[168:171]\n\t"
      "buffer_load_dwordx4 v38, s[40:43], s52 offen lds\n\t"
      "v_mfma_f32_16x16x32_bf16 v[172:175], a[44:47], a[16:19], v[172:175]\n\t"
      "s_add_u32 m0, s49, 7168\n\t"
      "v_mfma_f32_16x16x32_bf16 v[176:179], a[16:19], a[48:51], v[176:179]\n\t"
      "buffer_load_dwordx4 v39, s[40:43], s52 offen lds\n\t"
      "v_mfma_f32_16x16x32_bf16 v[180:183], a[16:19], a[52:55], v[180:183]\n\t"
      "s_lshl_b32 s55, %[wid], 13\n\t"
      "v_mfma_f32_16x16x32_bf16 v[184:187], a[32:35], a[20:23], v[184:187]\n\t"
      "s_add_u32 s49, s48, s55\n\t"
      "v_mfma_f32_16x16x32_bf16 v[188:191], a[36:39], a[20:23], v[188:191]\n\t"
      "s_add_u32 s48, s48, 32768\n\t"
      "v_mfma_f32_16x16x32_bf16 v[192:195], a[40:43], a[20:23], v[192:195]\n\t"
      "s_cmp_eq_u32 s48, 98304\n\t"
      "s_cselect_b32 s48, 0, s48\n\t"
      "v_mfma_f32_16x16x32_bf16 v[196:199], a[44:47], a[20:23], v[196:199]\n\t"
      "s_lshl_b32 s55, %[wid], 11\n\t"
      "s_add_u32 s51, s50, s55\n\t"
      "v_mfma_f32_16x16x32_bf16 v[200:203], a[20:23], a[48:51], v[200:203]\n\t"
      "s_xor_b32 s50, s50, 24576\n\t"
      "v_mfma_f32_16x16x32_bf16 v[204:207], a[20:23], a[52:55], v[204:207]\n\t"
      "v_add_u32 v47, s48, %[xr]\n\t"
      "v_xor_b32 v48, 64, v47\n\t"
      "v_mfma_f32_16x16x32_bf16 v[208:211], a[32:35], a[24:27], v[208:211]\n\t"
      "v_add_u32 v49, s50, %[wr]\n\t"
      "v_xor_b32 v50, 32, v49\n\t"
      "v_mfma_f32_16x16x32_bf16 v[212:215], a[36:39], a[24:27], v[212:215]\n\t"
      "s_add_u32 s52, s52, 128\n\t"
      "s_add_u32 s53, s53, 393216\n\t"
      "v_mfma_f32_16x16x32_bf16 v[216:219], a[40:43], a[24:27], v[216:219]\n\t"
      "s_cmp_lt_u32 s54, 13\n\t"
      "s_cselect_b32 s42, 0x400000, 0\n\t"
      "v_mfma_f32_16x16x32_bf16 v[220:223], a[44:47], a[24:27], v[220:223]\n\t"
      "s_cmp_lt_u32 s54, 14\n\t"
      "s_cselect_b32 s46, 0x600000, 0\n\t"
      "v_mfma_f32_16x16x32_bf16 v[224:227], a[24:27], a[48:51], v[224:227]\n\t"
      "s_add_u32 s54, s54, 1\n\t"
      "v_mfma_f32_16x16x32_bf16 v[228:231], a[24:27], a[52:55], v[228:231]\n\t"
      "v_mfma_f32_16x16x32_bf16 v[232:235], a[32:35], a[28:31], v[232:235]\n\t"
      "v_mfma_f32_16x16x32_bf16 v[236:239], a[36:39], a[28:31], v[236:239]\n\t"
      "v_mfma_f32_16x16x32_bf16 v[240:243], a[40:43], a[28:31], v[240:243]\n\t"
      "v_mfma_f32_16x16x32_bf16 v[244:247], a[44:47], a[28:31], v[244:247]\n\t"
      "v_mfma_f32_16x16x32_bf16 v[248:251], a[28:31], a[48:51], v[248:251]\n\t"
      "v_mfma_f32_16x16x32_bf16 v[252:255], a[28:31], a[52:55], v[252:255]\n\t"
      "s_waitcnt vmcnt(10) lgkmcnt(0)\n\t"
      "s_barrier\n\t"
      "v_mfma_f32_16x16x32_bf16 v[64:67], a[88:91], a[56:59], v[64:67]\n\t"
      "s_add_u32 m0, s51, 0\n\t"
      "v_mfma_f32_16x16x32_bf16 v[68:71], a[92:95], a[56:59], v[68:71]\n\t"
      "buffer_load_dwordx4 v41, s[44:47], s53 offen lds\n\t"
      "v_mfma_f32_16x16x32_bf16 v[72:75], a[96:99], a[56:59], v[72:75]\n\t"
      "ds_read_b64_tr_b16 a[32:33], v49 offset:0\n\t"
      "v_mfma_f32_16x16x32_bf16 v[76:79], a[100:103], a[56:59], v[76:79]\n\t"
      "s_add_u32 m0, s51, 1024\n\t"
      "v_mfma_f32_16x16x32_bf16 v[80:83], a[56:59], a[104:107], v[80:83]\n\t"
      "buffer_load_dwordx4 v42, s[44:47], s53 offen lds\n\t"
      "v_mfma_f32_16x16x32_bf16 v[84:87], a[56:59], a[108:111], v[84:87]\n\t"
      "ds_read_b64_tr_b16 a[34:35], v49 offset:512\n\t"
      "v_mfma_f32_16x16x32_bf16 v[88:91], a[88:91], a[60:63], v[88:91]\n\t"
      "s_add_u32 m0, s51, 8192\n\t"
      "v_mfma_f32_16x16x32_bf16 v[92:95], a[92:95], a[60:63], v[92:95]\n\t"
      "buffer_load_dwordx4 v43, s[44:47], s53 offen lds\n\t"
      "v_mfma_f32_16x16x32_bf16 v[96:99], a[96:99], a[60:63], v[96:99]\n\t"
      "ds_read_b64_tr_b16 a[36:37], v50 offset:0\n\t"
      "v_mfma_f32_16x16x32_bf16 v[100:103], a[100:103], a[60:63], v[100:103]\n\t"
      "s_add_u32 m0, s51, 9216\n\t"
      "v_mfma_f32_16x16x32_bf16 v[104:107], a[60:63], a[104:107], v[104:107]\n\t"
      "buffer_load_dwordx4 v44, s[44:47], s53 offen lds\n\t"
      "v_mfma_f32_16x16x32_bf16 v[108:111], a[60:63], a[108:111], v[108:111]\n\t"
      "ds_read_b64_tr_b16 a[38:39], v50 offset:512\n\t"
      "v_mfma_f32_16x16x32_bf16 v[112:115], a[88:91], a[64:67], v[112:115]\n\t"
      "s_add_u32 m0, s51, 16384\n\t"
      "v_mfma_f32_16x16x32_bf16 v[116:119], a[92:95], a[64:67], v[116:119]\n\t"
      "buffer_load_dwordx4 v45, s[44:47], s53 offen lds\n\t"
      "v_mfma_f32_16x16x32_bf16 v[120:123], a[96:99], a[64:67], v[120:123]\n\t"
      "ds_read_b64_tr_b16 a[40:41], v49 offset:8192\n\t"
      "v_mfma_f32_16x16x32_bf16 v[124:127], a[100:103], a[64:67], v[124:127]\n\t"
      "s_add_u32 m0, s51, 17408\n\t"
      "v_mfma_f32_16x16x32_bf16 v[128:131], a[64:67], a[104:107], v[128:131]\n\t"
      "buffer_load_dwordx4 v46, s[44:47], s53 offen lds\n\t"
      "v_mfma_f32_16x16x32_bf16 v[132:135], a[64:67], a[108:111], v[132:135]\n\t"
      "ds_read_b64_tr_b16 a[42:43], v49 offset:8704\n\t"
      "v_mfma_f32_16x16x32_bf16 v[136:139], a[88:91], a[68:71], v[136:139]\n\t"
      "s_mov_b32 m0, 153600\n\t"
      "v_mfma_f32_16x16x32_bf16 v[140:143], a[92:95], a[68:71], v[140:143]\n\t"
      "buffer_load_dwordx4 v55, s[96:99], 0 offen lds\n\t"
      "v_add_u32 v55, 0x80000, v55\n\t"
      "v_mfma_f32_16x16x32_bf16 v[144:147], a[96:99], a[68:71], v[144:147]\n\t"
      "global_load_dwordx4 v[56:59], v54, s[62:63]\n\t"
      "v_mfma_f32_16x16x32_bf16 v[148:151], a[100:103], a[68:71], v[148:151]\n\t"
      "ds_read_b64_tr_b16 a[44:45], v50 offset:8192\n\t"
      "v_mfma_f32_16x16x32_bf16 v[152:155], a[68:71], a[104:107], v[152:155]\n\t"
      "ds_read_b64_tr_b16 a[46:47], v50 offset:8704\n\t"
      "v_mfma_f32_16x16x32_bf16 v[156:159], a[68:71], a[108:111], v[156:159]\n\t"
      "ds_read_b64_tr_b16 a[48:49], v49 offset:16384\n\t"
      "v_mfma_f32_16x16x32_bf16 v[160:163], a[88:91], a[72:75], v[160:163]\n\t"
      "ds_read_b64_tr_b16 a[50:51], v49 offset:16896\n\t"
      "v_mfma_f32_16x16x32_bf16 v[164:167], a[92:95], a[72:75], v[164:167]\n\t"
      "ds_read_b64_tr_b16 a[52:53], v50 offset:16384\n\t"
      "v_mfma_f32_16x16x32_bf16 v[168:171], a[96:99], a[72:75], v[168:171]\n\t"
      "ds_read_b64_tr_b16 a[54:55], v50 offset:16896\n\t"
      "v_mfma_f32_16x16x32_bf16 v[172:175], a[100:103], a[72:75], v[172:175]\n\t"
      "ds_read_b128 a[0:3], v47 offset:0\n\t"
      "v_mfma_f32_16x16x32_bf16 v[176:179], a[72:75], a[104:107], v[176:179]\n\t"
      "ds_read_b128 a[4:7], v47 offset:2048\n\t"
      "v_mfma_f32_16x16x32_bf16 v[180:183], a[72:75], a[108:111], v[180:183]\n\t"
      "ds_read_b128 a[8:11], v47 offset:4096\n\t"
      "v_mfma_f32_16x16x32_bf16 v[184:187], a[88:91], a[76:79], v[184:187]\n\t"
      "ds_read_b128 a[12:15], v47 offset:6144\n\t"
      "v_mfma_f32_16x16x32_bf16 v[188:191], a[92:95], a[76:79], v[188:191]\n\t"
      "ds_read_b128 a[16:19], v47 offset:8192\n\t"
      "v_mfma_f32_16x16x32_bf16 v[192:195], a[96:99], a[76:79], v[192:195]\n\t"
      "ds_read_b128 a[20:23], v47 offset:10240\n\t"
      "v_mfma_f32_16x16x32_bf16 v[196:199], a[100:103], a[76:79], v[196:199]\n\t"
      "ds_read_b128 a[24:27], v47 offset:12288\n\t"
      "v_mfma_f32_16x16x32_bf16 v[200:203], a[76:79], a[104:107], v[200:203]\n\t"
      "ds_read_b128 a[28:31], v47 offset:14336\n\t"
      "v_mfma_f32_16x16x32_bf16 v[204:207], a[76:79], a[108:111], v[204:207]\n\t"
      "s_add_u32 m0, s49, 0\n\t"
      "v_mfma_f32_16x16x32_bf16 v[208:211], a[88:91], a[80:83], v[208:211]\n\t"
      "buffer_load_dwordx4 v32, s[40:43], s52 offen lds\n\t"
      "v_mfma_f32_16x16x32_bf16 v[212:215], a[92:95], a[80:83], v[212:215]\n\t"
      "s_add_u32 m0, s49, 1024\n\t"
      "v_mfma_f32_16x16x32_bf16 v[216:219], a[96:99], a[80:83], v[216:219]\n\t"
      "buffer_load_dwordx4 v33, s[40:43], s52 offen lds\n\t"
      "v_mfma_f32_16x16x32_bf16 v[220:223], a[100:103], a[80:83], v[220:223]\n\t"
      "s_add_u32 m0, s49, 2048\n\t"
      "v_mfma_f32_16x16x32_bf16 v[224:227], a[80:83], a[104:107], v[224:227]\n\t"
      "buffer_load_dwordx4 v34, s[40:43], s52 offen lds\n\t"
      "v_mfma_f32_16x16x32_bf16 v[228:231], a[80:83], a[108:111], v[228:231]\n\t"
      "s_add_u32 m0, s49, 3072\n\t"
      "v_mfma_f32_16x16x32_bf16 v[232:235], a[88:91], a[84:87], v[232:235]\n\t"
      "buffer_load_dwordx4 v35, s[40:43], s52 offen lds\n\t"
      "v_mfma_f32_16x16x32_bf16 v[236:239], a[92:95], a[84:87], v[236:239]\n\t"
      "v_mfma_f32_16x16x32_bf16 v[240:243], a[96:99], a[84:87], v[240:243]\n\t"
      "v_mfma_f32_16x16x32_bf16 v[244:247], a[100:103], a[84:87], v[244:247]\n\t"
      "v_mfma_f32_16x16x32_bf16 v[248:251], a[84:87], a[104:107], v[248:251]\n\t"
      "s_cmp_lt_u32 s54, 14\n\t"
      "v_mfma_f32_16x16x32_bf16 v[252:255], a[84:87], a[108:111], v[252:255]\n\t"
      "s_waitcnt lgkmcnt(0)\n\t"
      "s_cbranch_scc1 G1LOOP0_%=\n\t"
      "v_add_u32 %[tag], 1, v53\n\t"
      "v_mov_b32 v51, %[xr]\n\t"
      "v_xor_b32 v52, 64, %[xr]\n\t"
      "v_add_u32 v41, 122880, %[wr]\n\t"
      "v_xor_b32 v42, 32, v41\n\t"
      "v_mfma_f32_16x16x32_bf16 v[72:75], a[40:43], a[0:3], v[72:75]\n\t"
      "ds_read_b64_tr_b16 a[96:97], v49 offset:12288\n\t"
      "v_mfma_f32_16x16x32_bf16 v[76:79], a[44:47], a[0:3], v[76:79]\n\t"
      "ds_read_b64_tr_b16 a[98:99], v49 offset:12800\n\t"
      "v_mfma_f32_16x16x32_bf16 v[80:83], a[0:3], a[48:51], v[80:83]\n\t"
      "ds_read_b64_tr_b16 a[100:101], v50 offset:12288\n\t"
      "v_mfma_f32_16x16x32_bf16 v[84:87], a[0:3], a[52:55], v[84:87]\n\t"
      "ds_read_b64_tr_b16 a[102:103], v50 offset:12800\n\t"
      "v_mfma_f32_16x16x32_bf16 v[96:99], a[40:43], a[4:7], v[96:99]\n\t"
      "ds_read_b64_tr_b16 a[104:105], v49 offset:20480\n\t"
      "v_mfma_f32_16x16x32_bf16 v[100:103], a[44:47], a[4:7], v[100:103]\n\t"
      "ds_read_b64_tr_b16 a[106:107], v49 offset:20992\n\t"
      "v_mfma_f32_16x16x32_bf16 v[104:107], a[4:7], a[48:51], v[104:107]\n\t"
      "ds_read_b64_tr_b16 a[108:109], v50 offset:20480\n\t"
      "v_mfma_f32_16x16x32_bf16 v[108:111], a[4:7], a[52:55], v[108:111]\n\t"
      "ds_read_b64_tr_b16 a[110:111], v50 offset:20992\n\t"
      "v_mfma_f32_16x16x32_bf16 v[120:123], a[40:43], a[8:11], v[120:123]\n\t"
      "ds_read_b128 a[56:59], v48 offset:0\n\t"
      "v_mfma_f32_16x16x32_bf16 v[124:127], a[44:47], a[8:11], v[124:127]\n\t"
      "ds_read_b128 a[60:63], v48 offset:2048\n\t"
      "v_mfma_f32_16x16x32_bf16 v[128:131], a[8:11], a[48:51], v[128:131]\n\t"
      "ds_read_b128 a[64:67], v48 offset:4096\n\t"
      "v_mfma_f32_16x16x32_bf16 v[132:135], a[8:11], a[52:55], v[132:135]\n\t"
      "ds_read_b128 a[68:71], v48 offset:6144\n\t"
      "v_mfma_f32_16x16x32_bf16 v[144:147], a[40:43], a[12:15], v[144:147]\n\t"
      "ds_read_b128 a[72:75], v48 offset:8192\n\t"
      "v_mfma_f32_16x16x32_bf16 v[148:151], a[44:47], a[12:15], v[148:151]\n\t"
      "ds_read_b128 a[76:79], v48 offset:10240\n\t"
      "v_mfma_f32_16x16x32_bf16 v[152:155], a[12:15], a[48:51], v[152:155]\n\t"
      "ds_read_b128 a[80:83], v48 offset:12288\n\t"
      "v_mfma_f32_16x16x32_bf16 v[156:159], a[12:15], a[52:55], v[156:159]\n\t"
      "ds_read_b128 a[84:87], v48 offset:14336\n\t"
      "v_mfma_f32_16x16x32_bf16 v[168:171], a[40:43], a[16:19], v[168:171]\n\t"
      "v_mfma_f32_16x16x32_bf16 v[172:175], a[44:47], a[16:19], v[172:175]\n\t"
      "v_mfma_f32_16x16x32_bf16 v[176:179], a[16:19], a[48:51], v[176:179]\n\t"
      "v_mfma_f32_16x16x32_bf16 v[180:183], a[16:19], a[52:55], v[180:183]\n\t"
      "v_mfma_f32_16x16x32_bf16 v[192:195], a[40:43], a[20:23], v[192:195]\n\t"
      "v_mfma_f32_16x16x32_bf16 v[196:199], a[44:47], a[20:23], v[196:199]\n\t"
      "v_mfma_f32_16x16x32_bf16 v[200:203], a[20:23], a[48:51], v[200:203]\n\t"
      "v_mfma_f32_16x16x32_bf16 v[204:207], a[20:23], a[52:55], v[204:207]\n\t"
      "v_mfma_f32_16x16x32_bf16 v[216:219], a[40:43], a[24:27], v[216:219]\n\t"
      "v_mfma_f32_16x16x32_bf16 v[220:223], a[44:47], a[24:27], v[220:223]\n\t"
      "v_mfma_f32_16x16x32_bf16 v[224:227], a[24:27], a[48:51], v[224:227]\n\t"
      "v_mfma_f32_16x16x32_bf16 v[228:231], a[24:27], a[52:55], v[228:231]\n\t"
      "v_mfma_f32_16x16x32_bf16 v[240:243], a[40:43], a[28:31], v[240:243]\n\t"
      "v_mfma_f32_16x16x32_bf16 v[244:247], a[44:47], a[28:31], v[244:247]\n\t"
      "v_mfma_f32_16x16x32_bf16 v[248:251], a[28:31], a[48:51], v[248:251]\n\t"
      "v_mfma_f32_16x16x32_bf16 v[252:255], a[28:31], a[52:55], v[252:255]\n\t"
      "s_waitcnt vmcnt(0) lgkmcnt(0)\n\t"
      "s_barrier\n\t"
      "v_mfma_f32_16x16x32_bf16 v[72:75], a[96:99], a[56:59], v[72:75]\n\t"
      "ds_read_b64_tr_b16 a[40:41], v41 offset:8192\n\t"
      "v_mfma_f32_16x16x32_bf16 v[76:79], a[100:103], a[56:59], v[76:79]\n\t"
      "ds_read_b64_tr_b16 a[42:43], v41 offset:8704\n\t"
      "v_mfma_f32_16x16x32_bf16 v[80:83], a[56:59], a[104:107], v[80:83]\n\t"
      "ds_read_b64_tr_b16 a[44:45], v42 offset:8192\n\t"
      "v_mfma_f32_16x16x32_bf16 v[84:87], a[56:59], a[108:111], v[84:87]\n\t"
      "ds_read_b64_tr_b16 a[46:47], v42 offset:8704\n\t"
      "v_mfma_f32_16x16x32_bf16 v[96:99], a[96:99], a[60:63], v[96:99]\n\t"
      "ds_read_b64_tr_b16 a[48:49], v41 offset:16384\n\t"
      "v_mfma_f32_16x16x32_bf16 v[100:103], a[100:103], a[60:63], v[100:103]\n\t"
      "ds_read_b64_tr_b16 a[50:51], v41 offset:16896\n\t"
      "v_mfma_f32_16x16x32_bf16 v[104:107], a[60:63], a[104:107], v[104:107]\n\t"
      "ds_read_b64_tr_b16 a[52:53], v42 offset:16384\n\t"
      "v_mfma_f32_16x16x32_bf16 v[108:111], a[60:63], a[108:111], v[108:111]\n\t"
      "ds_read_b64_tr_b16 a[54:55], v42 offset:16896\n\t"
      "v_mfma_f32_16x16x32_bf16 v[120:123], a[96:99], a[64:67], v[120:123]\n\t"
      "ds_read_b128 a[0:3], v51 offset:0\n\t"
      "v_mfma_f32_16x16x32_bf16 v[124:127], a[100:103], a[64:67], v[124:127]\n\t"
      "ds_read_b128 a[4:7], v51 offset:2048\n\t"
      "v_mfma_f32_16x16x32_bf16 v[128:131], a[64:67], a[104:107], v[128:131]\n\t"
      "ds_read_b128 a[8:11], v51 offset:4096\n\t"
      "v_mfma_f32_16x16x32_bf16 v[132:135], a[64:67], a[108:111], v[132:135]\n\t"
      "ds_read_b128 a[12:15], v51 offset:6144\n\t"
      "v_mfma_f32_16x16x32_bf16 v[144:147], a[96:99], a[68:71], v[144:147]\n\t"
      "ds_read_b128 a[16:19], v51 offset:8192\n\t"
      "v_mfma_f32_16x16x32_bf16 v[148:151], a[100:103], a[68:71], v[148:151]\n\t"
      "ds_read_b128 a[20:23], v51 offset:10240\n\t"
      "v_mfma_f32_16x16x32_bf16 v[152:155], a[68:71], a[104:107], v[152:155]\n\t"
      "ds_read_b128 a[24:27], v51 offset:12288\n\t"
      "v_mfma_f32_16x16x32_bf16 v[156:159], a[68:71], a[108:111], v[156:159]\n\t"
      "ds_read_b128 a[28:31], v51 offset:14336\n\t"
      "v_mfma_f32_16x16x32_bf16 v[168:171], a[96:99], a[72:75], v[168:171]\n\t"
      "v_mfma_f32_16x16x32_bf16 v[172:175], a[100:103], a[72:75], v[172:175]\n\t"
      "v_mfma_f32_16x16x32_bf16 v[176:179], a[72:75], a[104:107], v[176:179]\n\t"
      "v_mfma_f32_16x16x32_bf16 v[180:183], a[72:75], a[108:111], v[180:183]\n\t"
      "v_mfma_f32_16x16x32_bf16 v[192:195], a[96:99], a[76:79], v[192:195]\n\t"
      "v_mfma_f32_16x16x32_bf16 v[196:199], a[100:103], a[76:79], v[196:199]\n\t"
      "v_mfma_f32_16x16x32_bf16 v[200:203], a[76:79], a[104:107], v[200:203]\n\t"
      "v_mfma_f32_16x16x32_bf16 v[204:207], a[76:79], a[108:111], v[204:207]\n\t"
      "v_mfma_f32_16x16x32_bf16 v[216:219], a[96:99], a[80:83], v[216:219]\n\t"
      "v_mfma_f32_16x16x32_bf16 v[220:223], a[100:103], a[80:83], v[220:223]\n\t"
      "v_mfma_f32_16x16x32_bf16 v[224:227], a[80:83], a[104:107], v[224:227]\n\t"
      "v_mfma_f32_16x16x32_bf16 v[228:231], a[80:83], a[108:111], v[228:231]\n\t"
      "v_mfma_f32_16x16x32_bf16 v[240:243], a[96:99], a[84:87], v[240:243]\n\t"
      "v_mfma_f32_16x16x32_bf16 v[244:247], a[100:103], a[84:87], v[244:247]\n\t"
      "v_mfma_f32_16x16x32_bf16 v[248:251], a[84:87], a[104:107], v[248:251]\n\t"
      "v_mfma_f32_16x16x32_bf16 v[252:255], a[84:87], a[108:111], v[252:255]\n\t"
      "s_waitcnt lgkmcnt(0)\n\t"
      "v_mfma_f32_16x16x32_bf16 v[72:75], a[40:43], a[0:3], v[72:75]\n\t"
      "ds_read_b64_tr_b16 a[96:97], v41 offset:12288\n\t"
      "v_mfma_f32_16x16x32_bf16 v[76:79], a[44:47], a[0:3], v[76:79]\n\t"
      "ds_read_b64_tr_b16 a[98:99], v41 offset:12800\n\t"
      "v_mfma_f32_16x16x32_bf16 v[80:83], a[0:3], a[48:51], v[80:83]\n\t"
      "ds_read_b64_tr_b16 a[100:101], v42 offset:12288\n\t"
      "v_mfma_f32_16x16x32_bf16 v[84:87], a[0:3], a[52:55], v[84:87]\n\t"
      "ds_read_b64_tr_b16 a[102:103], v42 offset:12800\n\t"
      "v_mfma_f32_16x16x32_bf16 v[96:99], a[40:43], a[4:7], v[96:99]\n\t"
      "ds_read_b64_tr_b16 a[104:105], v41 offset:20480\n\t"
      "v_mfma_f32_16x16x32_bf16 v[100:103], a[44:47], a[4:7], v[100:103]\n\t"
      "ds_read_b64_tr_b16 a[106:107], v41 offset:20992\n\t"
      "v_mfma_f32_16x16x32_bf16 v[104:107], a[4:7], a[48:51], v[104:107]\n\t"
      "ds_read_b64_tr_b16 a[108:109], v42 offset:20480\n\t"
      "v_mfma_f32_16x16x32_bf16 v[108:111], a[4:7], a[52:55], v[108:111]\n\t"
      "ds_read_b64_tr_b16 a[110:111], v42 offset:20992\n\t"
      "v_mfma_f32_16x16x32_bf16 v[120:123], a[40:43], a[8:11], v[120:123]\n\t"
      "ds_read_b128 a[56:59], v52 offset:0\n\t"
      "v_mfma_f32_16x16x32_bf16 v[124:127], a[44:47], a[8:11], v[124:127]\n\t"
      "ds_read_b128 a[60:63], v52 offset:2048\n\t"
      "v_mfma_f32_16x16x32_bf16 v[128:131], a[8:11], a[48:51], v[128:131]\n\t"
      "ds_read_b128 a[64:67], v52 offset:4096\n\t"
      "v_mfma_f32_16x16x32_bf16 v[132:135], a[8:11], a[52:55], v[132:135]\n\t"
      "ds_read_b128 a[68:71], v52 offset:6144\n\t"
      "v_mfma_f32_16x16x32_bf16 v[144:147], a[40:43], a[12:15], v[144:147]\n\t"
      "ds_read_b128 a[72:75], v52 offset:8192\n\t"
      "v_mfma_f32_16x16x32_bf16 v[148:151], a[44:47], a[12:15], v[148:151]\n\t"
      "ds_read_b128 a[76:79], v52 offset:10240\n\t"
      "v_mfma_f32_16x16x32_bf16 v[152:155], a[12:15], a[48:51], v[152:155]\n\t"
      "ds_read_b128 a[80:83], v52 offset:12288\n\t"
      "v_mfma_f32_16x16x32_bf16 v[156:159], a[12:15], a[52:55], v[156:159]\n\t"
      "ds_read_b128 a[84:87], v52 offset:14336\n\t"
      "v_mfma_f32_16x16x32_bf16 v[168:171], a[40:43], a[16:19], v[168:171]\n\t"
      "v_mfma_f32_16x16x32_bf16 v[172:175], a[44:47], a[16:19], v[172:175]\n\t"
      "v_mfma_f32_16x16x32_bf16 v[176:179], a[16:19], a[48:51], v[176:179]\n\t"
      "v_mfma_f32_16x16x32_bf16 v[180:183], a[16:19], a[52:55], v[180:183]\n\t"
      "v_mfma_f32_16x16x32_bf16 v[192:195], a[40:43], a[20:23], v[192:195]\n\t"
      "v_mfma_f32_16x16x32_bf16 v[196:199], a[44:47], a[20:23], v[196:199]\n\t"
      "v_mfma_f32_16x16x32_bf16 v[200:203], a[20:23], a[48:51], v[200:203]\n\t"
      "v_mfma_f32_16x16x32_bf16 v[204:207], a[20:23], a[52:55], v[204:207]\n\t"
      "v_mfma_f32_16x16x32_bf16 v[216:219], a[40:43], a[24:27], v[216:219]\n\t"
      "v_mfma_f32_16x16x32_bf16 v[220:223], a[44:47], a[24:27], v[220:223]\n\t"
      "v_mfma_f32_16x16x32_bf16 v[224:227], a[24:27], a[48:51], v[224:227]\n\t"
      "v_mfma_f32_16x16x32_bf16 v[228:231], a[24:27], a[52:55], v[228:231]\n\t"
      "v_mfma_f32_16x16x32_bf16 v[240:243], a[40:43], a[28:31], v[240:243]\n\t"
      "v_mfma_f32_16x16x32_bf16 v[244:247], a[44:47], a[28:31], v[244:247]\n\t"
      "v_mfma_f32_16x16x32_bf16 v[248:251], a[28:31], a[48:51], v[248:251]\n\t"
      "v_mfma_f32_16x16x32_bf16 v[252:255], a[28:31], a[52:55], v[252:255]\n\t"
      "s_waitcnt lgkmcnt(0)\n\t"
      "v_mfma_f32_16x16x32_bf16 v[72:75], a[96:99], a[56:59], v[72:75]\n\t"
      "ds_read_b64_tr_b16 a[32:33], v49 offset:0\n\t"
      "v_mfma_f32_16x16x32_bf16 v[76:79], a[100:103], a[56:59], v[76:79]\n\t"
      "ds_read_b64_tr_b16 a[34:35], v49 offset:512\n\t"
      "v_mfma_f32_16x16x32_bf16 v[80:83], a[56:59], a[104:107], v[80:83]\n\t"
      "ds_read_b64_tr_b16 a[36:37], v50 offset:0\n\t"
      "v_mfma_f32_16x16x32_bf16 v[84:87], a[56:59], a[108:111], v[84:87]\n\t"
      "ds_read_b64_tr_b16 a[38:39], v50 offset:512\n\t"
      "v_mfma_f32_16x16x32_bf16 v[96:99], a[96:99], a[60:63], v[96:99]\n\t"
      "ds_read_b128 a[0:3], v47 offset:0\n\t"
      "v_mfma_f32_16x16x32_bf16 v[100:103], a[100:103], a[60:63], v[100:103]\n\t"
      "ds_read_b128 a[4:7], v47 offset:2048\n\t"
      "v_mfma_f32_16x16x32_bf16 v[104:107], a[60:63], a[104:107], v[104:107]\n\t"
      "ds_read_b128 a[8:11], v47 offset:4096\n\t"
      "v_mfma_f32_16x16x32_bf16 v[108:111], a[60:63], a[108:111], v[108:111]\n\t"
      "ds_read_b128 a[12:15], v47 offset:6144\n\t"
      "v_mfma_f32_16x16x32_bf16 v[120:123], a[96:99], a[64:67], v[120:123]\n\t"
      "ds_read_b128 a[16:19], v47 offset:8192\n\t"
      "v_mfma_f32_16x16x32_bf16 v[124:127], a[100:103], a[64:67], v[124:127]\n\t"
      "ds_read_b128 a[20:23], v47 offset:10240\n\t"
      "v_mfma_f32_16x16x32_bf16 v[128:131], a[64:67], a[104:107], v[128:131]\n\t"
      "ds_read_b128 a[24:27], v47 offset:12288\n\t"
      "v_mfma_f32_16x16x32_bf16 v[132:135], a[64:67], a[108:111], v[132:135]\n\t"
      "ds_read_b128 a[28:31], v47 offset:14336\n\t"
      "s_movk_i32 s55, 0\n\t"
      "v_mfma_f32_16x16x32_bf16 v[144:147], a[96:99], a[68:71], v[144:147]\n\t"
      "v_cvt_pk_bf16_f32 v72, v72, v73\n\t"
      "v_mfma_f32_16x16x32_bf16 v[148:151], a[100:103], a[68:71], v[148:151]\n\t"
      "v_cvt_pk_bf16_f32 v73, v74, v75\n\t"
      "v_mfma_f32_16x16x32_bf16 v[152:155], a[68:71], a[104:107], v[152:155]\n\t"
      "v_cvt_pk_bf16_f32 v74, v76, v77\n\t"
      "v_mfma_f32_16x16x32_bf16 v[156:159], a[68:71], a[108:111], v[156:159]\n\t"
      "v_cvt_pk_bf16_f32 v75, v78, v79\n\t"
      "v_mfma_f32_16x16x32_bf16 v[168:171], a[96:99], a[72:75], v[168:171]\n\t"
      "buffer_store_dwordx4 v[72:75], %[vk], %[so], s55 offen offset:0 sc1\n\t"
      "v_mfma_f32_16x16x32_bf16 v[172:175], a[100:103], a[72:75], v[172:175]\n\t"
      "v_cvt_pk_bf16_f32 v96, v96, v97\n\t"
      "v_mfma_f32_16x16x32_bf16 v[176:179], a[72:75], a[104:107], v[176:179]\n\t"
      "v_cvt_pk_bf16_f32 v97, v98, v99\n\t"
      "v_mfma_f32_16x16x32_bf16 v[180:183], a[72:75], a[108:111], v[180:183]\n\t"
      "v_cvt_pk_bf16_f32 v98, v100, v101\n\t"
      "v_mfma_f32_16x16x32_bf16 v[192:195], a[96:99], a[76:79], v[192:195]\n\t"
      "v_cvt_pk_bf16_f32 v99, v102, v103\n\t"
      "v_mfma_f32_16x16x32_bf16 v[196:199], a[100:103], a[76:79], v[196:199]\n\t"
      "buffer_store_dwordx4 v[96:99], %[vk], %[so], s55 offen offset:64 sc1\n\t"
      "v_mfma_f32_16x16x32_bf16 v[200:203], a[76:79], a[104:107], v[200:203]\n\t"
      "v_cvt_pk_bf16_f32 v80, v80, v81\n\t"
      "v_mfma_f32_16x16x32_bf16 v[204:207], a[76:79], a[108:111], v[204:207]\n\t"
      "v_cvt_pk_bf16_f32 v81, v82, v83\n\t"
      "v_mfma_f32_16x16x32_bf16 v[216:219], a[96:99], a[80:83], v[216:219]\n\t"
      "v_cvt_pk_bf16_f32 v82, v104, v105\n\t"
      "v_mfma_f32_16x16x32_bf16 v[220:223], a[100:103], a[80:83], v[220:223]\n\t"
      "v_cvt_pk_bf16_f32 v83, v106, v107\n\t"
      "v_mfma_f32_16x16x32_bf16 v[224:227], a[80:83], a[104:107], v[224:227]\n\t"
      "buffer_store_dwordx4 v[80:83], %[vv], %[so], s55 offen offset:2048 sc1\n\t"
      "v_mfma_f32_16x16x32_bf16 v[228:231], a[80:83], a[108:111], v[228:231]\n\t"
      "v_cvt_pk_bf16_f32 v84, v84, v85\n\t"
      "v_mfma_f32_16x16x32_bf16 v[240:243], a[96:99], a[84:87], v[240:243]\n\t"
      "v_cvt_pk_bf16_f32 v85, v86, v87\n\t"
      "v_mfma_f32_16x16x32_bf16 v[244:247], a[100:103], a[84:87], v[244:247]\n\t"
      "v_cvt_pk_bf16_f32 v86, v108, v109\n\t"
      "v_mfma_f32_16x16x32_bf16 v[248:251], a[84:87], a[104:107], v[248:251]\n\t"
      "v_cvt_pk_bf16_f32 v87, v110, v111\n\t"
      "v_mfma_f32_16x16x32_bf16 v[252:255], a[84:87], a[108:111], v[252:255]\n\t"
      "buffer_store_dwordx4 v[84:87], %[vv], %[so], s55 offen offset:2304 sc1\n\t"
      "s_waitcnt lgkmcnt(0)\n\t"
      "v_mfma_f32_16x16x32_bf16 v[64:67], a[32:35], a[0:3], v[64:67]\n\t"
      "ds_read_b64_tr_b16 a[88:89], v49 offset:4096\n\t"
      "s_movk_i32 s55, 4096\n\t"
      "v_mfma_f32_16x16x32_bf16 v[68:71], a[36:39], a[0:3], v[68:71]\n\t"
      "ds_read_b64_tr_b16 a[90:91], v49 offset:4608\n\t"
      "v_cvt_pk_bf16_f32 v120, v120, v121\n\t"
      "v_mfma_f32_16x16x32_bf16 v[88:91], a[32:35], a[4:7], v[88:91]\n\t"
      "ds_read_b64_tr_b16 a[92:93], v50 offset:4096\n\t"
      "v_cvt_pk_bf16_f32 v121, v122, v123\n\t"
      "v_mfma_f32_16x16x32_bf16 v[92:95], a[36:39], a[4:7], v[92:95]\n\t"
      "ds_read_b64_tr_b16 a[94:95], v50 offset:4608\n\t"
      "v_cvt_pk_bf16_f32 v122, v124, v125\n\t"
      "v_mfma_f32_16x16x32_bf16 v[112:115], a[32:35], a[8:11], v[112:115]\n\t"
      "ds_read_b128 a[56:59], v48 offset:0\n\t"
      "v_cvt_pk_bf16_f32 v123, v126, v127\n\t"
      "v_mfma_f32_16x16x32_bf16 v[116:119], a[36:39], a[8:11], v[116:119]\n\t"
      "ds_read_b128 a[60:63], v48 offset:2048\n\t"
      "buffer_store_dwordx4 v[120:123], %[vk], %[so], s55 offen offset:0 sc1\n\t"
      "v_mfma_f32_16x16x32_bf16 v[136:139], a[32:35], a[12:15], v[136:139]\n\t"
      "ds_read_b128 a[64:67], v48 offset:4096\n\t"
      "v_cvt_pk_bf16_f32 v144, v144, v145\n\t"
      "v_mfma_f32_16x16x32_bf16 v[140:143], a[36:39], a[12:15], v[140:143]\n\t"
      "ds_read_b128 a[68:71], v48 offset:6144\n\t"
      "v_cvt_pk_bf16_f32 v145, v146, v147\n\t"
      "v_mfma_f32_16x16x32_bf16 v[160:163], a[32:35], a[16:19], v[160:163]\n\t"
      "ds_read_b128 a[72:75], v48 offset:8192\n\t"
      "v_cvt_pk_bf16_f32 v146, v148, v149\n\t"
      "v_mfma_f32_16x16x32_bf16 v[164:167], a[36:39], a[16:19], v[164:167]\n\t"
      "ds_read_b128 a[76:79], v48 offset:10240\n\t"
      "v_cvt_pk_bf16_f32 v147, v150, v151\n\t"
      "v_mfma_f32_16x16x32_bf16 v[184:187], a[32:35], a[20:23], v[184:187]\n\t"
      "ds_read_b128 a[80:83], v48 offset:12288\n\t"
      "buffer_store_dwordx4 v[144:147], %[vk], %[so], s55 offen offset:64 sc1\n\t"
      "v_mfma_f32_16x16x32_bf16 v[188:191], a[36:39], a[20:23], v[188:191]\n\t"
      "ds_read_b128 a[84:87], v48 offset:14336\n\t"
      "v_cvt_pk_bf16_f32 v128, v128, v129\n\t"
      "v_mfma_f32_16x16x32_bf16 v[208:211], a[32:35], a[24:27], v[208:211]\n\t"
      "v_cvt_pk_bf16_f32 v129, v130, v131\n\t"
      "v_mfma_f32_16x16x32_bf16 v[212:215], a[36:39], a[24:27], v[212:215]\n\t"
      "v_cvt_pk_bf16_f32 v130, v152, v153\n\t"
      "v_mfma_f32_16x16x32_bf16 v[232:235], a[32:35], a[28:31], v[232:235]\n\t"
      "v_cvt_pk_bf16_f32 v131, v154, v155\n\t"
      "v_mfma_f32_16x16x32_bf16 v[236:239], a[36:39], a[28:31], v[236:239]\n\t"
      "buffer_store_dwordx4 v[128:131], %[vv], %[so], s55 offen offset:2048 sc1\n\t"
      "s_waitcnt lgkmcnt(0)\n\t"
      "v_mfma_f32_16x16x32_bf16 v[64:67], a[88:91], a[56:59], v[64:67]\n\t"
      "ds_read_b64_tr_b16 a[32:33], v41 offset:0\n\t"
      "v_cvt_pk_bf16_f32 v132, v132, v133\n\t"
      "v_mfma_f32_16x16x32_bf16 v[68:71], a[92:95], a[56:59], v[68:71]\n\t"
      "ds_read_b64_tr_b16 a[34:35], v41 offset:512\n\t"
      "v_cvt_pk_bf16_f32 v133, v134, v135\n\t"
      "v_mfma_f32_16x16x32_bf16 v[88:91], a[88:91], a[60:63], v[88:91]\n\t"
      "ds_read_b64_tr_b16 a[36:37], v42 offset:0\n\t"
      "v_cvt_pk_bf16_f32 v134, v156, v157\n\t"
      "v_mfma_f32_16x16x32_bf16 v[92:95], a[92:95], a[60:63], v[92:95]\n\t"
      "ds_read_b64_tr_b16 a[38:39], v42 offset:512\n\t"
      "v_cvt_pk_bf16_f32 v135, v158, v159\n\t"
      "v_mfma_f32_16x16x32_bf16 v[112:115], a[88:91], a[64:67], v[112:115]\n\t"
      "ds_read_b128 a[0:3], v51 offset:0\n\t"
      "buffer_store_dwordx4 v[132:135], %[vv], %[so], s55 offen offset:2304 sc1\n\t"
      "v_mfma_f32_16x16x32_bf16 v[116:119], a[92:95], a[64:67], v[116:119]\n\t"
      "ds_read_b128 a[4:7], v51 offset:2048\n\t"
      "s_movk_i32 s55, 8192\n\t"
      "v_mfma_f32_16x16x32_bf16 v[136:139], a[88:91], a[68:71], v[136:139]\n\t"
      "ds_read_b128 a[8:11], v51 offset:4096\n\t"
      "v_cvt_pk_bf16_f32 v168, v168, v169\n\t"
      "v_mfma_f32_16x16x32_bf16 v[140:143], a[92:95], a[68:71], v[140:143]\n\t"
      "ds_read_b128 a[12:15], v51 offset:6144\n\t"
      "v_cvt_pk_bf16_f32 v169, v170, v171\n\t"
      "v_mfma_f32_16x16x32_bf16 v[160:163], a[88:91], a[72:75], v[160:163]\n\t"
      "ds_read_b128 a[16:19], v51 offset:8192\n\t"
      "v_cvt_pk_bf16_f32 v170, v172, v173\n\t"
      "v_mfma_f32_16x16x32_bf16 v[164:167], a[92:95], a[72:75], v[164:167]\n\t"
      "ds_read_b128 a[20:23], v51 offset:10240\n\t"
      "v_cvt_pk_bf16_f32 v171, v174, v175\n\t"
      "v_mfma_f32_16x16x32_bf16 v[184:187], a[88:91], a[76:79], v[184:187]\n\t"
      "ds_read_b128 a[24:27], v51 offset:12288\n\t"
      "buffer_store_dwordx4 v[168:171], %[vk], %[so], s55 offen offset:0 sc1\n\t"
      "v_mfma_f32_16x16x32_bf16 v[188:191], a[92:95], a[76:79], v[188:191]\n\t"
      "ds_read_b128 a[28:31], v51 offset:14336\n\t"
      "v_cvt_pk_bf16_f32 v192, v192, v193\n\t"
      "v_mfma_f32_16x16x32_bf16 v[208:211], a[88:91], a[80:83], v[208:211]\n\t"
      "v_cvt_pk_bf16_f32 v193, v194, v195\n\t"
      "v_mfma_f32_16x16x32_bf16 v[212:215], a[92:95], a[80:83], v[212:215]\n\t"
      "v_cvt_pk_bf16_f32 v194, v196, v197\n\t"
      "v_mfma_f32_16x16x32_bf16 v[232:235], a[88:91], a[84:87], v[232:235]\n\t"
      "v_cvt_pk_bf16_f32 v195, v198, v199\n\t"
      "v_mfma_f32_16x16x32_bf16 v[236:239], a[92:95], a[84:87], v[236:239]\n\t"
      "buffer_store_dwordx4 v[192:195], %[vk], %[so], s55 offen offset:64 sc1\n\t"
      "s_waitcnt lgkmcnt(0)\n\t"
      "v_mfma_f32_16x16x32_bf16 v[64:67], a[32:35], a[0:3], v[64:67]\n\t"
      "ds_read_b64_tr_b16 a[88:89], v41 offset:4096\n\t"
      "v_cvt_pk_bf16_f32 v176, v176, v177\n\t"
      "v_mfma_f32_16x16x32_bf16 v[68:71], a[36:39], a[0:3], v[68:71]\n\t"
      "ds_read_b64_tr_b16 a[90:91], v41 offset:4608\n\t"
      "v_cvt_pk_bf16_f32 v177, v178, v179\n\t"
      "v_mfma_f32_16x16x32_bf16 v[88:91], a[32:35], a[4:7], v[88:91]\n\t"
      "ds_read_b64_tr_b16 a[92:93], v42 offset:4096\n\t"
      "v_cvt_pk_bf16_f32 v178, v200, v201\n\t"
      "v_mfma_f32_16x16x32_bf16 v[92:95], a[36:39], a[4:7], v[92:95]\n\t"
      "ds_read_b64_tr_b16 a[94:95], v42 offset:4608\n\t"
      "v_cvt_pk_bf16_f32 v179, v202, v203\n\t"
      "v_mfma_f32_16x16x32_bf16 v[112:115], a[32:35], a[8:11], v[112:115]\n\t"
      "ds_read_b128 a[56:59], v52 offset:0\n\t"
      "buffer_store_dwordx4 v[176:179], %[vv], %[so], s55 offen offset:2048 sc1\n\t"
      "v_mfma_f32_16x16x32_bf16 v[116:119], a[36:39], a[8:11], v[116:119]\n\t"
      "ds_read_b128 a[60:63], v52 offset:2048\n\t"
      "v_cvt_pk_bf16_f32 v180, v180, v181\n\t"
      "v_mfma_f32_16x16x32_bf16 v[136:139], a[32:35], a[12:15], v[136:139]\n\t"
      "ds_read_b128 a[64:67], v52 offset:4096\n\t"
      "v_cvt_pk_bf16_f32 v181, v182, v183\n\t"
      "v_mfma_f32_16x16x32_bf16 v[140:143], a[36:39], a[12:15], v[140:143]\n\t"
      "ds_read_b128 a[68:71], v52 offset:6144\n\t"
      "v_cvt_pk_bf16_f32 v182, v204, v205\n\t"
      "v_mfma_f32_16x16x32_bf16 v[160:163], a[32:35], a[16:19], v[160:163]\n\t"
      "ds_read_b128 a[72:75], v52 offset:8192\n\t"
      "v_cvt_pk_bf16_f32 v183, v206, v207\n\t"
      "v_mfma_f32_16x16x32_bf16 v[164:167], a[36:39], a[16:19], v[164:167]\n\t"
      "ds_read_b128 a[76:79], v52 offset:10240\n\t"
      "buffer_store_dwordx4 v[180:183], %[vv], %[so], s55 offen offset:2304 sc1\n\t"
      "v_mfma_f32_16x16x32_bf16 v[184:187], a[32:35], a[20:23], v[184:187]\n\t"
      "ds_read_b128 a[80:83], v52 offset:12288\n\t"
      "s_movk_i32 s55, 12288\n\t"
      "v_mfma_f32_16x16x32_bf16 v[188:191], a[36:39], a[20:23], v[188:191]\n\t"
      "ds_read_b128 a[84:87], v52 offset:14336\n\t"
      "v_cvt_pk_bf16_f32 v216, v216, v217\n\t"
      "v_mfma_f32_16x16x32_bf16 v[208:211], a[32:35], a[24:27], v[208:211]\n\t"
      "v_cvt_pk_bf16_f32 v217, v218, v219\n\t"
      "v_mfma_f32_16x16x32_bf16 v[212:215], a[36:39], a[24:27], v[212:215]\n\t"
      "v_cvt_pk_bf16_f32 v218, v220, v221\n\t"
      "v_mfma_f32_16x16x32_bf16 v[232:235], a[32:35], a[28:31], v[232:235]\n\t"
      "v_cvt_pk_bf16_f32 v219, v222, v223\n\t"
      "v_mfma_f32_16x16x32_bf16 v[236:239], a[36:39], a[28:31], v[236:239]\n\t"
      "buffer_store_dwordx4 v[216:219], %[vk], %[so], s55 offen offset:0 sc1\n\t"
      "s_waitcnt lgkmcnt(0)\n\t"
      "v_mfma_f32_16x16x32_bf16 v[64:67], a[88:91], a[56:59], v[64:67]\n\t"
      "v_cvt_pk_bf16_f32 v240, v240, v241\n\t"
      "v_mfma_f32_16x16x32_bf16 v[68:71], a[92:95], a[56:59], v[68:71]\n\t"
      "v_cvt_pk_bf16_f32 v241, v242, v243\n\t"
      "v_mfma_f32_16x16x32_bf16 v[88:91], a[88:91], a[60:63], v[88:91]\n\t"
      "v_cvt_pk_bf16_f32 v242, v244, v245\n\t"
      "v_mfma_f32_16x16x32_bf16 v[92:95], a[92:95], a[60:63], v[92:95]\n\t"
      "v_cvt_pk_bf16_f32 v243, v246, v247\n\t"
      "v_mfma_f32_16x16x32_bf16 v[112:115], a[88:91], a[64:67], v[112:115]\n\t"
      "buffer_store_dwordx4 v[240:243], %[vk], %[so], s55 offen offset:64 sc1\n\t"
      "v_mfma_f32_16x16x32_bf16 v[116:119], a[92:95], a[64:67], v[116:119]\n\t"
      "v_cvt_pk_bf16_f32 v224, v224, v225\n\t"
      "v_mfma_f32_16x16x32_bf16 v[136:139], a[88:91], a[68:71], v[136:139]\n\t"
      "v_cvt_pk_bf16_f32 v225, v226, v227\n\t"
      "v_mfma_f32_16x16x32_bf16 v[140:143], a[92:95], a[68:71], v[140:143]\n\t"
      "v_cvt_pk_bf16_f32 v226, v248, v249\n\t"
      "v_mfma_f32_16x16x32_bf16 v[160:163], a[88:91], a[72:75], v[160:163]\n\t"
      "v_cvt_pk_bf16_f32 v227, v250, v251\n\t"
      "v_mfma_f32_16x16x32_bf16 v[164:167], a[92:95], a[72:75], v[164:167]\n\t"
      "buffer_store_dwordx4 v[224:227], %[vv], %[so], s55 offen offset:2048 sc1\n\t"
      "v_mfma_f32_16x16x32_bf16 v[184:187], a[88:91], a[76:79], v[184:187]\n\t"
      "v_cvt_pk_bf16_f32 v228, v228, v229\n\t"
      "v_mfma_f32_16x16x32_bf16 v[188:191], a[92:95], a[76:79], v[188:191]\n\t"
      "v_cvt_pk_bf16_f32 v229, v230, v231\n\t"
      "v_mfma_f32_16x16x32_bf16 v[208:211], a[88:91], a[80:83], v[208:211]\n\t"
      "v_cvt_pk_bf16_f32 v230, v252, v253\n\t"
      "v_mfma_f32_16x16x32_bf16 v[212:215], a[92:95], a[80:83], v[212:215]\n\t"
      "v_cvt_pk_bf16_f32 v231, v254, v255\n\t"
      "v_mfma_f32_16x16x32_bf16 v[232:235], a[88:91], a[84:87], v[232:235]\n\t"
      "buffer_store_dwordx4 v[228:231], %[vv], %[so], s55 offen offset:2304 sc1\n\t"
      "v_mfma_f32_16x16x32_bf16 v[236:239], a[92:95], a[84:87], v[236:239]\n\t"
      "s_barrier\n\t"
      "s_movk_i32 s55, 0\n\t"
      "v_mul_f32 v64, 0x3e8293ee, v64\n\t"
      "v_mul_f32 v65, 0x3e8293ee, v65\n\t"
      "v_mul_f32 v66, 0x3e8293ee, v66\n\t"
      "v_mul_f32 v67, 0x3e8293ee, v67\n\t"
      "v_mul_f32 v68, 0x3e8293ee, v68\n\t"
      "v_mul_f32 v69, 0x3e8293ee, v69\n\t"
      "v_mul_f32 v70, 0x3e8293ee, v70\n\t"
      "v_mul_f32 v71, 0x3e8293ee, v71\n\t"
      "v_cvt_pk_bf16_f32 v64, v64, v65\n\t"
      "v_cvt_pk_bf16_f32 v65, v66, v67\n\t"
      "v_cvt_pk_bf16_f32 v66, v68, v69\n\t"
      "v_cvt_pk_bf16_f32 v67, v70, v71\n\t"
      "buffer_store_dwordx4 v[64:67], %[vq], %[so], s55 offen offset:0 sc1\n\t"
      "s_nop 0\n\t"
      "v_mfma_f32_16x16x32_bf16 v[68:71], v[72:75], v[64:67], 0\n\t"
      "v_mul_f32 v88, 0x3e8293ee, v88\n\t"
      "v_mul_f32 v89, 0x3e8293ee, v89\n\t"
      "v_mul_f32 v90, 0x3e8293ee, v90\n\t"
      "v_mul_f32 v91, 0x3e8293ee, v91\n\t"
      "v_mul_f32 v92, 0x3e8293ee, v92\n\t"
      "v_mul_f32 v93, 0x3e8293ee, v93\n\t"
      "v_mul_f32 v94, 0x3e8293ee, v94\n\t"
      "v_mul_f32 v95, 0x3e8293ee, v95\n\t"
      "v_cvt_pk_bf16_f32 v88, v88, v89\n\t"
      "v_cvt_pk_bf16_f32 v89, v90, v91\n\t"
      "v_cvt_pk_bf16_f32 v90, v92, v93\n\t"
      "v_cvt_pk_bf16_f32 v91, v94, v95\n\t"
      "buffer_store_dwordx4 v[88:91], %[vq], %[so], s55 offen offset:256 sc1\n\t"
      "s_nop 0\n\t"
      "v_mfma_f32_16x16x32_bf16 v[92:95], v[96:99], v[88:91], 0\n\t"
      "s_movk_i32 s55, 2048\n\t"
      "v_mul_f32 v112, 0x3e8293ee, v112\n\t"
      "v_mul_f32 v113, 0x3e8293ee, v113\n\t"
      "v_mul_f32 v114, 0x3e8293ee, v114\n\t"
      "v_mul_f32 v115, 0x3e8293ee, v115\n\t"
      "v_mul_f32 v116, 0x3e8293ee, v116\n\t"
      "v_mul_f32 v117, 0x3e8293ee, v117\n\t"
      "v_mul_f32 v118, 0x3e8293ee, v118\n\t"
      "v_mul_f32 v119, 0x3e8293ee, v119\n\t"
      "v_cvt_pk_bf16_f32 v112, v112, v113\n\t"
      "v_cvt_pk_bf16_f32 v113, v114, v115\n\t"
      "v_cvt_pk_bf16_f32 v114, v116, v117\n\t"
      "v_cvt_pk_bf16_f32 v115, v118, v119\n\t"
      "buffer_store_dwordx4 v[112:115], %[vq], %[so], s55 offen offset:0 sc1\n\t"
      "s_nop 0\n\t"
      "v_mfma_f32_16x16x32_bf16 v[116:119], v[120:123], v[112:115], 0\n\t"
      "v_mul_f32 v136, 0x3e8293ee, v136\n\t"
      "v_mul_f32 v137, 0x3e8293ee, v137\n\t"
      "v_mul_f32 v138, 0x3e8293ee, v138\n\t"
      "v_mul_f32 v139, 0x3e8293ee, v139\n\t"
      "v_mul_f32 v140, 0x3e8293ee, v140\n\t"
      "v_mul_f32 v141, 0x3e8293ee, v141\n\t"
      "v_mul_f32 v142, 0x3e8293ee, v142\n\t"
      "v_mul_f32 v143, 0x3e8293ee, v143\n\t"
      "v_cvt_pk_bf16_f32 v136, v136, v137\n\t"
      "v_cvt_pk_bf16_f32 v137, v138, v139\n\t"
      "v_cvt_pk_bf16_f32 v138, v140, v141\n\t"
      "v_cvt_pk_bf16_f32 v139, v142, v143\n\t"
      "buffer_store_dwordx4 v[136:139], %[vq], %[so], s55 offen offset:256 sc1\n\t"
      "s_nop 0\n\t"
      "v_mfma_f32_16x16x32_bf16 v[140:143], v[144:147], v[136:139], 0\n\t"
      "s_movk_i32 s55, 4096\n\t"
      "v_mul_f32 v160, 0x3e8293ee, v160\n\t"
      "v_mul_f32 v161, 0x3e8293ee, v161\n\t"
      "v_mul_f32 v162, 0x3e8293ee, v162\n\t"
      "v_mul_f32 v163, 0x3e8293ee, v163\n\t"
      "v_mul_f32 v164, 0x3e8293ee, v164\n\t"
      "v_mul_f32 v165, 0x3e8293ee, v165\n\t"
      "v_mul_f32 v166, 0x3e8293ee, v166\n\t"
      "v_mul_f32 v167, 0x3e8293ee, v167\n\t"
      "v_cvt_pk_bf16_f32 v160, v160, v161\n\t"
      "v_cvt_pk_bf16_f32 v161, v162, v163\n\t"
      "v_cvt_pk_bf16_f32 v162, v164, v165\n\t"
      "v_cvt_pk_bf16_f32 v163, v166, v167\n\t"
      "buffer_store_dwordx4 v[160:163], %[vq], %[so], s55 offen offset:0 sc1\n\t"
      "s_nop 0\n\t"
      "v_mfma_f32_16x16x32_bf16 v[164:167], v[168:171], v[160:163], 0\n\t"
      "v_mul_f32 v184, 0x3e8293ee, v184\n\t"
      "v_mul_f32 v185, 0x3e8293ee, v185\n\t"
      "v_mul_f32 v186, 0x3e8293ee, v186\n\t"
      "v_mul_f32 v187, 0x3e8293ee, v187\n\t"
      "v_mul_f32 v188, 0x3e8293ee, v188\n\t"
      "v_mul_f32 v189, 0x3e8293ee, v189\n\t"
      "v_mul_f32 v190, 0x3e8293ee, v190\n\t"
      "v_mul_f32 v191, 0x3e8293ee, v191\n\t"
      "v_cvt_pk_bf16_f32 v184, v184, v185\n\t"
      "v_cvt_pk_bf16_f32 v185, v186, v187\n\t"
      "v_cvt_pk_bf16_f32 v186, v188, v189\n\t"
      "v_cvt_pk_bf16_f32 v187, v190, v191\n\t"
      "buffer_store_dwordx4 v[184:187], %[vq], %[so], s55 offen offset:256 sc1\n\t"
      "s_nop 0\n\t"
      "v_mfma_f32_16x16x32_bf16 v[188:191], v[192:195], v[184:187], 0\n\t"
      "s_movk_i32 s55, 6144\n\t"
      "v_mul_f32 v208, 0x3e8293ee, v208\n\t"
      "v_mul_f32 v209, 0x3e8293ee, v209\n\t"
      "v_mul_f32 v210, 0x3e8293ee, v210\n\t"
      "v_mul_f32 v211, 0x3e8293ee, v211\n\t"
      "v_mul_f32 v212, 0x3e8293ee, v212\n\t"
      "v_mul_f32 v213, 0x3e8293ee, v213\n\t"
      "v_mul_f32 v214, 0x3e8293ee, v214\n\t"
      "v_mul_f32 v215, 0x3e8293ee, v215\n\t"
      "v_cvt_pk_bf16_f32 v208, v208, v209\n\t"
      "v_cvt_pk_bf16_f32 v209, v210, v211\n\t"
      "v_cvt_pk_bf16_f32 v210, v212, v213\n\t"
      "v_cvt_pk_bf16_f32 v211, v214, v215\n\t"
      "buffer_store_dwordx4 v[208:211], %[vq], %[so], s55 offen offset:0 sc1\n\t"
      "s_nop 0\n\t"
      "v_mfma_f32_16x16x32_bf16 v[212:215], v[216:219], v[208:211], 0\n\t"
      "v_mul_f32 v232, 0x3e8293ee, v232\n\t"
      "v_mul_f32 v233, 0x3e8293ee, v233\n\t"
      "v_mul_f32 v234, 0x3e8293ee, v234\n\t"
      "v_mul_f32 v235, 0x3e8293ee, v235\n\t"
      "v_mul_f32 v236, 0x3e8293ee, v236\n\t"
      "v_mul_f32 v237, 0x3e8293ee, v237\n\t"
      "v_mul_f32 v238, 0x3e8293ee, v238\n\t"
      "v_mul_f32 v239, 0x3e8293ee, v239\n\t"
      "v_cvt_pk_bf16_f32 v232, v232, v233\n\t"
      "v_cvt_pk_bf16_f32 v233, v234, v235\n\t"
      "v_cvt_pk_bf16_f32 v234, v236, v237\n\t"
      "v_cvt_pk_bf16_f32 v235, v238, v239\n\t"
      "buffer_store_dwordx4 v[232:235], %[vq], %[so], s55 offen offset:256 sc1\n\t"
      "s_nop 0\n\t"
      "v_mfma_f32_16x16x32_bf16 v[236:239], v[240:243], v[232:235], 0\n\t"
      "v_max3_f32 v68, v68, v69, v70\n\t"
      "v_max_f32 v68, v68, v71\n\t"
      "v_max3_f32 v92, v92, v93, v94\n\t"
      "v_max_f32 v92, v92, v95\n\t"
      "v_max3_f32 v116, v116, v117, v118\n\t"
      "v_max_f32 v116, v116, v119\n\t"
      "v_max3_f32 v140, v140, v141, v142\n\t"
      "v_max_f32 v140, v140, v143\n\t"
      "v_max3_f32 v164, v164, v165, v166\n\t"
      "v_max_f32 v164, v164, v167\n\t"
      "v_max3_f32 v188, v188, v189, v190\n\t"
      "v_max_f32 v188, v188, v191\n\t"
      "v_max3_f32 v212, v212, v213, v214\n\t"
      "v_max_f32 v212, v212, v215\n\t"
      "v_max3_f32 v236, v236, v237, v238\n\t"
      "v_max_f32 v236, v236, v239\n\t"
      "v_permlane32_swap_b32 v68, v116\n\t"
      "v_permlane32_swap_b32 v92, v140\n\t"
      "v_permlane32_swap_b32 v164, v212\n\t"
      "v_permlane32_swap_b32 v188, v236\n\t"
      "v_max_f32 v68, v68, v116\n\t"
      "v_max_f32 v92, v92, v140\n\t"
      "v_max_f32 v164, v164, v212\n\t"
      "v_max_f32 v188, v188, v236\n\t"
      "v_permlane16_swap_b32 v68, v92\n\t"
      "s_nop 0\n\t"
      "v_permlane16_swap_b32 v164, v188\n\t"
      "v_max_f32 v68, v68, v92\n\t"
      "v_max_f32 v164, v164, v188\n\t"
      "v_sub_f32 v68, 0, v68\n\t"
      "v_sub_f32 v164, 0, v164\n\t"
      "buffer_store_dword v68, %[vm], %[so], 0 offen offset:0 sc1\n\t"
      "buffer_store_dword v164, %[vm], %[so], 0 offen offset:256 sc1\n\t"
      "s_mov_b64 s[40:41], %[pkv]\n\t"
      "s_mov_b32 s42, 0x40000\n\t"
      "s_mov_b32 s43, 0x20000\n\t"
      "s_mov_b64 s[44:45], %[pwb]\n\t"
      "s_mov_b32 s46, 0x200000\n\t"
      "s_mov_b32 s47, 0x20000\n\t"
      "s_lshl_b32 s54, %[wid], 12\n\t"
      "v_add_u32 v60, 0, %[vw]\n\t"
      "v_add_u32 v61, 15360, %[vw]\n\t"
      "v_add_u32 v62, 30720, %[vw]\n\t"
      "v_add_u32 v63, 46080, %[vw]\n\t"
      "s_waitcnt vmcnt(10)\n\t"
      "s_mov_b64 exec, 1\n\t"
      "global_store_dword %[vfk], %[tag], %[pf] sc1\n\t"
      "s_mov_b64 exec, -1\n\t"
      "s_waitcnt vmcnt(1)\n\t"
      "s_mov_b64 exec, 1\n\t"
      "global_store_dword %[vf1], %[tag], %[pf] sc1\n\t"
      "s_mov_b64 exec, -1\n\t"
      "s_barrier\n\t"
      "buffer_load_dwordx4 a[240:243], %[vqo], %[so], 0 offen offset:0 sc1\n\t"
      "buffer_load_dwordx4 a[244:247], %[vqo], %[so], 0 offen offset:1024 sc1\n\t"
      "buffer_load_dwordx4 a[248:251], %[vqo], %[so], 0 offen offset:2048 sc1\n\t"
      "buffer_load_dwordx4 a[252:255], %[vqo], %[so], 0 offen offset:3072 sc1\n\t"
      "buffer_load_dword a238, %[vmo], %[so], 0 offen offset:0 sc1\n\t"
      "buffer_load_dword a239, %[vmo], %[so], 0 offen offset:128 sc1\n\t"
      "s_add_u32 s52, %[krot], 0\n\t"
      "s_add_u32 m0, s54, 0\n\t"
      "s_and_b32 s52, s52, 0x3ffff\n\t"
      "s_nop 0\n\t"
      "buffer_load_dwordx4 %[vg], s[40:43], s52 offen offset:0 sc1 lds\n\t"
      "buffer_load_dwordx4 %[vg], s[40:43], s52 offen offset:1024 sc1 lds\n\t"
      "buffer_load_dwordx4 %[vg], s[40:43], s52 offen offset:2048 sc1 lds\n\t"
      "buffer_load_dwordx4 %[vg], s[40:43], s52 offen offset:3072 sc1 lds\n\t"
      "buffer_load_dword a237, %[vg], s[40:43], 0 offen sc1\n\t"
      "buffer_load_dword a237, %[vg], s[40:43], 0 offen sc1\n\t"
      "buffer_load_dword a237, %[vg], s[40:43], 0 offen sc1\n\t"
      "buffer_load_dword a237, %[vg], s[40:43], 0 offen sc1\n\t"
      "buffer_load_dword a237, %[vg], s[40:43], 0 offen sc1\n\t"
      "buffer_load_dword a237, %[vg], s[40:43], 0 offen sc1\n\t"
      "buffer_load_dword a237, %[vg], s[40:43], 0 offen sc1\n\t"
      "buffer_load_dword a237, %[vg], s[40:43], 0 offen sc1\n\t"
        : [tag] "=&v"(tag), "={a[240:255]}"(qfr), "={a[238:239]}"(ref)
        : [px] "s"(px), [pw] "s"(pw), [wid] "s"(wid), [vx0] "v"(vx0), [vw0] "v"(vw0),
          [xr] "v"(xr), [wr] "v"(wrd), [vq] "v"(vq), [vk] "v"(vk), [vv] "v"(vv), [vm] "v"(vm), [so] "s"(sws), [pf] "s"(pf), [vf1] "v"(vf1), [vfk] "v"(vfk),
          [pkv] "s"(pkv), [pwb] "s"(wob), [vw] "v"(vw), [wv1k] "s"(wv1k), [vqo] "v"(vqo), [vmo] "v"(vmo), [vg] "v"(vg), [cpo] "v"(cpo), [krot] "s"(krot)
        : "v32", "v33", "v34", "v35", "v36", "v37", "v38", "v39", "v40", "v41", "v42", "v43", "v44", "v45", "v46", "v47", "v48", "v49", "v50", "v51", "v52", "v53", "v54", "v55", "v56", "v57", "v58", "v59", "v60", "v61", "v62", "v63", "v64", "v65", "v66", "v67", "v68", "v69", "v70", "v71", "v72", "v73", "v74", "v75", "v76", "v77", "v78", "v79", "v80", "v81", "v82", "v83", "v84", "v85", "v86", "v87", "v88", "v89", "v90", "v91", "v92", "v93", "v94", "v95", "v96", "v97", "v98", "v99", "v100", "v101", "v102", "v103", "v104", "v105", "v106", "v107", "v108", "v109", "v110", "v111", "v112", "v113", "v114", "v115", "v116", "v117", "v118", "v119", "v120", "v121", "v122", "v123", "v124", "v125", "v126", "v127", "v128", "v129", "v130", "v131", "v132", "v133", "v134", "v135", "v136", "v137", "v138", "v139", "v140", "v141", "v142", "v143", "v144", "v145", "v146", "v147", "v148", "v149", "v150", "v151", "v152", "v153", "v154", "v155", "v156", "v157", "v158", "v159", "v160", "v161", "v162", "v163", "v164", "v165", "v166", "v167", "v168", "v169", "v170", "v171", "v172", "v173", "v174", "v175", "v176", "v177", "v178", "v179", "v180", "v181", "v182", "v183", "v184", "v185", "v186", "v187", "v188", "v189", "v190", "v191", "v192", "v193", "v194", "v195", "v196", "v197", "v198", "v199", "v200", "v201", "v202", "v203", "v204", "v205", "v206", "v207", "v208", "v209", "v210", "v211", "v212", "v213", "v214", "v215", "v216", "v217", "v218", "v219", "v220", "v221", "v222", "v223", "v224", "v225", "v226", "v227", "v228", "v229", "v230", "v231", "v232", "v233", "v234", "v235", "v236", "v237", "v238", "v239", "v240", "v241", "v242", "v243", "v244", "v245", "v246", "v247", "v248", "v249", "v250", "v251", "v252", "v253", "v254", "v255", "a0", "a1", "a2", "a3", "a4", "a5", "a6", "a7", "a8", "a9", "a10", "a11", "a12", "a13", "a14", "a15", "a16", "a17", "a18", "a19", "a20", "a21", "a22", "a23", "a24", "a25", "a26", "a27", "a28", "a29", "a30", "a31", "a32", "a33", "a34", "a35", "a36", "a37", "a38", "a39", "a40", "a41", "a42", "a43", "a44", "a45", "a46", "a47", "a48", "a49", "a50", "a51", "a52", "a53", "a54", "a55", "a56", "a57", "a58", "a59", "a60", "a61", "a62", "a63", "a64", "a65", "a66", "a67", "a68", "a69", "a70", "a71", "a72", "a73", "a74", "a75", "a76", "a77", "a78", "a79", "a80", "a81", "a82", "a83", "a84", "a85", "a86", "a87", "a88", "a89", "a90", "a91", "a92", "a93", "a94", "a95", "a96", "a97", "a98", "a99", "a100", "a101", "a102", "a103", "a104", "a105", "a106", "a107", "a108", "a109", "a110", "a111", "a112", "a113", "a114", "a115", "a116", "a117", "a118", "a119", "a120", "a121", "a122", "a123", "a124", "a125", "a126", "a127", "a128", "a129", "a130", "a131", "a132", "a133", "a134", "a135", "a237", "s40", "s41", "s42", "s43", "s44", "s45", "s46", "s47", "s48", "s49", "s50", "s51", "s52", "s53", "s54", "s55", "s56", "s57", "s58", "s59", "s60", "s61", "s62", "s63", "s92", "s93", "s94", "s95", "s96", "s97", "s98", "s99", "scc", "vcc", "memory");
    } else {
      const unsigned xh = base + 147456u + swz;
      const unsigned pk = F_KV + ahead * 2048u + spread(2u * mb + (l & 1u), FS1), pq = F_Q + (ahead * 8u + mb) * 128u + (l & 1u) * 4u;
      const unsigned vp1 = pq + ((l >> 1) & 1u) * (pk - pq);
      const unsigned par = 0x5555u << (s & 1u), le = (2u << s) - 1u;
      const unsigned mc = (~(par & le) >> (4u * g)) & 15u, mp = (~(par & ~((4u << s) - 1u)) >> (4u * g)) & 15u;
      const unsigned msk = mc | (mp << 4) | ((mp | ((mb | wr) ? 0u : 15u)) << 8);
      const unsigned vq = WS_O + 0x400000u + (16u * mb + 8u * wr) * 32768u + head * 1024u + l * 16u;
      asm volatile(
      "s_mov_b64 s[40:41], %[px]\n\t"
      "s_mov_b32 s42, 0x400000\n\t"
      "s_mov_b32 s43, 0x20000\n\t"
      "s_mov_b64 s[44:45], %[pw]\n\t"
      "s_mov_b32 s46, 0x600000\n\t"
      "s_mov_b32 s47, 0x20000\n\t"
      "v_mov_b32 v41, %[vw0]\n\t"
      "v_xor_b32 v42, 64, %[vw0]\n\t"
      "v_add_u32 v42, 49152, v42\n\t"
      "v_add_u32 v43, 2048, v41\n\t"
      "v_add_u32 v44, 2048, v42\n\t"
      "v_add_u32 v45, 4096, v41\n\t"
      "v_add_u32 v46, 4096, v42\n\t"
      "v_mov_b32 v32, %[vx0]\n\t"
      "v_xor_b32 v33, 64, %[vx0]\n\t"
      "v_add_u32 v33, 16384, v33\n\t"
      "v_add_u32 v34, 32768, v32\n\t"
      "v_add_u32 v35, 32768, v33\n\t"
      "v_add_u32 v36, 65536, v32\n\t"
      "v_add_u32 v37, 65536, v33\n\t"
      "v_add_u32 v38, 98304, v32\n\t"
      "v_add_u32 v39, 98304, v33\n\t"
      "v_mov_b32 v40, %[vxh]\n\t"
      "v_mov_b32 v53, 0\n\t"
      "global_load_dword v53, v53, %[pf] sc1\n\t"
      "s_getpc_b64 s[62:63]\n\t"
      ".Lpc0_%=:\n"
      "s_sub_u32 s62, s62, .Lpc0_%=-.Lfwd_begin\n\t"
      "s_subb_u32 s63, s63, 0\n\t"
      "v_min_u32 v54, .Lfwd_end-.Lfwd_begin-16, %[cpo]\n\t"
      "s_mov_b64 s[96:97], %[pw]\n\t"
      "s_mov_b32 s98, 0x600000\n\t"
      "s_mov_b32 s99, 0x20000\n\t"
      "v_and_b32 v55, 1023, %[vg]\n\t"
      "v_add_u32 v62, %[wv1k], v55\n\t"
      "v_add_u32 v55, 786432, v62\n\t"
      "s_lshl_b32 s51, %[wid], 11\n\t"
      "s_add_u32 s51, s51, 122880\n\t"
      "s_mov_b32 s53, 393216\n\t"
      "s_add_u32 m0, s51, 0\n\t"
      "s_nop 0\n\t"
      "buffer_load_dwordx4 v41, s[44:47], s53 offen lds\n\t"
      "s_add_u32 m0, s51, 1024\n\t"
      "s_nop 0\n\t"
      "buffer_load_dwordx4 v42, s[44:47], s53 offen lds\n\t"
      "s_add_u32 m0, s51, 8192\n\t"
      "s_nop 0\n\t"
      "buffer_load_dwordx4 v43, s[44:47], s53 offen lds\n\t"
      "s_add_u32 m0, s51, 9216\n\t"
      "s_nop 0\n\t"
      "buffer_load_dwordx4 v44, s[44:47], s53 offen lds\n\t"
      "s_add_u32 m0, s51, 16384\n\t"
      "s_nop 0\n\t"
      "buffer_load_dwordx4 v45, s[44:47], s53 offen lds\n\t"
      "s_add_u32 m0, s51, 17408\n\t"
      "s_nop 0\n\t"
      "buffer_load_dwordx4 v46, s[44:47], s53 offen lds\n\t"
      "s_lshl_b32 s49, %[wid], 13\n\t"
      "s_add_u32 s49, s49, 32768\n\t"
      "s_mov_b32 s52, 128\n\t"
      "s_add_u32 s56, %[hd], 2048\n\t"
      "s_add_u32 m0, s49, 0\n\t"
      "s_nop 0\n\t"
      "buffer_load_dwordx4 v32, s[40:43], s52 offen lds\n\t"
      "s_add_u32 m0, s49, 1024\n\t"
      "s_nop 0\n\t"
      "buffer_load_dwordx4 v33, s[40:43], s52 offen lds\n\t"
      "s_add_u32 m0, s49, 2048\n\t"
      "s_nop 0\n\t"
      "buffer_load_dwordx4 v34, s[40:43], s52 offen lds\n\t"
      "s_add_u32 m0, s49, 3072\n\t"
      "s_nop 0\n\t"
      "buffer_load_dwordx4 v35, s[40:43], s52 offen lds\n\t"
      "s_add_u32 m0, s49, 4096\n\t"
      "s_nop 0\n\t"
      "buffer_load_dwordx4 v36, s[40:43], s52 offen lds\n\t"
      "s_add_u32 m0, s49, 5120\n\t"
      "s_nop 0\n\t"
      "buffer_load_dwordx4 v37, s[40:43], s52 offen lds\n\t"
      "s_add_u32 m0, s49, 6144\n\t"
      "s_nop 0\n\t"
      "buffer_load_dwordx4 v38, s[40:43], s52 offen lds\n\t"
      "s_add_u32 m0, s49, 7168\n\t"
      "s_nop 0\n\t"
      "buffer_load_dwordx4 v39, s[40:43], s52 offen lds\n\t"
      "s_mov_b32 m0, s56\n\t"
      "s_nop 0\n\t"
      "buffer_load_dwordx4 v40, s[40:43], s52 offen lds\n\t"
      "s_lshl_b32 s49, %[wid], 13\n\t"
      "s_add_u32 s49, s49, 65536\n\t"
      "s_mov_b32 s52, 256\n\t"
      "s_add_u32 s56, %[hd], 4096\n\t"
      "s_add_u32 m0, s49, 0\n\t"
      "s_nop 0\n\t"
      "buffer_load_dwordx4 v32, s[40:43], s52 offen lds\n\t"
      "s_add_u32 m0, s49, 1024\n\t"
      "s_nop 0\n\t"
      "buffer_load_dwordx4 v33, s[40:43], s52 offen lds\n\t"
      "s_add_u32 m0, s49, 2048\n\t"
      "s_nop 0\n\t"
      "buffer_load_dwordx4 v34, s[40:43], s52 offen lds\n\t"
      "s_add_u32 m0, s49, 3072\n\t"
      "s_nop 0\n\t"
      "buffer_load_dwordx4 v35, s[40:43], s52 offen lds\n\t"
      "s_mov_b32 m0, 153600\n\t"
      "s_nop 0\n\t"
      "buffer_load_dwordx4 v55, s[96:99], 0 offen lds\n\t"
      "v_add_u32 v55, 0x80000, v55\n\t"
      "s_mov_b32 m0, 153600\n\t"
      "s_nop 0\n\t"
      "buffer_load_dwordx4 v55, s[96:99], 0 offen lds\n\t"
      "v_add_u32 v55, 0x80000, v55\n\t"
      "v_mov_b32 v64, 0\n\t"
      "v_mov_b32 v65, 0\n\t"
      "v_mov_b32 v66, 0\n\t"
      "v_mov_b32 v67, 0\n\t"
      "s_nop 1\n\t"
      "v_mfma_f32_32x32x16_bf16 v[240:255], v[64:67], v[64:67], 0\n\t"
      "v_mfma_f32_32x32x16_bf16 v[224:239], v[64:67], v[64:67], 0\n\t"
      "v_mfma_f32_32x32x16_bf16 v[208:223], v[64:67], v[64:67], 0\n\t"
      "v_mfma_f32_32x32x16_bf16 v[192:207], v[64:67], v[64:67], 0\n\t"
      "v_mfma_f32_32x32x16_bf16 v[176:191], v[64:67], v[64:67], 0\n\t"
      "v_mfma_f32_32x32x16_bf16 v[160:175], v[64:67], v[64:67], 0\n\t"
      "v_mfma_f32_32x32x16_bf16 v[144:159], v[64:67], v[64:67], 0\n\t"
      "v_mfma_f32_32x32x16_bf16 v[128:143], v[64:67], v[64:67], 0\n\t"
      "v_mfma_f32_32x32x16_bf16 v[112:127], v[64:67], v[64:67], 0\n\t"
      "v_mfma_f32_32x32x16_bf16 v[96:111], v[64:67], v[64:67], 0\n\t"
      "v_mfma_f32_32x32x16_bf16 v[80:95], v[64:67], v[64:67], 0\n\t"
      "v_mfma_f32_32x32x16_bf16 v[64:79], v[64:67], v[64:67], 0\n\t"
      "v_accvgpr_write_b32 a112, 0\n\t"
      "v_accvgpr_write_b32 a113, 0\n\t"
      "v_accvgpr_write_b32 a114, 0\n\t"
      "v_accvgpr_write_b32 a115, 0\n\t"
      "v_accvgpr_write_b32 a116, 0\n\t"
      "v_accvgpr_write_b32 a117, 0\n\t"
      "v_accvgpr_write_b32 a118, 0\n\t"
      "v_accvgpr_write_b32 a119, 0\n\t"
      "s_mov_b32 s48, 0\n\t"
      "s_mov_b32 s50, 98304\n\t"
      "s_mov_b32 s54, 0\n\t"
      "v_mov_b32 v47, %[xr]\n\t"
      "v_xor_b32 v48, 64, %[xr]\n\t"
      "v_add_u32 v49, 98304, %[wr]\n\t"
      "v_xor_b32 v50, 32, v49\n\t"
      "v_mov_b32 v51, %[xh]\n\t"
      "v_xor_b32 v52, 64, %[xh]\n\t"
      "s_waitcnt vmcnt(22)\n\t"
      "s_barrier\n\t"
      "ds_read_b64_tr_b16 a[32:33], v49 offset:0\n\t"
      "ds_read_b64_tr_b16 a[34:35], v49 offset:512\n\t"
      "ds_read_b64_tr_b16 a[36:37], v50 offset:0\n\t"
      "ds_read_b64_tr_b16 a[38:39], v50 offset:512\n\t"
      "ds_read_b64_tr_b16 a[40:41], v49 offset:8192\n\t"
      "ds_read_b64_tr_b16 a[42:43], v49 offset:8704\n\t"
      "ds_read_b64_tr_b16 a[44:45], v50 offset:8192\n\t"
      "ds_read_b64_tr_b16 a[46:47], v50 offset:8704\n\t"
      "ds_read_b64_tr_b16 a[48:49], v49 offset:16384\n\t"
      "ds_read_b64_tr_b16 a[50:51], v49 offset:16896\n\t"
      "ds_read_b64_tr_b16 a[52:53], v50 offset:16384\n\t"
      "ds_read_b64_tr_b16 a[54:55], v50 offset:16896\n\t"
      "ds_read_b128 a[128:131], v51\n\t"
      "ds_read_b128 a[0:3], v47 offset:0\n\t"
      "ds_read_b128 a[4:7], v47 offset:2048\n\t"
      "ds_read_b128 a[8:11], v47 offset:4096\n\t"
      "ds_read_b128 a[12:15], v47 offset:6144\n\t"
      "ds_read_b128 a[16:19], v47 offset:8192\n\t"
      "ds_read_b128 a[20:23], v47 offset:10240\n\t"
      "ds_read_b128 a[24:27], v47 offset:12288\n\t"
      "ds_read_b128 a[28:31], v47 offset:14336\n\t"
      "s_waitcnt lgkmcnt(0)\n\t"
      "s_cmp_ge_u32 %[wid], 2\n\t"
      "s_cbranch_scc1 G1LOOP2_%=\n\t"
      "G1LOOP1_%=:\n"
      "v_mfma_f32_16x16x32_bf16 a[112:115], a[40:43], a[128:131], a[112:115]\n\t"
      "ds_read_b64_tr_b16 a[88:89], v49 offset:4096\n\t"
      "v_mfma_f32_16x16x32_bf16 a[116:119], a[44:47], a[128:131], a[116:119]\n\t"
      "ds_read_b64_tr_b16 a[90:91], v49 offset:4608\n\t"
      "v_mfma_f32_16x16x32_bf16 v[64:67], a[32:35], a[0:3], v[64:67]\n\t"
      "ds_read_b64_tr_b16 a[92:93], v50 offset:4096\n\t"
      "v_mfma_f32_16x16x32_bf16 v[68:71], a[36:39], a[0:3], v[68:71]\n\t"
      "ds_read_b64_tr_b16 a[94:95], v50 offset:4608\n\t"
      "v_mfma_f32_16x16x32_bf16 v[72:75], a[40:43], a[0:3], v[72:75]\n\t"
      "ds_read_b64_tr_b16 a[96:97], v49 offset:12288\n\t"
      "v_mfma_f32_16x16x32_bf16 v[76:79], a[44:47], a[0:3], v[76:79]\n\t"
      "ds_read_b64_tr_b16 a[98:99], v49 offset:12800\n\t"
      "v_mfma_f32_16x16x32_bf16 v[80:83], a[0:3], a[48:51], v[80:83]\n\t"
      "ds_read_b64_tr_b16 a[100:101], v50 offset:12288\n\t"
      "v_mfma_f32_16x16x32_bf16 v[84:87], a[0:3], a[52:55], v[84:87]\n\t"
      "ds_read_b64_tr_b16 a[102:103], v50 offset:12800\n\t"
      "v_mfma_f32_16x16x32_bf16 v[88:91], a[32:35], a[4:7], v[88:91]\n\t"
      "ds_read_b64_tr_b16 a[104:105], v49 offset:20480\n\t"
      "v_mfma_f32_16x16x32_bf16 v[92:95], a[36:39], a[4:7], v[92:95]\n\t"
      "ds_read_b64_tr_b16 a[106:107], v49 offset:20992\n\t"
      "v_mfma_f32_16x16x32_bf16 v[96:99], a[40:43], a[4:7], v[96:99]\n\t"
      "ds_read_b64_tr_b16 a[108:109], v50 offset:20480\n\t"
      "v_mfma_f32_16x16x32_bf16 v[100:103], a[44:47], a[4:7], v[100:103]\n\t"
      "ds_read_b64_tr_b16 a[110:111], v50 offset:20992\n\t"
      "v_mfma_f32_16x16x32_bf16 v[104:107], a[4:7], a[48:51], v[104:107]\n\t"
      "ds_read_b128 a[132:135], v52\n\t"
      "v_mfma_f32_16x16x32_bf16 v[108:111], a[4:7], a[52:55], v[108:111]\n\t"
      "ds_read_b128 a[56:59], v48 offset:0\n\t"
      "v_mfma_f32_16x16x32_bf16 v[112:115], a[32:35], a[8:11], v[112:115]\n\t"
      "ds_read_b128 a[60:63], v48 offset:2048\n\t"
      "v_mfma_f32_16x16x32_bf16 v[116:119], a[36:39], a[8:11], v[116:119]\n\t"
      "ds_read_b128 a[64:67], v48 offset:4096\n\t"
      "v_mfma_f32_16x16x32_bf16 v[120:123], a[40:43], a[8:11], v[120:123]\n\t"
      "ds_read_b128 a[68:71], v48 offset:6144\n\t"
      "v_mfma_f32_16x16x32_bf16 v[124:127], a[44:47], a[8:11], v[124:127]\n\t"
      "ds_read_b128 a[72:75], v48 offset:8192\n\t"
      "v_mfma_f32_16x16x32_bf16 v[128:131], a[8:11], a[48:51], v[128:131]\n\t"
      "ds_read_b128 a[76:79], v48 offset:10240\n\t"
      "v_mfma_f32_16x16x32_bf16 v[132:135], a[8:11], a[52:55], v[132:135]\n\t"
      "ds_read_b128 a[80:83], v48 offset:12288\n\t"
      "v_mfma_f32_16x16x32_bf16 v[136:139], a[32:35], a[12:15], v[136:139]\n\t"
      "ds_read_b128 a[84:87], v48 offset:14336\n\t"
      "v_mfma_f32_16x16x32_bf16 v[140:143], a[36:39], a[12:15], v[140:143]\n\t"
      "v_mfma_f32_16x16x32_bf16 v[144:147], a[40:43], a[12:15], v[144:147]\n\t"
      "s_add_u32 m0, s49, 4096\n\t"
      "v_mfma_f32_16x16x32_bf16 v[148:151], a[44:47], a[12:15], v[148:151]\n\t"
      "buffer_load_dwordx4 v36, s[40:43], s52 offen lds\n\t"
      "v_mfma_f32_16x16x32_bf16 v[152:155], a[12:15], a[48:51], v[152:155]\n\t"
      "s_add_u32 m0, s49, 5120\n\t"
      "v_mfma_f32_16x16x32_bf16 v[156:159], a[12:15], a[52:55], v[156:159]\n\t"
      "buffer_load_dwordx4 v37, s[40:43], s52 offen lds\n\t"
      "v_mfma_f32_16x16x32_bf16 v[160:163], a[32:35], a[16:19], v[160:163]\n\t"
      "s_add_u32 m0, s49, 6144\n\t"
      "v_mfma_f32_16x16x32_bf16 v[164:167], a[36:39], a[16:19], v[164:167]\n\t"
      "buffer_load_dwordx4 v38, s[40:43], s52 offen lds\n\t"
      "v_mfma_f32_16x16x32_bf16 v[168:171], a[40:43], a[16:19], v[168:171]\n\t"
      "s_add_u32 m0, s49, 7168\n\t"
      "v_mfma_f32_16x16x32_bf16 v[172:175], a[44:47], a[16:19], v[172:175]\n\t"
      "buffer_load_dwordx4 v39, s[40:43], s52 offen lds\n\t"
      "v_mfma_f32_16x16x32_bf16 v[176:179], a[16:19], a[48:51], v[176:179]\n\t"
      "s_mov_b32 m0, s56\n\t"
      "v_mfma_f32_16x16x32_bf16 v[180:183], a[16:19], a[52:55], v[180:183]\n\t"
      "buffer_load_dwordx4 v40, s[40:43], s52 offen lds\n\t"
      "v_mfma_f32_16x16x32_bf16 v[184:187], a[32:35], a[20:23], v[184:187]\n\t"
      "s_lshl_b32 s55, %[wid], 13\n\t"
      "v_mfma_f32_16x16x32_bf16 v[188:191], a[36:39], a[20:23], v[188:191]\n\t"
      "s_add_u32 s49, s48, s55\n\t"
      "v_mfma_f32_16x16x32_bf16 v[192:195], a[40:43], a[20:23], v[192:195]\n\t"
      "s_lshr_b32 s55, s48, 4\n\t"
      "s_add_u32 s56, %[hd], s55\n\t"
      "v_mfma_f32_16x16x32_bf16 v[196:199], a[44:47], a[20:23], v[196:199]\n\t"
      "s_add_u32 s48, s48, 32768\n\t"
      "v_mfma_f32_16x16x32_bf16 v[200:203], a[20:23], a[48:51], v[200:203]\n\t"
      "s_cmp_eq_u32 s48, 98304\n\t"
      "s_cselect_b32 s48, 0, s48\n\t"
      "v_mfma_f32_16x16x32_bf16 v[204:207], a[20:23], a[52:55], v[204:207]\n\t"
      "s_lshl_b32 s55, %[wid], 11\n\t"
      "s_add_u32 s51, s50, s55\n\t"
      "v_mfma_f32_16x16x32_bf16 v[208:211], a[32:35], a[24:27], v[208:211]\n\t"
      "s_xor_b32 s50, s50, 24576\n\t"
      "v_mfma_f32_16x16x32_bf16 v[212:215], a[36:39], a[24:27], v[212:215]\n\t"
      "v_add_u32 v47, s48, %[xr]\n\t"
      "v_xor_b32 v48, 64, v47\n\t"
      "v_mfma_f32_16x16x32_bf16 v[216:219], a[40:43], a[24:27], v[216:219]\n\t"
      "v_add_u32 v49, s50, %[wr]\n\t"
      "v_xor_b32 v50, 32, v49\n\t"
      "v_mfma_f32_16x16x32_bf16 v[220:223], a[44:47], a[24:27], v[220:223]\n\t"
      "s_lshr_b32 s57, s48, 4\n\t"
      "v_add_u32 v51, s57, %[xh]\n\t"
      "v_mfma_f32_16x16x32_bf16 v[224:227], a[24:27], a[48:51], v[224:227]\n\t"
      "v_xor_b32 v52, 64, v51\n\t"
      "v_mfma_f32_16x16x32_bf16 v[228:231], a[24:27], a[52:55], v[228:231]\n\t"
      "s_add_u32 s52, s52, 128\n\t"
      "s_add_u32 s53, s53, 393216\n\t"
      "v_mfma_f32_16x16x32_bf16 v[232:235], a[32:35], a[28:31], v[232:235]\n\t"
      "s_cmp_lt_u32 s54, 13\n\t"
      "s_cselect_b32 s42, 0x400000, 0\n\t"
      "v_mfma_f32_16x16x32_bf16 v[236:239], a[36:39], a[28:31], v[236:239]\n\t"
      "s_cmp_lt_u32 s54, 14\n\t"
      "s_cselect_b32 s46, 0x600000, 0\n\t"
      "v_mfma_f32_16x16x32_bf16 v[240:243], a[40:43], a[28:31], v[240:243]\n\t"
      "s_add_u32 s54, s54, 1\n\t"
      "v_mfma_f32_16x16x32_bf16 v[244:247], a[44:47], a[28:31], v[244:247]\n\t"
      "v_mfma_f32_16x16x32_bf16 v[248:251], a[28:31], a[48:51], v[248:251]\n\t"
      "v_mfma_f32_16x16x32_bf16 v[252:255], a[28:31], a[52:55], v[252:255]\n\t"
      "s_waitcnt vmcnt(11) lgkmcnt(0)\n\t"
      "s_barrier\n\t"
      "v_mfma_f32_16x16x32_bf16 a[112:115], a[96:99], a[132:135], a[112:115]\n\t"
      "s_add_u32 m0, s51, 0\n\t"
      "v_mfma_f32_16x16x32_bf16 a[116:119], a[100:103], a[132:135], a[116:119]\n\t"
      "buffer_load_dwordx4 v41, s[44:47], s53 offen lds\n\t"
      "v_mfma_f32_16x16x32_bf16 v[64:67], a[88:91], a[56:59], v[64:67]\n\t"
      "ds_read_b64_tr_b16 a[32:33], v49 offset:0\n\t"
      "v_mfma_f32_16x16x32_bf16 v[68:71], a[92:95], a[56:59], v[68:71]\n\t"
      "s_add_u32 m0, s51, 1024\n\t"
      "v_mfma_f32_16x16x32_bf16 v[72:75], a[96:99], a[56:59], v[72:75]\n\t"
      "buffer_load_dwordx4 v42, s[44:47], s53 offen lds\n\t"
      "v_mfma_f32_16x16x32_bf16 v[76:79], a[100:103], a[56:59], v[76:79]\n\t"
      "ds_read_b64_tr_b16 a[34:35], v49 offset:512\n\t"
      "v_mfma_f32_16x16x32_bf16 v[80:83], a[56:59], a[104:107], v[80:83]\n\t"
      "s_add_u32 m0, s51, 8192\n\t"
      "v_mfma_f32_16x16x32_bf16 v[84:87], a[56:59], a[108:111], v[84:87]\n\t"
      "buffer_load_dwordx4 v43, s[44:47], s53 offen lds\n\t"
      "v_mfma_f32_16x16x32_bf16 v[88:91], a[88:91], a[60:63], v[88:91]\n\t"
      "ds_read_b64_tr_b16 a[36:37], v50 offset:0\n\t"
      "v_mfma_f32_16x16x32_bf16 v[92:95], a[92:95], a[60:63], v[92:95]\n\t"
      "s_add_u32 m0, s51, 9216\n\t"
      "v_mfma_f32_16x16x32_bf16 v[96:99], a[96:99], a[60:63], v[96:99]\n\t"
      "buffer_load_dwordx4 v44, s[44:47], s53 offen lds\n\t"
      "v_mfma_f32_16x16x32_bf16 v[100:103], a[100:103], a[60:63], v[100:103]\n\t"
      "ds_read_b64_tr_b16 a[38:39], v50 offset:512\n\t"
      "v_mfma_f32_16x16x32_bf16 v[104:107], a[60:63], a[104:107], v[104:107]\n\t"
      "s_add_u32 m0, s51, 16384\n\t"
      "v_mfma_f32_16x16x32_bf16 v[108:111], a[60:63], a[108:111], v[108:111]\n\t"
      "buffer_load_dwordx4 v45, s[44:47], s53 offen lds\n\t"
      "v_mfma_f32_16x16x32_bf16 v[112:115], a[88:91], a[64:67], v[112:115]\n\t"
      "ds_read_b64_tr_b16 a[40:41], v49 offset:8192\n\t"
      "v_mfma_f32_16x16x32_bf16 v[116:119], a[92:95], a[64:67], v[116:119]\n\t"
      "s_add_u32 m0, s51, 17408\n\t"
      "v_mfma_f32_16x16x32_bf16 v[120:123], a[96:99], a[64:67], v[120:123]\n\t"
      "buffer_load_dwordx4 v46, s[44:47], s53 offen lds\n\t"
      "v_mfma_f32_16x16x32_bf16 v[124:127], a[100:103], a[64:67], v[124:127]\n\t"
      "ds_read_b64_tr_b16 a[42:43], v49 offset:8704\n\t"
      "v_mfma_f32_16x16x32_bf16 v[128:131], a[64:67], a[104:107], v[128:131]\n\t"
      "s_mov_b32 m0, 153600\n\t"
      "v_mfma_f32_16x16x32_bf16 v[132:135], a[64:67], a[108:111], v[132:135]\n\t"
      "buffer_load_dwordx4 v55, s[96:99], 0 offen lds\n\t"
      "v_add_u32 v55, 0x80000, v55\n\t"
      "v_mfma_f32_16x16x32_bf16 v[136:139], a[88:91], a[68:71], v[136:139]\n\t"
      "global_load_dwordx4 v[56:59], v54, s[62:63]\n\t"
      "v_mfma_f32_16x16x32_bf16 v[140:143], a[92:95], a[68:71], v[140:143]\n\t"
      "ds_read_b64_tr_b16 a[44:45], v50 offset:8192\n\t"
      "v_mfma_f32_16x16x32_bf16 v[144:147], a[96:99], a[68:71], v[144:147]\n\t"
      "ds_read_b64_tr_b16 a[46:47], v50 offset:8704\n\t"
      "v_mfma_f32_16x16x32_bf16 v[148:151], a[100:103], a[68:71], v[148:151]\n\t"
      "ds_read_b64_tr_b16 a[48:49], v49 offset:16384\n\t"
      "v_mfma_f32_16x16x32_bf16 v[152:155], a[68:71], a[104:107], v[152:155]\n\t"
      "ds_read_b64_tr_b16 a[50:51], v49 offset:16896\n\t"
      "v_mfma_f32_16x16x32_bf16 v[156:159], a[68:71], a[108:111], v[156:159]\n\t"
      "ds_read_b64_tr_b16 a[52:53], v50 offset:16384\n\t"
      "v_mfma_f32_16x16x32_bf16 v[160:163], a[88:91], a[72:75], v[160:163]\n\t"
      "ds_read_b64_tr_b16 a[54:55], v50 offset:16896\n\t"
      "v_mfma_f32_16x16x32_bf16 v[164:167], a[92:95], a[72:75], v[164:167]\n\t"
      "ds_read_b128 a[128:131], v51\n\t"
      "v_mfma_f32_16x16x32_bf16 v[168:171], a[96:99], a[72:75], v[168:171]\n\t"
      "ds_read_b128 a[0:3], v47 offset:0\n\t"
      "v_mfma_f32_16x16x32_bf16 v[172:175], a[100:103], a[72:75], v[172:175]\n\t"
      "ds_read_b128 a[4:7], v47 offset:2048\n\t"
      "v_mfma_f32_16x16x32_bf16 v[176:179], a[72:75], a[104:107], v[176:179]\n\t"
      "ds_read_b128 a[8:11], v47 offset:4096\n\t"
      "v_mfma_f32_16x16x32_bf16 v[180:183], a[72:75], a[108:111], v[180:183]\n\t"
      "ds_read_b128 a[12:15], v47 offset:6144\n\t"
      "v_mfma_f32_16x16x32_bf16 v[184:187], a[88:91], a[76:79], v[184:187]\n\t"
      "ds_read_b128 a[16:19], v47 offset:8192\n\t"
      "v_mfma_f32_16x16x32_bf16 v[188:191], a[92:95], a[76:79], v[188:191]\n\t"
      "ds_read_b128 a[20:23], v47 offset:10240\n\t"
      "v_mfma_f32_16x16x32_bf16 v[192:195], a[96:99], a[76:79], v[192:195]\n\t"
      "ds_read_b128 a[24:27], v47 offset:12288\n\t"
      "v_mfma_f32_16x16x32_bf16 v[196:199], a[100:103], a[76:79], v[196:199]\n\t"
      "ds_read_b128 a[28:31], v47 offset:14336\n\t"
      "v_mfma_f32_16x16x32_bf16 v[200:203], a[76:79], a[104:107], v[200:203]\n\t"
      "s_add_u32 m0, s49, 0\n\t"
      "v_mfma_f32_16x16x32_bf16 v[204:207], a[76:79], a[108:111], v[204:207]\n\t"
      "buffer_load_dwordx4 v32, s[40:43], s52 offen lds\n\t"
      "v_mfma_f32_16x16x32_bf16 v[208:211], a[88:91], a[80:83], v[208:211]\n\t"
      "s_add_u32 m0, s49, 1024\n\t"
      "v_mfma_f32_16x16x32_bf16 v[212:215], a[92:95], a[80:83], v[212:215]\n\t"
      "buffer_load_dwordx4 v33, s[40:43], s52 offen lds\n\t"
      "v_mfma_f32_16x16x32_bf16 v[216:219], a[96:99], a[80:83], v[216:219]\n\t"
      "s_add_u32 m0, s49, 2048\n\t"
      "v_mfma_f32_16x16x32_bf16 v[220:223], a[100:103], a[80:83], v[220:223]\n\t"
      "buffer_load_dwordx4 v34, s[40:43], s52 offen lds\n\t"
      "v_mfma_f32_16x16x32_bf16 v[224:227], a[80:83], a[104:107], v[224:227]\n\t"
      "s_add_u32 m0, s49, 3072\n\t"
      "v_mfma_f32_16x16x32_bf16 v[228:231], a[80:83], a[108:111], v[228:231]\n\t"
      "buffer_load_dwordx4 v35, s[40:43], s52 offen lds\n\t"
      "v_mfma_f32_16x16x32_bf16 v[232:235], a[88:91], a[84:87], v[232:235]\n\t"
      "v_mfma_f32_16x16x32_bf16 v[236:239], a[92:95], a[84:87], v[236:239]\n\t"
      "v_mfma_f32_16x16x32_bf16 v[240:243], a[96:99], a[84:87], v[240:243]\n\t"
      "v_mfma_f32_16x16x32_bf16 v[244:247], a[100:103], a[84:87], v[244:247]\n\t"
      "v_mfma_f32_16x16x32_bf16 v[248:251], a[84:87], a[104:107], v[248:251]\n\t"
      "s_cmp_lt_u32 s54, 16\n\t"
      "v_mfma_f32_16x16x32_bf16 v[252:255], a[84:87], a[108:111], v[252:255]\n\t"
      "s_waitcnt lgkmcnt(0)\n\t"
      "s_cbranch_scc1 G1LOOP1_%=\n\t"
      "s_and_b32 s55, %[wid], 1\n\t"
      "s_lshl_b32 s55, s55, 12\n\t"
      "s_add_u32 s55, s55, 155648\n\t"
      "v_and_b32 v41, 1023, %[vg]\n\t"
      "v_add_u32 v41, s55, v41\n\t"
      "v_accvgpr_read_b32 v44, a112\n\t"
      "v_accvgpr_read_b32 v45, a113\n\t"
      "v_accvgpr_read_b32 v46, a114\n\t"
      "v_accvgpr_read_b32 v47, a115\n\t"
      "v_accvgpr_read_b32 v48, a116\n\t"
      "v_accvgpr_read_b32 v49, a117\n\t"
      "v_accvgpr_read_b32 v50, a118\n\t"
      "v_accvgpr_read_b32 v51, a119\n\t"
      "v_cvt_pk_bf16_f32 v32, v44, v45\n\t"
      "v_cvt_pk_bf16_f32 v33, v46, v47\n\t"
      "v_cvt_pk_bf16_f32 v34, v48, v49\n\t"
      "v_cvt_pk_bf16_f32 v35, v50, v51\n\t"
      "v_cvt_pk_bf16_f32 v44, v240, v241\n\t"
      "v_cvt_pk_bf16_f32 v45, v242, v243\n\t"
      "v_cvt_pk_bf16_f32 v46, v244, v245\n\t"
      "v_cvt_pk_bf16_f32 v47, v246, v247\n\t"
      "v_cvt_pk_bf16_f32 v48, v248, v249\n\t"
      "v_cvt_pk_bf16_f32 v49, v250, v251\n\t"
      "v_cvt_pk_bf16_f32 v50, v252, v253\n\t"
      "v_cvt_pk_bf16_f32 v51, v254, v255\n\t"
      "ds_write_b128 v41, v[44:47]\n\t"
      "ds_write_b128 v41, v[48:51] offset:1024\n\t"
      "s_waitcnt vmcnt(0) lgkmcnt(0)\n\t"
      "s_barrier\n\t"
      "ds_read_b128 v[36:39], v41 offset:2048\n\t"
      "s_branch G1JOIN_%=\n\t"
      "G1LOOP2_%=:\n"
      "v_mfma_f32_16x16x32_bf16 a[112:115], a[128:131], a[48:51], a[112:115]\n\t"
      "ds_read_b64_tr_b16 a[88:89], v49 offset:4096\n\t"
      "v_mfma_f32_16x16x32_bf16 a[116:119], a[128:131], a[52:55], a[116:119]\n\t"
      "ds_read_b64_tr_b16 a[90:91], v49 offset:4608\n\t"
      "v_mfma_f32_16x16x32_bf16 v[64:67], a[32:35], a[0:3], v[64:67]\n\t"
      "ds_read_b64_tr_b16 a[92:93], v50 offset:4096\n\t"
      "v_mfma_f32_16x16x32_bf16 v[68:71], a[36:39], a[0:3], v[68:71]\n\t"
      "ds_read_b64_tr_b16 a[94:95], v50 offset:4608\n\t"
      "v_mfma_f32_16x16x32_bf16 v[72:75], a[40:43], a[0:3], v[72:75]\n\t"
      "ds_read_b64_tr_b16 a[96:97], v49 offset:12288\n\t"
      "v_mfma_f32_16x16x32_bf16 v[76:79], a[44:47], a[0:3], v[76:79]\n\t"
      "ds_read_b64_tr_b16 a[98:99], v49 offset:12800\n\t"
      "v_mfma_f32_16x16x32_bf16 v[80:83], a[0:3], a[48:51], v[80:83]\n\t"
      "ds_read_b64_tr_b16 a[100:101], v50 offset:12288\n\t"
      "v_mfma_f32_16x16x32_bf16 v[84:87], a[0:3], a[52:55], v[84:87]\n\t"
      "ds_read_b64_tr_b16 a[102:103], v50 offset:12800\n\t"
      "v_mfma_f32_16x16x32_bf16 v[88:91], a[32:35], a[4:7], v[88:91]\n\t"
      "ds_read_b64_tr_b16 a[104:105], v49 offset:20480\n\t"
      "v_mfma_f32_16x16x32_bf16 v[92:95], a[36:39], a[4:7], v[92:95]\n\t"
      "ds_read_b64_tr_b16 a[106:107], v49 offset:20992\n\t"
      "v_mfma_f32_16x16x32_bf16 v[96:99], a[40:43], a[4:7], v[96:99]\n\t"
      "ds_read_b64_tr_b16 a[108:109], v50 offset:20480\n\t"
      "v_mfma_f32_16x16x32_bf16 v[100:103], a[44:47], a[4:7], v[100:103]\n\t"
      "ds_read_b64_tr_b16 a[110:111], v50 offset:20992\n\t"
      "v_mfma_f32_16x16x32_bf16 v[104:107], a[4:7], a[48:51], v[104:107]\n\t"
      "ds_read_b128 a[132:135], v52\n\t"
      "v_mfma_f32_16x16x32_bf16 v[108:111], a[4:7], a[52:55], v[108:111]\n\t"
      "ds_read_b128 a[56:59], v48 offset:0\n\t"
      "v_mfma_f32_16x16x32_bf16 v[112:115], a[32:35], a[8:11], v[112:115]\n\t"
      "ds_read_b128 a[60:63], v48 offset:2048\n\t"
      "v_mfma_f32_16x16x32_bf16 v[116:119], a[36:39], a[8:11], v[116:119]\n\t"
      "ds_read_b128 a[64:67], v48 offset:4096\n\t"
      "v_mfma_f32_16x16x32_bf16 v[120:123], a[40:43], a[8:11], v[120:123]\n\t"
      "ds_read_b128 a[68:71], v48 offset:6144\n\t"
      "v_mfma_f32_16x16x32_bf16 v[124:127], a[44:47], a[8:11], v[124:127]\n\t"
      "ds_read_b128 a[72:75], v48 offset:8192\n\t"
      "v_mfma_f32_16x16x32_bf16 v[128:131], a[8:11], a[48:51], v[128:131]\n\t"
      "ds_read_b128 a[76:79], v48 offset:10240\n\t"
      "v_mfma_f32_16x16x32_bf16 v[132:135], a[8:11], a[52:55], v[132:135]\n\t"
      "ds_read_b128 a[80:83], v48 offset:12288\n\t"
      "v_mfma_f32_16x16x32_bf16 v[136:139], a[32:35], a[12:15], v[136:139]\n\t"
      "ds_read_b128 a[84:87], v48 offset:14336\n\t"
      "v_mfma_f32_16x16x32_bf16 v[140:143], a[36:39], a[12:15], v[140:143]\n\t"
      "v_mfma_f32_16x16x32_bf16 v[144:147], a[40:43], a[12:15], v[144:147]\n\t"
      "s_add_u32 m0, s49, 4096\n\t"
      "v_mfma_f32_16x16x32_bf16 v[148:151], a[44:47], a[12:15], v[148:151]\n\t"
      "buffer_load_dwordx4 v36, s[40:43], s52 offen lds\n\t"
      "v_mfma_f32_16x16x32_bf16 v[152:155], a[12:15], a[48:51], v[152:155]\n\t"
      "s_add_u32 m0, s49, 5120\n\t"
      "v_mfma_f32_16x16x32_bf16 v[156:159], a[12:15], a[52:55], v[156:159]\n\t"
      "buffer_load_dwordx4 v37, s[40:43], s52 offen lds\n\t"
      "v_mfma_f32_16x16x32_bf16 v[160:163], a[32:35], a[16:19], v[160:163]\n\t"
      "s_add_u32 m0, s49, 6144\n\t"
      "v_mfma_f32_16x16x32_bf16 v[164:167], a[36:39], a[16:19], v[164:167]\n\t"
      "buffer_load_dwordx4 v38, s[40:43], s52 offen lds\n\t"
      "v_mfma_f32_16x16x32_bf16 v[168:171], a[40:43], a[16:19], v[168:171]\n\t"
      "s_add_u32 m0, s49, 7168\n\t"
      "v_mfma_f32_16x16x32_bf16 v[172:175], a[44:47], a[16:19], v[172:175]\n\t"
      "buffer_load_dwordx4 v39, s[40:43], s52 offen lds\n\t"
      "v_mfma_f32_16x16x32_bf16 v[176:179], a[16:19], a[48:51], v[176:179]\n\t"
      "s_mov_b32 m0, s56\n\t"
      "v_mfma_f32_16x16x32_bf16 v[180:183], a[16:19], a[52:55], v[180:183]\n\t"
      "buffer_load_dwordx4 v40, s[40:43], s52 offen lds\n\t"
      "v_mfma_f32_16x16x32_bf16 v[184:187], a[32:35], a[20:23], v[184:187]\n\t"
      "s_lshl_b32 s55, %[wid], 13\n\t"
      "v_mfma_f32_16x16x32_bf16 v[188:191], a[36:39], a[20:23], v[188:191]\n\t"
      "s_add_u32 s49, s48, s55\n\t"
      "v_mfma_f32_16x16x32_bf16 v[192:195], a[40:43], a[20:23], v[192:195]\n\t"
      "s_lshr_b32 s55, s48, 4\n\t"
      "s_add_u32 s56, %[hd], s55\n\t"
      "v_mfma_f32_16x16x32_bf16 v[196:199], a[44:47], a[20:23], v[196:199]\n\t"
      "s_add_u32 s48, s48, 32768\n\t"
      "v_mfma_f32_16x16x32_bf16 v[200:203], a[20:23], a[48:51], v[200:203]\n\t"
      "s_cmp_eq_u32 s48, 98304\n\t"
      "s_cselect_b32 s48, 0, s48\n\t"
      "v_mfma_f32_16x16x32_bf16 v[204:207], a[20:23], a[52:55], v[204:207]\n\t"
      "s_lshl_b32 s55, %[wid], 11\n\t"
      "s_add_u32 s51, s50, s55\n\t"
      "v_mfma_f32_16x16x32_bf16 v[208:211], a[32:35], a[24:27], v[208:211]\n\t"
      "s_xor_b32 s50, s50, 24576\n\t"
      "v_mfma_f32_16x16x32_bf16 v[212:215], a[36:39], a[24:27], v[212:215]\n\t"
      "v_add_u32 v47, s48, %[xr]\n\t"
      "v_xor_b32 v48, 64, v47\n\t"
      "v_mfma_f32_16x16x32_bf16 v[216:219], a[40:43], a[24:27], v[216:219]\n\t"
      "v_add_u32 v49, s50, %[wr]\n\t"
      "v_xor_b32 v50, 32, v49\n\t"
      "v_mfma_f32_16x16x32_bf16 v[220:223], a[44:47], a[24:27], v[220:223]\n\t"
      "s_lshr_b32 s57, s48, 4\n\t"
      "v_add_u32 v51, s57, %[xh]\n\t"
      "v_mfma_f32_16x16x32_bf16 v[224:227], a[24:27], a[48:51], v[224:227]\n\t"
      "v_xor_b32 v52, 64, v51\n\t"
      "v_mfma_f32_16x16x32_bf16 v[228:231], a[24:27], a[52:55], v[228:231]\n\t"
      "s_add_u32 s52, s52, 128\n\t"
      "s_add_u32 s53, s53, 393216\n\t"
      "v_mfma_f32_16x16x32_bf16 v[232:235], a[32:35], a[28:31], v[232:235]\n\t"
      "s_cmp_lt_u32 s54, 13\n\t"
      "s_cselect_b32 s42, 0x400000, 0\n\t"
      "v_mfma_f32_16x16x32_bf16 v[236:239], a[36:39], a[28:31], v[236:239]\n\t"
      "s_cmp_lt_u32 s54, 14\n\t"
      "s_cselect_b32 s46, 0x600000, 0\n\t"
      "v_mfma_f32_16x16x32_bf16 v[240:243], a[40:43], a[28:31], v[240:243]\n\t"
      "s_add_u32 s54, s54, 1\n\t"
      "v_mfma_f32_16x16x32_bf16 v[244:247], a[44:47], a[28:31], v[244:247]\n\t"
      "v_mfma_f32_16x16x32_bf16 v[248:251], a[28:31], a[48:51], v[248:251]\n\t"
      "v_mfma_f32_16x16x32_bf16 v[252:255], a[28:31], a[52:55], v[252:255]\n\t"
      "s_waitcnt vmcnt(11) lgkmcnt(0)\n\t"
      "s_barrier\n\t"
      "v_mfma_f32_16x16x32_bf16 a[112:115], a[132:135], a[104:107], a[112:115]\n\t"
      "s_add_u32 m0, s51, 0\n\t"
      "v_mfma_f32_16x16x32_bf16 a[116:119], a[132:135], a[108:111], a[116:119]\n\t"
      "buffer_load_dwordx4 v41, s[44:47], s53 offen lds\n\t"
      "v_mfma_f32_16x16x32_bf16 v[64:67], a[88:91], a[56:59], v[64:67]\n\t"
      "ds_read_b64_tr_b16 a[32:33], v49 offset:0\n\t"
      "v_mfma_f32_16x16x32_bf16 v[68:71], a[92:95], a[56:59], v[68:71]\n\t"
      "s_add_u32 m0, s51, 1024\n\t"
      "v_mfma_f32_16x16x32_bf16 v[72:75], a[96:99], a[56:59], v[72:75]\n\t"
      "buffer_load_dwordx4 v42, s[44:47], s53 offen lds\n\t"
      "v_mfma_f32_16x16x32_bf16 v[76:79], a[100:103], a[56:59], v[76:79]\n\t"
      "ds_read_b64_tr_b16 a[34:35], v49 offset:512\n\t"
      "v_mfma_f32_16x16x32_bf16 v[80:83], a[56:59], a[104:107], v[80:83]\n\t"
      "s_add_u32 m0, s51, 8192\n\t"
      "v_mfma_f32_16x16x32_bf16 v[84:87], a[56:59], a[108:111], v[84:87]\n\t"
      "buffer_load_dwordx4 v43, s[44:47], s53 offen lds\n\t"
      "v_mfma_f32_16x16x32_bf16 v[88:91], a[88:91], a[60:63], v[88:91]\n\t"
      "ds_read_b64_tr_b16 a[36:37], v50 offset:0\n\t"
      "v_mfma_f32_16x16x32_bf16 v[92:95], a[92:95], a[60:63], v[92:95]\n\t"
      "s_add_u32 m0, s51, 9216\n\t"
      "v_mfma_f32_16x16x32_bf16 v[96:99], a[96:99], a[60:63], v[96:99]\n\t"
      "buffer_load_dwordx4 v44, s[44:47], s53 offen lds\n\t"
      "v_mfma_f32_16x16x32_bf16 v[100:103], a[100:103], a[60:63], v[100:103]\n\t"
      "ds_read_b64_tr_b16 a[38:39], v50 offset:512\n\t"
      "v_mfma_f32_16x16x32_bf16 v[104:107], a[60:63], a[104:107], v[104:107]\n\t"
      "s_add_u32 m0, s51, 16384\n\t"
      "v_mfma_f32_16x16x32_bf16 v[108:111], a[60:63], a[108:111], v[108:111]\n\t"
      "buffer_load_dwordx4 v45, s[44:47], s53 offen lds\n\t"
      "v_mfma_f32_16x16x32_bf16 v[112:115], a[88:91], a[64:67], v[112:115]\n\t"
      "ds_read_b64_tr_b16 a[40:41], v49 offset:8192\n\t"
      "v_mfma_f32_16x16x32_bf16 v[116:119], a[92:95], a[64:67], v[116:119]\n\t"
      "s_add_u32 m0, s51, 17408\n\t"
      "v_mfma_f32_16x16x32_bf16 v[120:123], a[96:99], a[64:67], v[120:123]\n\t"
      "buffer_load_dwordx4 v46, s[44:47], s53 offen lds\n\t"
      "v_mfma_f32_16x16x32_bf16 v[124:127], a[100:103], a[64:67], v[124:127]\n\t"
      "ds_read_b64_tr_b16 a[42:43], v49 offset:8704\n\t"
      "v_mfma_f32_16x16x32_bf16 v[128:131], a[64:67], a[104:107], v[128:131]\n\t"
      "s_mov_b32 m0, 153600\n\t"
      "v_mfma_f32_16x16x32_bf16 v[132:135], a[64:67], a[108:111], v[132:135]\n\t"
      "buffer_load_dwordx4 v55, s[96:99], 0 offen lds\n\t"
      "v_add_u32 v55, 0x80000, v55\n\t"
      "v_mfma_f32_16x16x32_bf16 v[136:139], a[88:91], a[68:71], v[136:139]\n\t"
      "global_load_dwordx4 v[56:59], v54, s[62:63]\n\t"
      "v_mfma_f32_16x16x32_bf16 v[140:143], a[92:95], a[68:71], v[140:143]\n\t"
      "ds_read_b64_tr_b16 a[44:45], v50 offset:8192\n\t"
      "v_mfma_f32_16x16x32_bf16 v[144:147], a[96:99], a[68:71], v[144:147]\n\t"
      "ds_read_b64_tr_b16 a[46:47], v50 offset:8704\n\t"
      "v_mfma_f32_16x16x32_bf16 v[148:151], a[100:103], a[68:71], v[148:151]\n\t"
      "ds_read_b64_tr_b16 a[48:49], v49 offset:16384\n\t"
      "v_mfma_f32_16x16x32_bf16 v[152:155], a[68:71], a[104:107], v[152:155]\n\t"
      "ds_read_b64_tr_b16 a[50:51], v49 offset:16896\n\t"
      "v_mfma_f32_16x16x32_bf16 v[156:159], a[68:71], a[108:111], v[156:159]\n\t"
      "ds_read_b64_tr_b16 a[52:53], v50 offset:16384\n\t"
      "v_mfma_f32_16x16x32_bf16 v[160:163], a[88:91], a[72:75], v[160:163]\n\t"
      "ds_read_b64_tr_b16 a[54:55], v50 offset:16896\n\t"
      "v_mfma_f32_16x16x32_bf16 v[164:167], a[92:95], a[72:75], v[164:167]\n\t"
      "ds_read_b128 a[128:131], v51\n\t"
      "v_mfma_f32_16x16x32_bf16 v[168:171], a[96:99], a[72:75], v[168:171]\n\t"
      "ds_read_b128 a[0:3], v47 offset:0\n\t"
      "v_mfma_f32_16x16x32_bf16 v[172:175], a[100:103], a[72:75], v[172:175]\n\t"
      "ds_read_b128 a[4:7], v47 offset:2048\n\t"
      "v_mfma_f32_16x16x32_bf16 v[176:179], a[72:75], a[104:107], v[176:179]\n\t"
      "ds_read_b128 a[8:11], v47 offset:4096\n\t"
      "v_mfma_f32_16x16x32_bf16 v[180:183], a[72:75], a[108:111], v[180:183]\n\t"
      "ds_read_b128 a[12:15], v47 offset:6144\n\t"
      "v_mfma_f32_16x16x32_bf16 v[184:187], a[88:91], a[76:79], v[184:187]\n\t"
      "ds_read_b128 a[16:19], v47 offset:8192\n\t"
      "v_mfma_f32_16x16x32_bf16 v[188:191], a[92:95], a[76:79], v[188:191]\n\t"
      "ds_read_b128 a[20:23], v47 offset:10240\n\t"
      "v_mfma_f32_16x16x32_bf16 v[192:195], a[96:99], a[76:79], v[192:195]\n\t"
      "ds_read_b128 a[24:27], v47 offset:12288\n\t"
      "v_mfma_f32_16x16x32_bf16 v[196:199], a[100:103], a[76:79], v[196:199]\n\t"
      "ds_read_b128 a[28:31], v47 offset:14336\n\t"
      "v_mfma_f32_16x16x32_bf16 v[200:203], a[76:79], a[104:107], v[200:203]\n\t"
      "s_add_u32 m0, s49, 0\n\t"
      "v_mfma_f32_16x16x32_bf16 v[204:207], a[76:79], a[108:111], v[204:207]\n\t"
      "buffer_load_dwordx4 v32, s[40:43], s52 offen lds\n\t"
      "v_mfma_f32_16x16x32_bf16 v[208:211], a[88:91], a[80:83], v[208:211]\n\t"
      "s_add_u32 m0, s49, 1024\n\t"
      "v_mfma_f32_16x16x32_bf16 v[212:215], a[92:95], a[80:83], v[212:215]\n\t"
      "buffer_load_dwordx4 v33, s[40:43], s52 offen lds\n\t"
      "v_mfma_f32_16x16x32_bf16 v[216:219], a[96:99], a[80:83], v[216:219]\n\t"
      "s_add_u32 m0, s49, 2048\n\t"
      "v_mfma_f32_16x16x32_bf16 v[220:223], a[100:103], a[80:83], v[220:223]\n\t"
      "buffer_load_dwordx4 v34, s[40:43], s52 offen lds\n\t"
      "v_mfma_f32_16x16x32_bf16 v[224:227], a[80:83], a[104:107], v[224:227]\n\t"
      "s_add_u32 m0, s49, 3072\n\t"
      "v_mfma_f32_16x16x32_bf16 v[228:231], a[80:83], a[108:111], v[228:231]\n\t"
      "buffer_load_dwordx4 v35, s[40:43], s52 offen lds\n\t"
      "v_mfma_f32_16x16x32_bf16 v[232:235], a[88:91], a[84:87], v[232:235]\n\t"
      "v_mfma_f32_16x16x32_bf16 v[236:239], a[92:95], a[84:87], v[236:239]\n\t"
      "v_mfma_f32_16x16x32_bf16 v[240:243], a[96:99], a[84:87], v[240:243]\n\t"
      "v_mfma_f32_16x16x32_bf16 v[244:247], a[100:103], a[84:87], v[244:247]\n\t"
      "v_mfma_f32_16x16x32_bf16 v[248:251], a[84:87], a[104:107], v[248:251]\n\t"
      "s_cmp_lt_u32 s54, 16\n\t"
      "v_mfma_f32_16x16x32_bf16 v[252:255], a[84:87], a[108:111], v[252:255]\n\t"
      "s_waitcnt lgkmcnt(0)\n\t"
      "s_cbranch_scc1 G1LOOP2_%=\n\t"
      "s_and_b32 s55, %[wid], 1\n\t"
      "s_lshl_b32 s55, s55, 12\n\t"
      "s_add_u32 s55, s55, 155648\n\t"
      "v_and_b32 v41, 1023, %[vg]\n\t"
      "v_add_u32 v41, s55, v41\n\t"
      "v_accvgpr_read_b32 v44, a112\n\t"
      "v_accvgpr_read_b32 v45, a113\n\t"
      "v_accvgpr_read_b32 v46, a114\n\t"
      "v_accvgpr_read_b32 v47, a115\n\t"
      "v_accvgpr_read_b32 v48, a116\n\t"
      "v_accvgpr_read_b32 v49, a117\n\t"
      "v_accvgpr_read_b32 v50, a118\n\t"
      "v_accvgpr_read_b32 v51, a119\n\t"
      "v_cvt_pk_bf16_f32 v36, v44, v45\n\t"
      "v_cvt_pk_bf16_f32 v37, v46, v47\n\t"
      "v_cvt_pk_bf16_f32 v38, v48, v49\n\t"
      "v_cvt_pk_bf16_f32 v39, v50, v51\n\t"
      "ds_write_b128 v41, v[36:39] offset:2048\n\t"
      "s_waitcnt vmcnt(0) lgkmcnt(0)\n\t"
      "s_barrier\n\t"
      "ds_read_b128 v[32:35], v41\n\t"
      "ds_read_b128 v[36:39], v41 offset:1024\n\t"
      "G1JOIN_%=:\n"
      "v_add_u32 %[tag], 1, v53\n\t"
      "; ---- local (dilated window) attention on the accumulators, true row maxima\n\t"
      "s_mov_b64 s[40:41], %[pkv]\n\t"
      "s_mov_b32 s42, 0x40000\n\t"
      "s_mov_b32 s43, 0x20000\n\t"
      "s_mov_b64 s[44:45], %[pwb]\n\t"
      "s_mov_b32 s46, 0x200000\n\t"
      "s_mov_b32 s47, 0x20000\n\t"
      "s_lshl_b32 s54, %[wid], 12\n\t"
      "v_add_u32 v60, 0, %[vw]\n\t"
      "v_add_u32 v61, 15360, %[vw]\n\t"
      "v_add_u32 v62, 30720, %[vw]\n\t"
      "v_add_u32 v63, 46080, %[vw]\n\t"
      "v_bfe_i32 v48, %[msk], 0, 1\n\t"
      "v_and_b32 v48, 0xff800000, v48\n\t"
      "v_bfe_i32 v44, %[msk], 4, 1\n\t"
      "v_and_b32 v44, 0xff800000, v44\n\t"
      "v_bfe_i32 v52, %[msk], 8, 1\n\t"
      "v_and_b32 v52, 0xff800000, v52\n\t"
      "v_bfe_i32 v49, %[msk], 1, 1\n\t"
      "v_and_b32 v49, 0xff800000, v49\n\t"
      "v_bfe_i32 v45, %[msk], 5, 1\n\t"
      "v_and_b32 v45, 0xff800000, v45\n\t"
      "v_bfe_i32 v53, %[msk], 9, 1\n\t"
      "v_and_b32 v53, 0xff800000, v53\n\t"
      "v_bfe_i32 v50, %[msk], 2, 1\n\t"
      "v_and_b32 v50, 0xff800000, v50\n\t"
      "v_bfe_i32 v46, %[msk], 6, 1\n\t"
      "v_and_b32 v46, 0xff800000, v46\n\t"
      "v_bfe_i32 v54, %[msk], 10, 1\n\t"
      "v_and_b32 v54, 0xff800000, v54\n\t"
      "v_bfe_i32 v51, %[msk], 3, 1\n\t"
      "v_and_b32 v51, 0xff800000, v51\n\t"
      "v_bfe_i32 v47, %[msk], 7, 1\n\t"
      "v_and_b32 v47, 0xff800000, v47\n\t"
      "v_bfe_i32 v55, %[msk], 11, 1\n\t"
      "v_and_b32 v55, 0xff800000, v55\n\t"
      "v_cvt_pk_bf16_f32 v64, v64, v65\n\t"
      "v_cvt_pk_bf16_f32 v65, v66, v67\n\t"
      "v_cvt_pk_bf16_f32 v66, v68, v69\n\t"
      "v_cvt_pk_bf16_f32 v67, v70, v71\n\t"
      "v_cvt_pk_bf16_f32 v72, v72, v73\n\t"
      "v_cvt_pk_bf16_f32 v73, v74, v75\n\t"
      "v_cvt_pk_bf16_f32 v74, v76, v77\n\t"
      "v_cvt_pk_bf16_f32 v75, v78, v79\n\t"
      "v_cvt_pk_bf16_f32 v88, v88, v89\n\t"
      "v_cvt_pk_bf16_f32 v89, v90, v91\n\t"
      "v_cvt_pk_bf16_f32 v90, v92, v93\n\t"
      "v_cvt_pk_bf16_f32 v91, v94, v95\n\t"
      "v_cvt_pk_bf16_f32 v96, v96, v97\n\t"
      "v_cvt_pk_bf16_f32 v97, v98, v99\n\t"
      "v_cvt_pk_bf16_f32 v98, v100, v101\n\t"
      "v_cvt_pk_bf16_f32 v99, v102, v103\n\t"
      "v_cvt_pk_bf16_f32 v112, v112, v113\n\t"
      "v_cvt_pk_bf16_f32 v113, v114, v115\n\t"
      "v_cvt_pk_bf16_f32 v114, v116, v117\n\t"
      "v_cvt_pk_bf16_f32 v115, v118, v119\n\t"
      "v_cvt_pk_bf16_f32 v120, v120, v121\n\t"
      "v_cvt_pk_bf16_f32 v121, v122, v123\n\t"
      "v_cvt_pk_bf16_f32 v122, v124, v125\n\t"
      "v_cvt_pk_bf16_f32 v123, v126, v127\n\t"
      "v_cvt_pk_bf16_f32 v136, v136, v137\n\t"
      "v_cvt_pk_bf16_f32 v137, v138, v139\n\t"
      "v_cvt_pk_bf16_f32 v138, v140, v141\n\t"
      "v_cvt_pk_bf16_f32 v139, v142, v143\n\t"
      "v_cvt_pk_bf16_f32 v144, v144, v145\n\t"
      "v_cvt_pk_bf16_f32 v145, v146, v147\n\t"
      "v_cvt_pk_bf16_f32 v146, v148, v149\n\t"
      "v_cvt_pk_bf16_f32 v147, v150, v151\n\t"
      "v_cvt_pk_bf16_f32 v160, v160, v161\n\t"
      "v_cvt_pk_bf16_f32 v161, v162, v163\n\t"
      "v_cvt_pk_bf16_f32 v162, v164, v165\n\t"
      "v_cvt_pk_bf16_f32 v163, v166, v167\n\t"
      "v_cvt_pk_bf16_f32 v168, v168, v169\n\t"
      "v_cvt_pk_bf16_f32 v169, v170, v171\n\t"
      "v_cvt_pk_bf16_f32 v170, v172, v173\n\t"
      "v_cvt_pk_bf16_f32 v171, v174, v175\n\t"
      "v_cvt_pk_bf16_f32 v184, v184, v185\n\t"
      "v_cvt_pk_bf16_f32 v185, v186, v187\n\t"
      "v_cvt_pk_bf16_f32 v186, v188, v189\n\t"
      "v_cvt_pk_bf16_f32 v187, v190, v191\n\t"
      "v_cvt_pk_bf16_f32 v192, v192, v193\n\t"
      "v_cvt_pk_bf16_f32 v193, v194, v195\n\t"
      "v_cvt_pk_bf16_f32 v194, v196, v197\n\t"
      "v_cvt_pk_bf16_f32 v195, v198, v199\n\t"
      "v_cvt_pk_bf16_f32 v208, v208, v209\n\t"
      "v_cvt_pk_bf16_f32 v209, v210, v211\n\t"
      "v_cvt_pk_bf16_f32 v210, v212, v213\n\t"
      "v_cvt_pk_bf16_f32 v211, v214, v215\n\t"
      "v_cvt_pk_bf16_f32 v216, v216, v217\n\t"
      "v_cvt_pk_bf16_f32 v217, v218, v219\n\t"
      "v_cvt_pk_bf16_f32 v218, v220, v221\n\t"
      "v_cvt_pk_bf16_f32 v219, v222, v223\n\t"
      "v_cvt_pk_bf16_f32 v232, v232, v233\n\t"
      "v_cvt_pk_bf16_f32 v233, v234, v235\n\t"
      "v_cvt_pk_bf16_f32 v234, v236, v237\n\t"
      "v_cvt_pk_bf16_f32 v235, v238, v239\n\t"
      "v_cvt_pk_bf16_f32 v240, v240, v241\n\t"
      "v_cvt_pk_bf16_f32 v241, v242, v243\n\t"
      "v_cvt_pk_bf16_f32 v242, v244, v245\n\t"
      "v_cvt_pk_bf16_f32 v243, v246, v247\n\t"
      "s_waitcnt lgkmcnt(0)\n\t"
      "v_mfma_f32_16x16x32_bf16 v[68:71], v[32:35], v[64:67], v[52:55]\n\t"
      "v_cvt_pk_bf16_f32 v251, v250, v251\n\t"
      "v_cvt_pk_bf16_f32 v250, v248, v249\n\t"
      "v_cvt_pk_bf16_f32 v248, v224, v225\n\t"
      "v_cvt_pk_bf16_f32 v249, v226, v227\n\t"
      "v_mfma_f32_16x16x32_bf16 v[76:79], v[72:75], v[64:67], v[48:51]\n\t"
      "v_cvt_pk_bf16_f32 v255, v254, v255\n\t"
      "v_cvt_pk_bf16_f32 v254, v252, v253\n\t"
      "v_cvt_pk_bf16_f32 v252, v228, v229\n\t"
      "v_cvt_pk_bf16_f32 v253, v230, v231\n\t"
      "v_mfma_f32_16x16x32_bf16 v[92:95], v[72:75], v[88:91], v[44:47]\n\t"
      "v_cvt_pk_bf16_f32 v227, v226, v227\n\t"
      "v_cvt_pk_bf16_f32 v226, v224, v225\n\t"
      "v_cvt_pk_bf16_f32 v224, v200, v201\n\t"
      "v_cvt_pk_bf16_f32 v225, v202, v203\n\t"
      "v_mfma_f32_16x16x32_bf16 v[100:103], v[96:99], v[88:91], v[48:51]\n\t"
      "v_cvt_pk_bf16_f32 v231, v230, v231\n\t"
      "v_cvt_pk_bf16_f32 v230, v228, v229\n\t"
      "v_cvt_pk_bf16_f32 v228, v204, v205\n\t"
      "v_cvt_pk_bf16_f32 v229, v206, v207\n\t"
      "v_mfma_f32_16x16x32_bf16 v[116:119], v[96:99], v[112:115], v[44:47]\n\t"
      "v_cvt_pk_bf16_f32 v203, v202, v203\n\t"
      "v_cvt_pk_bf16_f32 v202, v200, v201\n\t"
      "v_cvt_pk_bf16_f32 v200, v176, v177\n\t"
      "v_cvt_pk_bf16_f32 v201, v178, v179\n\t"
      "v_mfma_f32_16x16x32_bf16 v[124:127], v[120:123], v[112:115], v[48:51]\n\t"
      "v_cvt_pk_bf16_f32 v207, v206, v207\n\t"
      "v_cvt_pk_bf16_f32 v206, v204, v205\n\t"
      "v_cvt_pk_bf16_f32 v204, v180, v181\n\t"
      "v_cvt_pk_bf16_f32 v205, v182, v183\n\t"
      "v_mfma_f32_16x16x32_bf16 v[140:143], v[120:123], v[136:139], v[44:47]\n\t"
      "v_cvt_pk_bf16_f32 v179, v178, v179\n\t"
      "v_cvt_pk_bf16_f32 v178, v176, v177\n\t"
      "v_cvt_pk_bf16_f32 v176, v152, v153\n\t"
      "v_cvt_pk_bf16_f32 v177, v154, v155\n\t"
      "v_mfma_f32_16x16x32_bf16 v[148:151], v[144:147], v[136:139], v[48:51]\n\t"
      "v_cvt_pk_bf16_f32 v183, v182, v183\n\t"
      "v_cvt_pk_bf16_f32 v182, v180, v181\n\t"
      "v_cvt_pk_bf16_f32 v180, v156, v157\n\t"
      "v_cvt_pk_bf16_f32 v181, v158, v159\n\t"
      "v_mfma_f32_16x16x32_bf16 v[164:167], v[144:147], v[160:163], v[44:47]\n\t"
      "v_cvt_pk_bf16_f32 v155, v154, v155\n\t"
      "v_cvt_pk_bf16_f32 v154, v152, v153\n\t"
      "v_cvt_pk_bf16_f32 v152, v128, v129\n\t"
      "v_cvt_pk_bf16_f32 v153, v130, v131\n\t"
      "v_mfma_f32_16x16x32_bf16 v[172:175], v[168:171], v[160:163], v[48:51]\n\t"
      "v_cvt_pk_bf16_f32 v159, v158, v159\n\t"
      "v_cvt_pk_bf16_f32 v158, v156, v157\n\t"
      "v_cvt_pk_bf16_f32 v156, v132, v133\n\t"
      "v_cvt_pk_bf16_f32 v157, v134, v135\n\t"
      "v_mfma_f32_16x16x32_bf16 v[188:191], v[168:171], v[184:187], v[44:47]\n\t"
      "v_cvt_pk_bf16_f32 v131, v130, v131\n\t"
      "v_cvt_pk_bf16_f32 v130, v128, v129\n\t"
      "v_cvt_pk_bf16_f32 v128, v104, v105\n\t"
      "v_cvt_pk_bf16_f32 v129, v106, v107\n\t"
      "v_mfma_f32_16x16x32_bf16 v[196:199], v[192:195], v[184:187], v[48:51]\n\t"
      "v_cvt_pk_bf16_f32 v135, v134, v135\n\t"
      "v_cvt_pk_bf16_f32 v134, v132, v133\n\t"
      "v_cvt_pk_bf16_f32 v132, v108, v109\n\t"
      "v_cvt_pk_bf16_f32 v133, v110, v111\n\t"
      "v_mfma_f32_16x16x32_bf16 v[212:215], v[192:195], v[208:211], v[44:47]\n\t"
      "v_cvt_pk_bf16_f32 v107, v106, v107\n\t"
      "v_cvt_pk_bf16_f32 v106, v104, v105\n\t"
      "v_cvt_pk_bf16_f32 v104, v80, v81\n\t"
      "v_cvt_pk_bf16_f32 v105, v82, v83\n\t"
      "v_mfma_f32_16x16x32_bf16 v[220:223], v[216:219], v[208:211], v[48:51]\n\t"
      "v_cvt_pk_bf16_f32 v111, v110, v111\n\t"
      "v_cvt_pk_bf16_f32 v110, v108, v109\n\t"
      "v_cvt_pk_bf16_f32 v108, v84, v85\n\t"
      "v_cvt_pk_bf16_f32 v109, v86, v87\n\t"
      "v_mfma_f32_16x16x32_bf16 v[236:239], v[216:219], v[232:235], v[44:47]\n\t"
      "v_cvt_pk_bf16_f32 v83, v82, v83\n\t"
      "v_cvt_pk_bf16_f32 v82, v80, v81\n\t"
      "v_mov_b32 v80, v36\n\t"
      "v_mov_b32 v81, v37\n\t"
      "v_mfma_f32_16x16x32_bf16 v[244:247], v[240:243], v[232:235], v[48:51]\n\t"
      "v_cvt_pk_bf16_f32 v87, v86, v87\n\t"
      "v_cvt_pk_bf16_f32 v86, v84, v85\n\t"
      "v_mov_b32 v84, v38\n\t"
      "v_mov_b32 v85, v39\n\t"
      "v_mov_b32 v56, 0x3f803f80\n\t"
      "v_mov_b32 v57, 0x3f803f80\n\t"
      "v_mov_b32 v58, 0x3f803f80\n\t"
      "v_mov_b32 v59, 0x3f803f80\n\t"
      "s_mov_b32 s57, 0x3e8293ee\n\t"
      "v_max3_f32 v64, v68, v69, v70\n\t"
      "v_max3_f32 v88, v92, v93, v94\n\t"
      "v_max3_f32 v112, v116, v117, v118\n\t"
      "v_max3_f32 v136, v140, v141, v142\n\t"
      "v_max3_f32 v160, v164, v165, v166\n\t"
      "v_max3_f32 v184, v188, v189, v190\n\t"
      "v_max3_f32 v208, v212, v213, v214\n\t"
      "v_max3_f32 v232, v236, v237, v238\n\t"
      "v_max3_f32 v65, v76, v77, v78\n\t"
      "v_max3_f32 v89, v100, v101, v102\n\t"
      "v_max3_f32 v113, v124, v125, v126\n\t"
      "v_max3_f32 v137, v148, v149, v150\n\t"
      "v_max3_f32 v161, v172, v173, v174\n\t"
      "v_max3_f32 v185, v196, v197, v198\n\t"
      "v_max3_f32 v209, v220, v221, v222\n\t"
      "v_max3_f32 v233, v244, v245, v246\n\t"
      "v_max3_f32 v64, v64, v71, v79\n\t"
      "v_max3_f32 v88, v88, v95, v103\n\t"
      "v_max3_f32 v112, v112, v119, v127\n\t"
      "v_max3_f32 v136, v136, v143, v151\n\t"
      "v_max3_f32 v160, v160, v167, v175\n\t"
      "v_max3_f32 v184, v184, v191, v199\n\t"
      "v_max3_f32 v208, v208, v215, v223\n\t"
      "v_max3_f32 v232, v232, v239, v247\n\t"
      "v_max_f32 v64, v64, v65\n\t"
      "v_max_f32 v88, v88, v89\n\t"
      "v_max_f32 v112, v112, v113\n\t"
      "v_max_f32 v136, v136, v137\n\t"
      "v_max_f32 v160, v160, v161\n\t"
      "v_max_f32 v184, v184, v185\n\t"
      "v_max_f32 v208, v208, v209\n\t"
      "v_max_f32 v232, v232, v233\n\t"
      "v_mov_b32 v65, v64\n\t"
      "v_mov_b32 v89, v88\n\t"
      "v_mov_b32 v113, v112\n\t"
      "v_mov_b32 v137, v136\n\t"
      "v_mov_b32 v161, v160\n\t"
      "v_mov_b32 v185, v184\n\t"
      "v_mov_b32 v209, v208\n\t"
      "v_mov_b32 v233, v232\n\t"
      "v_permlane32_swap_b32 v64, v65\n\t"
      "v_permlane32_swap_b32 v88, v89\n\t"
      "v_permlane32_swap_b32 v112, v113\n\t"
      "v_permlane32_swap_b32 v136, v137\n\t"
      "v_permlane32_swap_b32 v160, v161\n\t"
      "v_permlane32_swap_b32 v184, v185\n\t"
      "v_permlane32_swap_b32 v208, v209\n\t"
      "v_permlane32_swap_b32 v232, v233\n\t"
      "v_max_f32 v64, v64, v65\n\t"
      "v_max_f32 v88, v88, v89\n\t"
      "v_max_f32 v112, v112, v113\n\t"
      "v_max_f32 v136, v136, v137\n\t"
      "v_max_f32 v160, v160, v161\n\t"
      "v_max_f32 v184, v184, v185\n\t"
      "v_max_f32 v208, v208, v209\n\t"
      "v_max_f32 v232, v232, v233\n\t"
      "v_mov_b32 v65, v64\n\t"
      "v_mov_b32 v89, v88\n\t"
      "v_mov_b32 v113, v112\n\t"
      "v_mov_b32 v137, v136\n\t"
      "v_mov_b32 v161, v160\n\t"
      "v_mov_b32 v185, v184\n\t"
      "v_mov_b32 v209, v208\n\t"
      "v_mov_b32 v233, v232\n\t"
      "v_permlane16_swap_b32 v64, v65\n\t"
      "global_load_dword v60, %[vp1], %[pf] sc1\n\t"
      "v_permlane16_swap_b32 v88, v89\n\t"
      "v_permlane16_swap_b32 v112, v113\n\t"
      "v_permlane16_swap_b32 v136, v137\n\t"
      "v_permlane16_swap_b32 v160, v161\n\t"
      "v_permlane16_swap_b32 v184, v185\n\t"
      "v_permlane16_swap_b32 v208, v209\n\t"
      "v_permlane16_swap_b32 v232, v233\n\t"
      "v_max_f32 v64, v64, v65\n\t"
      "v_max_f32 v88, v88, v89\n\t"
      "v_max_f32 v112, v112, v113\n\t"
      "v_max_f32 v136, v136, v137\n\t"
      "v_max_f32 v160, v160, v161\n\t"
      "v_max_f32 v184, v184, v185\n\t"
      "v_max_f32 v208, v208, v209\n\t"
      "v_max_f32 v232, v232, v233\n\t"
      "v_mul_f32 v64, 0x3e8293ee, v64\n\t"
      "v_mul_f32 v88, 0x3e8293ee, v88\n\t"
      "v_mul_f32 v112, 0x3e8293ee, v112\n\t"
      "v_mul_f32 v136, 0x3e8293ee, v136\n\t"
      "v_mul_f32 v160, 0x3e8293ee, v160\n\t"
      "v_mul_f32 v184, 0x3e8293ee, v184\n\t"
      "v_mul_f32 v208, 0x3e8293ee, v208\n\t"
      "v_mul_f32 v232, 0x3e8293ee, v232\n\t"
      "v_fma_f32 v68, v68, s57, -v64\n\t"
      "v_fma_f32 v92, v92, s57, -v88\n\t"
      "v_fma_f32 v116, v116, s57, -v112\n\t"
      "v_fma_f32 v140, v140, s57, -v136\n\t"
      "v_fma_f32 v164, v164, s57, -v160\n\t"
      "v_fma_f32 v188, v188, s57, -v184\n\t"
      "v_fma_f32 v212, v212, s57, -v208\n\t"
      "v_fma_f32 v236, v236, s57, -v232\n\t"
      "v_fma_f32 v76, v76, s57, -v64\n\t"
      "v_fma_f32 v100, v100, s57, -v88\n\t"
      "v_fma_f32 v124, v124, s57, -v112\n\t"
      "v_fma_f32 v148, v148, s57, -v136\n\t"
      "v_fma_f32 v172, v172, s57, -v160\n\t"
      "v_fma_f32 v196, v196, s57, -v184\n\t"
      "v_fma_f32 v220, v220, s57, -v208\n\t"
      "v_fma_f32 v244, v244, s57, -v232\n\t"
      "v_fma_f32 v69, v69, s57, -v64\n\t"
      "v_fma_f32 v93, v93, s57, -v88\n\t"
      "v_fma_f32 v117, v117, s57, -v112\n\t"
      "v_fma_f32 v141, v141, s57, -v136\n\t"
      "v_fma_f32 v165, v165, s57, -v160\n\t"
      "v_fma_f32 v189, v189, s57, -v184\n\t"
      "v_fma_f32 v213, v213, s57, -v208\n\t"
      "v_fma_f32 v237, v237, s57, -v232\n\t"
      "v_fma_f32 v77, v77, s57, -v64\n\t"
      "v_fma_f32 v101, v101, s57, -v88\n\t"
      "v_fma_f32 v125, v125, s57, -v112\n\t"
      "v_fma_f32 v149, v149, s57, -v136\n\t"
      "v_fma_f32 v173, v173, s57, -v160\n\t"
      "v_fma_f32 v197, v197, s57, -v184\n\t"
      "v_fma_f32 v221, v221, s57, -v208\n\t"
      "v_fma_f32 v245, v245, s57, -v232\n\t"
      "v_fma_f32 v70, v70, s57, -v64\n\t"
      "v_fma_f32 v94, v94, s57, -v88\n\t"
      "v_fma_f32 v118, v118, s57, -v112\n\t"
      "v_fma_f32 v142, v142, s57, -v136\n\t"
      "v_fma_f32 v166, v166, s57, -v160\n\t"
      "v_fma_f32 v190, v190, s57, -v184\n\t"
      "v_fma_f32 v214, v214, s57, -v208\n\t"
      "v_fma_f32 v238, v238, s57, -v232\n\t"
      "v_fma_f32 v78, v78, s57, -v64\n\t"
      "v_fma_f32 v102, v102, s57, -v88\n\t"
      "v_fma_f32 v126, v126, s57, -v112\n\t"
      "v_fma_f32 v150, v150, s57, -v136\n\t"
      "v_fma_f32 v174, v174, s57, -v160\n\t"
      "v_fma_f32 v198, v198, s57, -v184\n\t"
      "v_fma_f32 v222, v222, s57, -v208\n\t"
      "v_fma_f32 v246, v246, s57, -v232\n\t"
      "v_fma_f32 v71, v71, s57, -v64\n\t"
      "v_fma_f32 v95, v95, s57, -v88\n\t"
      "v_fma_f32 v119, v119, s57, -v112\n\t"
      "v_fma_f32 v143, v143, s57, -v136\n\t"
      "v_fma_f32 v167, v167, s57, -v160\n\t"
      "v_fma_f32 v191, v191, s57, -v184\n\t"
      "v_fma_f32 v215, v215, s57, -v208\n\t"
      "v_fma_f32 v239, v239, s57, -v232\n\t"
      "v_fma_f32 v79, v79, s57, -v64\n\t"
      "v_fma_f32 v103, v103, s57, -v88\n\t"
      "v_fma_f32 v127, v127, s57, -v112\n\t"
      "v_fma_f32 v151, v151, s57, -v136\n\t"
      "v_fma_f32 v175, v175, s57, -v160\n\t"
      "v_fma_f32 v199, v199, s57, -v184\n\t"
      "v_fma_f32 v223, v223, s57, -v208\n\t"
      "v_fma_f32 v247, v247, s57, -v232\n\t"
      "v_exp_f32 v68, v68\n\t"
      "v_exp_f32 v92, v92\n\t"
      "v_exp_f32 v116, v116\n\t"
      "v_exp_f32 v140, v140\n\t"
      "v_exp_f32 v164, v164\n\t"
      "v_exp_f32 v188, v188\n\t"
      "v_exp_f32 v212, v212\n\t"
      "v_exp_f32 v236, v236\n\t"
      "v_exp_f32 v76, v76\n\t"
      "v_exp_f32 v100, v100\n\t"
      "v_exp_f32 v124, v124\n\t"
      "v_exp_f32 v148, v148\n\t"
      "v_exp_f32 v172, v172\n\t"
      "v_exp_f32 v196, v196\n\t"
      "v_exp_f32 v220, v220\n\t"
      "v_exp_f32 v244, v244\n\t"
      "v_exp_f32 v69, v69\n\t"
      "v_exp_f32 v93, v93\n\t"
      "v_exp_f32 v117, v117\n\t"
      "v_exp_f32 v141, v141\n\t"
      "v_exp_f32 v165, v165\n\t"
      "v_exp_f32 v189, v189\n\t"
      "v_exp_f32 v213, v213\n\t"
      "v_exp_f32 v237, v237\n\t"
      "v_exp_f32 v77, v77\n\t"
      "v_exp_f32 v101, v101\n\t"
      "v_exp_f32 v125, v125\n\t"
      "v_exp_f32 v149, v149\n\t"
      "v_exp_f32 v173, v173\n\t"
      "v_exp_f32 v197, v197\n\t"
      "v_exp_f32 v221, v221\n\t"
      "v_exp_f32 v245, v245\n\t"
      "v_exp_f32 v70, v70\n\t"
      "v_exp_f32 v94, v94\n\t"
      "v_exp_f32 v118, v118\n\t"
      "v_exp_f32 v142, v142\n\t"
      "v_exp_f32 v166, v166\n\t"
      "v_exp_f32 v190, v190\n\t"
      "v_exp_f32 v214, v214\n\t"
      "v_exp_f32 v238, v238\n\t"
      "v_exp_f32 v78, v78\n\t"
      "v_exp_f32 v102, v102\n\t"
      "v_exp_f32 v126, v126\n\t"
      "v_exp_f32 v150, v150\n\t"
      "v_exp_f32 v174, v174\n\t"
      "v_exp_f32 v198, v198\n\t"
      "v_exp_f32 v222, v222\n\t"
      "v_exp_f32 v246, v246\n\t"
      "v_exp_f32 v71, v71\n\t"
      "v_exp_f32 v95, v95\n\t"
      "v_exp_f32 v119, v119\n\t"
      "v_exp_f32 v143, v143\n\t"
      "v_exp_f32 v167, v167\n\t"
      "v_exp_f32 v191, v191\n\t"
      "v_exp_f32 v215, v215\n\t"
      "v_exp_f32 v239, v239\n\t"
      "v_exp_f32 v79, v79\n\t"
      "v_exp_f32 v103, v103\n\t"
      "v_exp_f32 v127, v127\n\t"
      "v_exp_f32 v151, v151\n\t"
      "v_exp_f32 v175, v175\n\t"
      "v_exp_f32 v199, v199\n\t"
      "v_exp_f32 v223, v223\n\t"
      "s_waitcnt vmcnt(0)\n\t"
      "s_mov_b32 s56, 0x40000\n\t"
      "W1P_%=:\n"
      "v_cmp_ne_u32 vcc, v60, %[tag]\n\t"
      "s_nop 4\n\t"
      "s_cbranch_vccz W1S_%=\n\t"
      "s_sleep 4\n\t"
      "global_load_dword v60, %[vp1], %[pf] sc1\n\t"
      "s_sub_u32 s56, s56, 1\n\t"
      "s_cmp_lg_u32 s56, 0\n\t"
      "s_waitcnt vmcnt(0)\n\t"
      "s_cbranch_scc1 W1P_%=\n\t"
      "W1S_%=:\n"
      "buffer_load_dwordx4 a[240:243], %[vqo], %[so], 0 offen offset:0 sc1\n\t"
      "buffer_load_dwordx4 a[244:247], %[vqo], %[so], 0 offen offset:1024 sc1\n\t"
      "buffer_load_dwordx4 a[248:251], %[vqo], %[so], 0 offen offset:2048 sc1\n\t"
      "buffer_load_dwordx4 a[252:255], %[vqo], %[so], 0 offen offset:3072 sc1\n\t"
      "buffer_load_dword a238, %[vmo], %[so], 0 offen offset:0 sc1\n\t"
      "buffer_load_dword a239, %[vmo], %[so], 0 offen offset:128 sc1\n\t"
      "s_add_u32 s52, %[krot], 0\n\t"
      "s_add_u32 m0, s54, 0\n\t"
      "s_and_b32 s52, s52, 0x3ffff\n\t"
      "s_nop 0\n\t"
      "buffer_load_dwordx4 %[vg], s[40:43], s52 offen offset:0 sc1 lds\n\t"
      "buffer_load_dwordx4 %[vg], s[40:43], s52 offen offset:1024 sc1 lds\n\t"
      "buffer_load_dwordx4 %[vg], s[40:43], s52 offen offset:2048 sc1 lds\n\t"
      "buffer_load_dwordx4 %[vg], s[40:43], s52 offen offset:3072 sc1 lds\n\t"
      "v_exp_f32 v247, v247\n\t"
      "v_cvt_pk_bf16_f32 v68, v68, v69\n\t"
      "v_cvt_pk_bf16_f32 v92, v92, v93\n\t"
      "v_cvt_pk_bf16_f32 v116, v116, v117\n\t"
      "v_cvt_pk_bf16_f32 v140, v140, v141\n\t"
      "v_cvt_pk_bf16_f32 v164, v164, v165\n\t"
      "v_cvt_pk_bf16_f32 v188, v188, v189\n\t"
      "v_cvt_pk_bf16_f32 v212, v212, v213\n\t"
      "v_cvt_pk_bf16_f32 v236, v236, v237\n\t"
      "v_cvt_pk_bf16_f32 v69, v70, v71\n\t"
      "v_cvt_pk_bf16_f32 v93, v94, v95\n\t"
      "v_cvt_pk_bf16_f32 v117, v118, v119\n\t"
      "v_cvt_pk_bf16_f32 v141, v142, v143\n\t"
      "v_cvt_pk_bf16_f32 v165, v166, v167\n\t"
      "v_cvt_pk_bf16_f32 v189, v190, v191\n\t"
      "v_cvt_pk_bf16_f32 v213, v214, v215\n\t"
      "v_cvt_pk_bf16_f32 v237, v238, v239\n\t"
      "v_cvt_pk_bf16_f32 v70, v76, v77\n\t"
      "v_cvt_pk_bf16_f32 v94, v100, v101\n\t"
      "v_cvt_pk_bf16_f32 v118, v124, v125\n\t"
      "v_cvt_pk_bf16_f32 v142, v148, v149\n\t"
      "v_cvt_pk_bf16_f32 v166, v172, v173\n\t"
      "v_cvt_pk_bf16_f32 v190, v196, v197\n\t"
      "v_cvt_pk_bf16_f32 v214, v220, v221\n\t"
      "v_cvt_pk_bf16_f32 v238, v244, v245\n\t"
      "v_cvt_pk_bf16_f32 v71, v78, v79\n\t"
      "v_cvt_pk_bf16_f32 v95, v102, v103\n\t"
      "v_cvt_pk_bf16_f32 v119, v126, v127\n\t"
      "v_cvt_pk_bf16_f32 v143, v150, v151\n\t"
      "v_cvt_pk_bf16_f32 v167, v174, v175\n\t"
      "v_cvt_pk_bf16_f32 v191, v198, v199\n\t"
      "v_cvt_pk_bf16_f32 v215, v222, v223\n\t"
      "v_cvt_pk_bf16_f32 v239, v246, v247\n\t"
      "v_mfma_f32_16x16x32_bf16 v[76:79], v[56:59], v[68:71], 0\n\t"
      "v_mfma_f32_16x16x32_bf16 v[100:103], v[56:59], v[92:95], 0\n\t"
      "v_mfma_f32_16x16x32_bf16 v[124:127], v[56:59], v[116:119], 0\n\t"
      "v_mfma_f32_16x16x32_bf16 v[148:151], v[56:59], v[140:143], 0\n\t"
      "v_mfma_f32_16x16x32_bf16 v[172:175], v[56:59], v[164:167], 0\n\t"
      "v_mfma_f32_16x16x32_bf16 v[196:199], v[56:59], v[188:191], 0\n\t"
      "v_mfma_f32_16x16x32_bf16 v[220:223], v[56:59], v[212:215], 0\n\t"
      "v_mfma_f32_16x16x32_bf16 v[244:247], v[56:59], v[236:239], 0\n\t"
      "v_mfma_f32_16x16x32_bf16 v[64:67], v[80:83], v[68:71], 0\n\t"
      "v_mfma_f32_16x16x32_bf16 v[88:91], v[104:107], v[92:95], 0\n\t"
      "v_mfma_f32_16x16x32_bf16 v[112:115], v[128:131], v[116:119], 0\n\t"
      "v_mfma_f32_16x16x32_bf16 v[136:139], v[152:155], v[140:143], 0\n\t"
      "v_mfma_f32_16x16x32_bf16 v[160:163], v[176:179], v[164:167], 0\n\t"
      "v_mfma_f32_16x16x32_bf16 v[184:187], v[200:203], v[188:191], 0\n\t"
      "v_mfma_f32_16x16x32_bf16 v[208:211], v[224:227], v[212:215], 0\n\t"
      "v_mfma_f32_16x16x32_bf16 v[232:235], v[248:251], v[236:239], 0\n\t"
      "v_mfma_f32_16x16x32_bf16 v[72:75], v[84:87], v[68:71], 0\n\t"
      "v_mfma_f32_16x16x32_bf16 v[96:99], v[108:111], v[92:95], 0\n\t"
      "v_mfma_f32_16x16x32_bf16 v[120:123], v[132:135], v[116:119], 0\n\t"
      "v_mfma_f32_16x16x32_bf16 v[144:147], v[156:159], v[140:143], 0\n\t"
      "v_mfma_f32_16x16x32_bf16 v[168:171], v[180:183], v[164:167], 0\n\t"
      "v_mfma_f32_16x16x32_bf16 v[192:195], v[204:207], v[188:191], 0\n\t"
      "v_mfma_f32_16x16x32_bf16 v[216:219], v[228:231], v[212:215], 0\n\t"
      "v_mfma_f32_16x16x32_bf16 v[240:243], v[252:255], v[236:239], 0\n\t"
      "v_rcp_f32 v76, v76\n\t"
      "v_rcp_f32 v100, v100\n\t"
      "v_rcp_f32 v124, v124\n\t"
      "v_rcp_f32 v148, v148\n\t"
      "v_rcp_f32 v172, v172\n\t"
      "v_rcp_f32 v196, v196\n\t"
      "v_rcp_f32 v220, v220\n\t"
      "v_rcp_f32 v244, v244\n\t"
      "v_mul_f32 v64, v64, v76\n\t"
      "v_mul_f32 v88, v88, v100\n\t"
      "v_mul_f32 v112, v112, v124\n\t"
      "v_mul_f32 v136, v136, v148\n\t"
      "v_mul_f32 v160, v160, v172\n\t"
      "v_mul_f32 v184, v184, v196\n\t"
      "v_mul_f32 v208, v208, v220\n\t"
      "v_mul_f32 v232, v232, v244\n\t"
      "v_mul_f32 v72, v72, v76\n\t"
      "v_mul_f32 v96, v96, v100\n\t"
      "v_mul_f32 v120, v120, v124\n\t"
      "v_mul_f32 v144, v144, v148\n\t"
      "v_mul_f32 v168, v168, v172\n\t"
      "v_mul_f32 v192, v192, v196\n\t"
      "v_mul_f32 v216, v216, v220\n\t"
      "v_mul_f32 v240, v240, v244\n\t"
      "v_mul_f32 v65, v65, v76\n\t"
      "v_mul_f32 v89, v89, v100\n\t"
      "v_mul_f32 v113, v113, v124\n\t"
      "v_mul_f32 v137, v137, v148\n\t"
      "v_mul_f32 v161, v161, v172\n\t"
      "v_mul_f32 v185, v185, v196\n\t"
      "v_mul_f32 v209, v209, v220\n\t"
      "v_mul_f32 v233, v233, v244\n\t"
      "v_mul_f32 v73, v73, v76\n\t"
      "v_mul_f32 v97, v97, v100\n\t"
      "v_mul_f32 v121, v121, v124\n\t"
      "v_mul_f32 v145, v145, v148\n\t"
      "v_mul_f32 v169, v169, v172\n\t"
      "v_mul_f32 v193, v193, v196\n\t"
      "v_mul_f32 v217, v217, v220\n\t"
      "v_mul_f32 v241, v241, v244\n\t"
      "v_mul_f32 v66, v66, v76\n\t"
      "v_mul_f32 v90, v90, v100\n\t"
      "v_mul_f32 v114, v114, v124\n\t"
      "v_mul_f32 v138, v138, v148\n\t"
      "v_mul_f32 v162, v162, v172\n\t"
      "v_mul_f32 v186, v186, v196\n\t"
      "v_mul_f32 v210, v210, v220\n\t"
      "v_mul_f32 v234, v234, v244\n\t"
      "v_mul_f32 v74, v74, v76\n\t"
      "v_mul_f32 v98, v98, v100\n\t"
      "v_mul_f32 v122, v122, v124\n\t"
      "v_mul_f32 v146, v146, v148\n\t"
      "v_mul_f32 v170, v170, v172\n\t"
      "v_mul_f32 v194, v194, v196\n\t"
      "v_mul_f32 v218, v218, v220\n\t"
      "v_mul_f32 v242, v242, v244\n\t"
      "v_mul_f32 v67, v67, v76\n\t"
      "v_mul_f32 v91, v91, v100\n\t"
      "v_mul_f32 v115, v115, v124\n\t"
      "v_mul_f32 v139, v139, v148\n\t"
      "v_mul_f32 v163, v163, v172\n\t"
      "v_mul_f32 v187, v187, v196\n\t"
      "v_mul_f32 v211, v211, v220\n\t"
      "v_mul_f32 v235, v235, v244\n\t"
      "v_mul_f32 v75, v75, v76\n\t"
      "v_mul_f32 v99, v99, v100\n\t"
      "v_mul_f32 v123, v123, v124\n\t"
      "v_mul_f32 v147, v147, v148\n\t"
      "v_mul_f32 v171, v171, v172\n\t"
      "v_mul_f32 v195, v195, v196\n\t"
      "v_mul_f32 v219, v219, v220\n\t"
      "v_mul_f32 v243, v243, v244\n\t"
      "v_cvt_pk_bf16_f32 v64, v64, v65\n\t"
      "v_cvt_pk_bf16_f32 v88, v88, v89\n\t"
      "v_cvt_pk_bf16_f32 v112, v112, v113\n\t"
      "v_cvt_pk_bf16_f32 v136, v136, v137\n\t"
      "v_cvt_pk_bf16_f32 v160, v160, v161\n\t"
      "v_cvt_pk_bf16_f32 v184, v184, v185\n\t"
      "v_cvt_pk_bf16_f32 v208, v208, v209\n\t"
      "v_cvt_pk_bf16_f32 v232, v232, v233\n\t"
      "v_cvt_pk_bf16_f32 v65, v66, v67\n\t"
      "v_cvt_pk_bf16_f32 v89, v90, v91\n\t"
      "v_cvt_pk_bf16_f32 v113, v114, v115\n\t"
      "v_cvt_pk_bf16_f32 v137, v138, v139\n\t"
      "v_cvt_pk_bf16_f32 v161, v162, v163\n\t"
      "v_cvt_pk_bf16_f32 v185, v186, v187\n\t"
      "v_cvt_pk_bf16_f32 v209, v210, v211\n\t"
      "v_cvt_pk_bf16_f32 v233, v234, v235\n\t"
      "v_cvt_pk_bf16_f32 v66, v72, v73\n\t"
      "v_cvt_pk_bf16_f32 v90, v96, v97\n\t"
      "v_cvt_pk_bf16_f32 v114, v120, v121\n\t"
      "v_cvt_pk_bf16_f32 v138, v144, v145\n\t"
      "v_cvt_pk_bf16_f32 v162, v168, v169\n\t"
      "v_cvt_pk_bf16_f32 v186, v192, v193\n\t"
      "v_cvt_pk_bf16_f32 v210, v216, v217\n\t"
      "v_cvt_pk_bf16_f32 v234, v240, v241\n\t"
      "v_cvt_pk_bf16_f32 v67, v74, v75\n\t"
      "v_cvt_pk_bf16_f32 v91, v98, v99\n\t"
      "v_cvt_pk_bf16_f32 v115, v122, v123\n\t"
      "v_cvt_pk_bf16_f32 v139, v146, v147\n\t"
      "v_cvt_pk_bf16_f32 v163, v170, v171\n\t"
      "v_cvt_pk_bf16_f32 v187, v194, v195\n\t"
      "v_cvt_pk_bf16_f32 v211, v218, v219\n\t"
      "v_cvt_pk_bf16_f32 v235, v242, v243\n\t"
      "s_mov_b32 s55, 0\n\t"
      "buffer_store_dwordx4 v[64:67], %[vq], %[so], s55 offen sc1\n\t"
      "s_mov_b32 s55, 32768\n\t"
      "buffer_store_dwordx4 v[88:91], %[vq], %[so], s55 offen sc1\n\t"
      "s_mov_b32 s55, 65536\n\t"
      "buffer_store_dwordx4 v[112:115], %[vq], %[so], s55 offen sc1\n\t"
      "s_mov_b32 s55, 98304\n\t"
      "buffer_store_dwordx4 v[136:139], %[vq], %[so], s55 offen sc1\n\t"
      "s_mov_b32 s55, 131072\n\t"
      "buffer_store_dwordx4 v[160:163], %[vq], %[so], s55 offen sc1\n\t"
      "s_mov_b32 s55, 163840\n\t"
      "buffer_store_dwordx4 v[184:187], %[vq], %[so], s55 offen sc1\n\t"
      "s_mov_b32 s55, 196608\n\t"
      "buffer_store_dwordx4 v[208:211], %[vq], %[so], s55 offen sc1\n\t"
      "s_mov_b32 s55, 229376\n\t"
      "buffer_store_dwordx4 v[232:235], %[vq], %[so], s55 offen sc1\n\t"
        : [tag] "=&v"(tag), "={a[240:255]}"(qfr), "={a[238:239]}"(ref)
        : [px] "s"(px), [pw] "s"(pw), [wid] "s"(wid), [vx0] "v"(vx0), [vw0] "v"(vw0),
          [xr] "v"(xr), [wr] "v"(wrd), [vq] "v"(vq), [so] "s"(sws), [pf] "s"(pf),
          [vxh] "v"(vxh), [hd] "s"(hd), [xh] "v"(xh), [msk] "v"(msk),
          [vp1] "v"(vp1), [pkv] "s"(pkv), [pwb] "s"(wob), [vw] "v"(vw), [wv1k] "s"(wv1k), [vqo] "v"(vqo), [vmo] "v"(vmo), [vg] "v"(vg), [cpo] "v"(cpo), [krot] "s"(krot)
        : "v32", "v33", "v34", "v35", "v36", "v37", "v38", "v39", "v40", "v41", "v42", "v43", "v44", "v45", "v46", "v47", "v48", "v49", "v50", "v51", "v52", "v53", "v54", "v55", "v56", "v57", "v58", "v59", "v60", "v61", "v62", "v63", "v64", "v65", "v66", "v67", "v68", "v69", "v70", "v71", "v72", "v73", "v74", "v75", "v76", "v77", "v78", "v79", "v80", "v81", "v82", "v83", "v84", "v85", "v86", "v87", "v88", "v89", "v90", "v91", "v92", "v93", "v94", "v95", "v96", "v97", "v98", "v99", "v100", "v101", "v102", "v103", "v104", "v105", "v106", "v107", "v108", "v109", "v110", "v111", "v112", "v113", "v114", "v115", "v116", "v117", "v118", "v119", "v120", "v121", "v122", "v123", "v124", "v125", "v126", "v127", "v128", "v129", "v130", "v131", "v132", "v133", "v134", "v135", "v136", "v137", "v138", "v139", "v140", "v141", "v142", "v143", "v144", "v145", "v146", "v147", "v148", "v149", "v150", "v151", "v152", "v153", "v154", "v155", "v156", "v157", "v158", "v159", "v160", "v161", "v162", "v163", "v164", "v165", "v166", "v167", "v168", "v169", "v170", "v171", "v172", "v173", "v174", "v175", "v176", "v177", "v178", "v179", "v180", "v181", "v182", "v183", "v184", "v185", "v186", "v187", "v188", "v189", "v190", "v191", "v192", "v193", "v194", "v195", "v196", "v197", "v198", "v199", "v200", "v201", "v202", "v203", "v204", "v205", "v206", "v207", "v208", "v209", "v210", "v211", "v212", "v213", "v214", "v215", "v216", "v217", "v218", "v219", "v220", "v221", "v222", "v223", "v224", "v225", "v226", "v227", "v228", "v229", "v230", "v231", "v232", "v233", "v234", "v235", "v236", "v237", "v238", "v239", "v240", "v241", "v242", "v243", "v244", "v245", "v246", "v247", "v248", "v249", "v250", "v251", "v252", "v253", "v254", "v255", "a0", "a1", "a2", "a3", "a4", "a5", "a6", "a7", "a8", "a9", "a10", "a11", "a12", "a13", "a14", "a15", "a16", "a17", "a18", "a19", "a20", "a21", "a22", "a23", "a24", "a25", "a26", "a27", "a28", "a29", "a30", "a31", "a32", "a33", "a34", "a35", "a36", "a37", "a38", "a39", "a40", "a41", "a42", "a43", "a44", "a45", "a46", "a47", "a48", "a49", "a50", "a51", "a52", "a53", "a54", "a55", "a56", "a57", "a58", "a59", "a60", "a61", "a62", "a63", "a64", "a65", "a66", "a67", "a68", "a69", "a70", "a71", "a72", "a73", "a74", "a75", "a76", "a77", "a78", "a79", "a80", "a81", "a82", "a83", "a84", "a85", "a86", "a87", "a88", "a89", "a90", "a91", "a92", "a93", "a94", "a95", "a96", "a97", "a98", "a99", "a100", "a101", "a102", "a103", "a104", "a105", "a106", "a107", "a108", "a109", "a110", "a111", "a112", "a113", "a114", "a115", "a116", "a117", "a118", "a119", "a120", "a121", "a122", "a123", "a124", "a125", "a126", "a127", "a128", "a129", "a130", "a131", "a132", "a133", "a134", "a135", "a237", "s40", "s41", "s42", "s43", "s44", "s45", "s46", "s47", "s48", "s49", "s50", "s51", "s52", "s53", "s54", "s55", "s56", "s57", "s58", "s59", "s60", "s61", "s62", "s63", "s92", "s93", "s94", "s95", "s96", "s97", "s98", "s99", "scc", "vcc", "memory");
    }
  }

  unsigned sm0, sm1, sm2, sm3;
  const unsigned bump = (blockIdx.x ^ 128u) | wid, vsm = F_OA + (l >> 5) * 4096u + spread(q, FS2);
  {
    const char* po = ws + WS_O;
    const u32x4 so = make_srd(out, 0x800000u);
    asm volatile(
    "s_mov_b64 s[40:41], %[pkv]\n\t"
    "s_mov_b32 s42, 0x40000\n\t"
    "s_mov_b32 s43, 0x20000\n\t"
    "s_mov_b64 s[44:45], %[pwb]\n\t"
    "s_mov_b32 s46, 0x200000\n\t"
    "s_mov_b32 s47, 0x20000\n\t"
    "s_lshl_b32 s54, %[wid], 12\n\t"
    "v_add_u32 v224, 0, %[vw]\n\t"
    "v_add_u32 v225, 15360, %[vw]\n\t"
    "v_add_u32 v226, 30720, %[vw]\n\t"
    "v_add_u32 v227, 46080, %[vw]\n\t"
    "s_getpc_b64 s[62:63]\n\t"
    ".Lpc1_%=:\n"
    "s_sub_u32 s62, s62, .Lpc1_%=-.Lfwd_begin\n\t"
    "s_subb_u32 s63, s63, 0\n\t"
    "v_min_u32 v248, .Lfwd_end-.Lfwd_begin-16, %[cpo]\n\t"
    "s_add_u32 s52, %[krot], 16384\n\t"
    "s_add_u32 m0, s54, 16384\n\t"
    "s_and_b32 s52, s52, 0x3ffff\n\t"
    "s_nop 0\n\t"
    "buffer_load_dwordx4 %[vg], s[40:43], s52 offen offset:0 sc1 lds\n\t"
    "buffer_load_dwordx4 %[vg], s[40:43], s52 offen offset:1024 sc1 lds\n\t"
    "buffer_load_dwordx4 %[vg], s[40:43], s52 offen offset:2048 sc1 lds\n\t"
    "buffer_load_dwordx4 %[vg], s[40:43], s52 offen offset:3072 sc1 lds\n\t"
    "s_mov_b64 s[48:49], %[po]\n\t"
    "s_mov_b32 s50, 0x800000\n\t"
    "s_mov_b32 s51, 0x20000\n\t"
    "s_lshl_b32 s59, %[wid], 13\n\t"
    "v_mov_b32 v228, %[wrd]\n\t"
    "v_xor_b32 v229, 32, %[wrd]\n\t"
    "v_mov_b32 v124, 0xdddddddd\n\t"
    "v_mov_b32 v244, %[vo]\n\t"
    "v_add_u32 v245, 2048, %[vo]\n\t"
    "v_add_u32 v246, 32768, %[vo]\n\t"
    "v_add_u32 v247, 34816, %[vo]\n\t"
    "v_mov_b32 v120, %[one0]\n\t"
    "v_mov_b32 v121, %[one0]\n\t"
    "v_mov_b32 v122, %[one1]\n\t"
    "v_mov_b32 v123, %[one1]\n\t"
    "v_mov_b32 v112, 0\n\t"
    "v_mov_b32 v113, 0\n\t"
    "v_mov_b32 v114, 0\n\t"
    "v_mov_b32 v115, 0\n\t"
    "v_mov_b32 v116, 0\n\t"
    "v_mov_b32 v117, 0\n\t"
    "v_mov_b32 v118, 0\n\t"
    "v_mov_b32 v119, 0\n\t"
    "v_mfma_f32_32x32x16_bf16 v[80:95], v[112:115], v[112:115], 0\n\t"
    "v_mfma_f32_32x32x16_bf16 v[96:111], v[112:115], v[112:115], 0\n\t"
    "s_waitcnt vmcnt(16)\n\t"
    "v_accvgpr_read_b32 v32, a240\n\t"
    "v_accvgpr_read_b32 v33, a241\n\t"
    "v_accvgpr_read_b32 v34, a242\n\t"
    "v_accvgpr_read_b32 v35, a243\n\t"
    "v_accvgpr_read_b32 v36, a244\n\t"
    "v_accvgpr_read_b32 v37, a245\n\t"
    "v_accvgpr_read_b32 v38, a246\n\t"
    "v_accvgpr_read_b32 v39, a247\n\t"
    "v_accvgpr_read_b32 v40, a248\n\t"
    "v_accvgpr_read_b32 v41, a249\n\t"
    "v_accvgpr_read_b32 v42, a250\n\t"
    "v_accvgpr_read_b32 v43, a251\n\t"
    "v_accvgpr_read_b32 v44, a252\n\t"
    "v_accvgpr_read_b32 v45, a253\n\t"
    "v_accvgpr_read_b32 v46, a254\n\t"
    "v_accvgpr_read_b32 v47, a255\n\t"
    "v_accvgpr_read_b32 v48, a238\n\t"
    "v_accvgpr_read_b32 v64, a239\n\t"
    "v_accvgpr_read_b32 v49, a238\n\t"
    "v_accvgpr_read_b32 v65, a239\n\t"
    "v_accvgpr_read_b32 v50, a238\n\t"
    "v_accvgpr_read_b32 v66, a239\n\t"
    "v_accvgpr_read_b32 v51, a238\n\t"
    "v_accvgpr_read_b32 v67, a239\n\t"
    "v_accvgpr_read_b32 v52, a238\n\t"
    "v_accvgpr_read_b32 v68, a239\n\t"
    "v_accvgpr_read_b32 v53, a238\n\t"
    "v_accvgpr_read_b32 v69, a239\n\t"
    "v_accvgpr_read_b32 v54, a238\n\t"
    "v_accvgpr_read_b32 v70, a239\n\t"
    "v_accvgpr_read_b32 v55, a238\n\t"
    "v_accvgpr_read_b32 v71, a239\n\t"
    "v_accvgpr_read_b32 v56, a238\n\t"
    "v_accvgpr_read_b32 v72, a239\n\t"
    "v_accvgpr_read_b32 v57, a238\n\t"
    "v_accvgpr_read_b32 v73, a239\n\t"
    "v_accvgpr_read_b32 v58, a238\n\t"
    "v_accvgpr_read_b32 v74, a239\n\t"
    "v_accvgpr_read_b32 v59, a238\n\t"
    "v_accvgpr_read_b32 v75, a239\n\t"
    "v_accvgpr_read_b32 v60, a238\n\t"
    "v_accvgpr_read_b32 v76, a239\n\t"
    "v_accvgpr_read_b32 v61, a238\n\t"
    "v_accvgpr_read_b32 v77, a239\n\t"
    "v_accvgpr_read_b32 v62, a238\n\t"
    "v_accvgpr_read_b32 v78, a239\n\t"
    "v_accvgpr_read_b32 v63, a238\n\t"
    "v_accvgpr_read_b32 v79, a239\n\t"
    "s_waitcnt vmcnt(12)\n\t"
    "s_barrier\n\t"
    "ds_read_b128 v[192:195], %[rb] offset:0\n\t"
    "ds_read_b128 v[196:199], %[rb] offset:1024\n\t"
    "ds_read_b128 v[208:211], %[rb] offset:2048\n\t"
    "ds_read_b128 v[212:215], %[rb] offset:3072\n\t"
    "ds_read_b128 v[200:203], %[rb] offset:4096\n\t"
    "ds_read_b128 v[204:207], %[rb] offset:5120\n\t"
    "s_waitcnt lgkmcnt(4)\n\t"
    "v_mfma_f32_32x32x16_bf16 v[128:143], v[192:195], v[32:35], v[48:63]\n\t"
    "v_mfma_f32_32x32x16_bf16 v[128:143], v[196:199], v[36:39], v[128:143]\n\t"
    "; ---- 128 steps (64 key tiles x 2 query tiles): exp2 of step n | QK of n+1 | P.V and row sums of n-1\n\t"
    "v_mfma_f32_32x32x16_bf16 v[144:159], v[192:195], v[40:43], v[64:79]\n\t"
    "s_nop 7\n\t"
    "s_nop 2\n\t"
    "v_exp_f32 v128, v128\n\t"
    "v_exp_f32 v129, v129\n\t"
    "v_exp_f32 v130, v130\n\t"
    "v_mfma_f32_32x32x16_bf16 v[144:159], v[196:199], v[44:47], v[144:159]\n\t"
    "v_exp_f32 v131, v131\n\t"
    "v_exp_f32 v132, v132\n\t"
    "v_exp_f32 v133, v133\n\t"
    "v_exp_f32 v134, v134\n\t"
    "v_exp_f32 v135, v135\n\t"
    "v_exp_f32 v136, v136\n\t"
    "ds_read_b128 v[216:219], %[rb] offset:6144\n\t"
    "v_exp_f32 v137, v137\n\t"
    "v_exp_f32 v138, v138\n\t"
    "s_waitcnt lgkmcnt(1)\n\t"
    "v_exp_f32 v139, v139\n\t"
    "v_exp_f32 v140, v140\n\t"
    "v_exp_f32 v141, v141\n\t"
    "ds_read_b128 v[220:223], %[rb] offset:7168\n\t"
    "v_exp_f32 v142, v142\n\t"
    "v_exp_f32 v143, v143\n\t"
    "v_mfma_f32_32x32x16_bf16 v[160:175], v[200:203], v[32:35], v[48:63]\n\t"
    "v_exp_f32 v144, v144\n\t"
    "v_exp_f32 v145, v145\n\t"
    "v_exp_f32 v146, v146\n\t"
    "v_mfma_f32_32x32x16_bf16 v[160:175], v[204:207], v[36:39], v[160:175]\n\t"
    "v_exp_f32 v147, v147\n\t"
    "v_exp_f32 v148, v148\n\t"
    "v_exp_f32 v149, v149\n\t"
    "v_smfmac_f32_32x32x32_bf16 v[80:95], v[208:211], v[128:135], v124\n\t"
    "v_exp_f32 v150, v150\n\t"
    "v_exp_f32 v151, v151\n\t"
    "v_exp_f32 v152, v152\n\t"
    "v_smfmac_f32_16x16x64_bf16 v[112:115], v[120:123], v[128:135], v124\n\t"
    "ds_read_b128 v[192:195], %[rb] offset:8192\n\t"
    "v_exp_f32 v153, v153\n\t"
    "v_exp_f32 v154, v154\n\t"
    "v_smfmac_f32_32x32x32_bf16 v[80:95], v[212:215], v[136:143], v124\n\t"
    "global_load_dword v232, %[vp3], %[pf] sc1\n\t"
    "v_exp_f32 v155, v155\n\t"
    "v_exp_f32 v156, v156\n\t"
    "v_exp_f32 v157, v157\n\t"
    "v_smfmac_f32_16x16x64_bf16 v[112:115], v[120:123], v[136:143], v124\n\t"
    "ds_read_b128 v[196:199], %[rb] offset:9216\n\t"
    "v_exp_f32 v158, v158\n\t"
    "v_exp_f32 v159, v159\n\t"
    "v_mfma_f32_32x32x16_bf16 v[176:191], v[200:203], v[40:43], v[64:79]\n\t"
    "v_exp_f32 v160, v160\n\t"
    "v_exp_f32 v161, v161\n\t"
    "v_exp_f32 v162, v162\n\t"
    "v_mfma_f32_32x32x16_bf16 v[176:191], v[204:207], v[44:47], v[176:191]\n\t"
    "v_exp_f32 v163, v163\n\t"
    "v_exp_f32 v164, v164\n\t"
    "v_exp_f32 v165, v165\n\t"
    "v_smfmac_f32_32x32x32_bf16 v[96:111], v[208:211], v[144:151], v124\n\t"
    "v_exp_f32 v166, v166\n\t"
    "v_exp_f32 v167, v167\n\t"
    "v_exp_f32 v168, v168\n\t"
    "v_smfmac_f32_16x16x64_bf16 v[116:119], v[120:123], v[144:151], v124\n\t"
    "ds_read_b128 v[208:211], %[rb] offset:10240\n\t"
    "v_exp_f32 v169, v169\n\t"
    "v_exp_f32 v170, v170\n\t"
    "v_smfmac_f32_32x32x32_bf16 v[96:111], v[212:215], v[152:159], v124\n\t"
    "s_waitcnt lgkmcnt(1)\n\t"
    "v_exp_f32 v171, v171\n\t"
    "v_exp_f32 v172, v172\n\t"
    "v_exp_f32 v173, v173\n\t"
    "v_smfmac_f32_16x16x64_bf16 v[116:119], v[120:123], v[152:159], v124\n\t"
    "ds_read_b128 v[212:215], %[rb] offset:11264\n\t"
    "v_exp_f32 v174, v174\n\t"
    "v_exp_f32 v175, v175\n\t"
    "v_mfma_f32_32x32x16_bf16 v[128:143], v[192:195], v[32:35], v[48:63]\n\t"
    "v_exp_f32 v176, v176\n\t"
    "v_exp_f32 v177, v177\n\t"
    "v_exp_f32 v178, v178\n\t"
    "v_mfma_f32_32x32x16_bf16 v[128:143], v[196:199], v[36:39], v[128:143]\n\t"
    "v_exp_f32 v179, v179\n\t"
    "v_exp_f32 v180, v180\n\t"
    "v_exp_f32 v181, v181\n\t"
    "v_smfmac_f32_32x32x32_bf16 v[80:95], v[216:219], v[160:167], v124\n\t"
    "v_exp_f32 v182, v182\n\t"
    "v_exp_f32 v183, v183\n\t"
    "v_exp_f32 v184, v184\n\t"
    "v_smfmac_f32_16x16x64_bf16 v[112:115], v[120:123], v[160:167], v124\n\t"
    "ds_read_b128 v[200:203], %[rb] offset:12288\n\t"
    "v_exp_f32 v185, v185\n\t"
    "v_exp_f32 v186, v186\n\t"
    "v_smfmac_f32_32x32x32_bf16 v[80:95], v[220:223], v[168:175], v124\n\t"
    "v_exp_f32 v187, v187\n\t"
    "v_exp_f32 v188, v188\n\t"
    "v_exp_f32 v189, v189\n\t"
    "v_smfmac_f32_16x16x64_bf16 v[112:115], v[120:123], v[168:175], v124\n\t"
    "ds_read_b128 v[204:207], %[rb] offset:13312\n\t"
    "v_exp_f32 v190, v190\n\t"
    "v_exp_f32 v191, v191\n\t"
    "v_mfma_f32_32x32x16_bf16 v[144:159], v[192:195], v[40:43], v[64:79]\n\t"
    "v_exp_f32 v128, v128\n\t"
    "v_exp_f32 v129, v129\n\t"
    "v_exp_f32 v130, v130\n\t"
    "v_mfma_f32_32x32x16_bf16 v[144:159], v[196:199], v[44:47], v[144:159]\n\t"
    "v_exp_f32 v131, v131\n\t"
    "v_exp_f32 v132, v132\n\t"
    "v_exp_f32 v133, v133\n\t"
    "v_smfmac_f32_32x32x32_bf16 v[96:111], v[216:219], v[176:183], v124\n\t"
    "v_exp_f32 v134, v134\n\t"
    "v_exp_f32 v135, v135\n\t"
    "v_exp_f32 v136, v136\n\t"
    "v_smfmac_f32_16x16x64_bf16 v[116:119], v[120:123], v[176:183], v124\n\t"
    "ds_read_b128 v[216:219], %[rb] offset:14336\n\t"
    "v_exp_f32 v137, v137\n\t"
    "v_exp_f32 v138, v138\n\t"
    "v_smfmac_f32_32x32x32_bf16 v[96:111], v[220:223], v[184:191], v124\n\t"
    "s_waitcnt lgkmcnt(1)\n\t"
    "v_exp_f32 v139, v139\n\t"
    "v_exp_f32 v140, v140\n\t"
    "v_exp_f32 v141, v141\n\t"
    "v_smfmac_f32_16x16x64_bf16 v[116:119], v[120:123], v[184:191], v124\n\t"
    "ds_read_b128 v[220:223], %[rb] offset:15360\n\t"
    "v_exp_f32 v142, v142\n\t"
    "v_exp_f32 v143, v143\n\t"
    "v_mfma_f32_32x32x16_bf16 v[160:175], v[200:203], v[32:35], v[48:63]\n\t"
    "v_exp_f32 v144, v144\n\t"
    "v_exp_f32 v145, v145\n\t"
    "v_exp_f32 v146, v146\n\t"
    "v_mfma_f32_32x32x16_bf16 v[160:175], v[204:207], v[36:39], v[160:175]\n\t"
    "v_exp_f32 v147, v147\n\t"
    "v_exp_f32 v148, v148\n\t"
    "v_exp_f32 v149, v149\n\t"
    "v_smfmac_f32_32x32x32_bf16 v[80:95], v[208:211], v[128:135], v124\n\t"
    "s_waitcnt vmcnt(1) lgkmcnt(0)\n\t"
    "s_barrier\n\t"
    "s_waitcnt vmcnt(0)\n\t"
    "s_mov_b32 s56, 0x40000\n\t"
    "W3P_%=:\n"
    "v_cmp_ne_u32 vcc, v232, %[tag]\n\t"
    "s_nop 4\n\t"
    "s_cbranch_vccz W3S_%=\n\t"
    "s_sleep 4\n\t"
    "global_load_dword v232, %[vp3], %[pf] sc1\n\t"
    "s_sub_u32 s56, s56, 1\n\t"
    "s_cmp_lg_u32 s56, 0\n\t"
    "s_waitcnt vmcnt(0)\n\t"
    "s_cbranch_scc1 W3P_%=\n\t"
    "W3S_%=:\n"
    "s_add_u32 s52, %[krot], 32768\n\t"
    "s_add_u32 m0, s54, 32768\n\t"
    "s_and_b32 s52, s52, 0x3ffff\n\t"
    "s_nop 0\n\t"
    "buffer_load_dwordx4 %[vg], s[40:43], s52 offen offset:0 sc1 lds\n\t"
    "buffer_load_dwordx4 %[vg], s[40:43], s52 offen offset:1024 sc1 lds\n\t"
    "v_exp_f32 v150, v150\n\t"
    "v_exp_f32 v151, v151\n\t"
    "v_exp_f32 v152, v152\n\t"
    "v_smfmac_f32_16x16x64_bf16 v[112:115], v[120:123], v[128:135], v124\n\t"
    "ds_read_b128 v[192:195], %[rb] offset:16384\n\t"
    "v_exp_f32 v153, v153\n\t"
    "v_exp_f32 v154, v154\n\t"
    "v_smfmac_f32_32x32x32_bf16 v[80:95], v[212:215], v[136:143], v124\n\t"
    "v_exp_f32 v155, v155\n\t"
    "v_exp_f32 v156, v156\n\t"
    "v_exp_f32 v157, v157\n\t"
    "v_smfmac_f32_16x16x64_bf16 v[112:115], v[120:123], v[136:143], v124\n\t"
    "ds_read_b128 v[196:199], %[rb] offset:17408\n\t"
    "v_exp_f32 v158, v158\n\t"
    "v_exp_f32 v159, v159\n\t"
    "v_mfma_f32_32x32x16_bf16 v[176:191], v[200:203], v[40:43], v[64:79]\n\t"
    "v_exp_f32 v160, v160\n\t"
    "v_exp_f32 v161, v161\n\t"
    "v_exp_f32 v162, v162\n\t"
    "v_mfma_f32_32x32x16_bf16 v[176:191], v[204:207], v[44:47], v[176:191]\n\t"
    "v_exp_f32 v163, v163\n\t"
    "v_exp_f32 v164, v164\n\t"
    "v_exp_f32 v165, v165\n\t"
    "v_smfmac_f32_32x32x32_bf16 v[96:111], v[208:211], v[144:151], v124\n\t"
    "buffer_load_dwordx4 %[vg], s[40:43], s52 offen offset:2048 sc1 lds\n\t"
    "buffer_load_dwordx4 %[vg], s[40:43], s52 offen offset:3072 sc1 lds\n\t"
    "v_exp_f32 v166, v166\n\t"
    "v_exp_f32 v167, v167\n\t"
    "v_exp_f32 v168, v168\n\t"
    "v_smfmac_f32_16x16x64_bf16 v[116:119], v[120:123], v[144:151], v124\n\t"
    "ds_read_b128 v[208:211], %[rb] offset:18432\n\t"
    "v_exp_f32 v169, v169\n\t"
    "v_exp_f32 v170, v170\n\t"
    "v_smfmac_f32_32x32x32_bf16 v[96:111], v[212:215], v[152:159], v124\n\t"
    "s_waitcnt lgkmcnt(1)\n\t"
    "v_exp_f32 v171, v171\n\t"
    "v_exp_f32 v172, v172\n\t"
    "v_exp_f32 v173, v173\n\t"
    "v_smfmac_f32_16x16x64_bf16 v[116:119], v[120:123], v[152:159], v124\n\t"
    "ds_read_b128 v[212:215], %[rb] offset:19456\n\t"
    "v_exp_f32 v174, v174\n\t"
    "v_exp_f32 v175, v175\n\t"
    "v_mfma_f32_32x32x16_bf16 v[128:143], v[192:195], v[32:35], v[48:63]\n\t"
    "s_add_u32 s52, %[krot], 49152\n\t"
    "v_exp_f32 v176, v176\n\t"
    "v_exp_f32 v177, v177\n\t"
    "v_exp_f32 v178, v178\n\t"
    "v_mfma_f32_32x32x16_bf16 v[128:143], v[196:199], v[36:39], v[128:143]\n\t"
    "s_add_u32 m0, s54, 49152\n\t"
    "s_and_b32 s52, s52, 0x3ffff\n\t"
    "v_exp_f32 v179, v179\n\t"
    "v_exp_f32 v180, v180\n\t"
    "v_exp_f32 v181, v181\n\t"
    "v_smfmac_f32_32x32x32_bf16 v[80:95], v[216:219], v[160:167], v124\n\t"
    "buffer_load_dwordx4 %[vg], s[40:43], s52 offen offset:0 sc1 lds\n\t"
    "buffer_load_dwordx4 %[vg], s[40:43], s52 offen offset:1024 sc1 lds\n\t"
    "v_exp_f32 v182, v182\n\t"
    "v_exp_f32 v183, v183\n\t"
    "v_exp_f32 v184, v184\n\t"
    "v_smfmac_f32_16x16x64_bf16 v[112:115], v[120:123], v[160:167], v124\n\t"
    "ds_read_b128 v[200:203], %[rb] offset:20480\n\t"
    "v_exp_f32 v185, v185\n\t"
    "v_exp_f32 v186, v186\n\t"
    "v_smfmac_f32_32x32x32_bf16 v[80:95], v[220:223], v[168:175], v124\n\t"
    "v_exp_f32 v187, v187\n\t"
    "v_exp_f32 v188, v188\n\t"
    "v_exp_f32 v189, v189\n\t"
    "v_smfmac_f32_16x16x64_bf16 v[112:115], v[120:123], v[168:175], v124\n\t"
    "ds_read_b128 v[204:207], %[rb] offset:21504\n\t"
    "v_exp_f32 v190, v190\n\t"
    "v_exp_f32 v191, v191\n\t"
    "v_mfma_f32_32x32x16_bf16 v[144:159], v[192:195], v[40:43], v[64:79]\n\t"
    "v_exp_f32 v128, v128\n\t"
    "v_exp_f32 v129, v129\n\t"
    "v_exp_f32 v130, v130\n\t"
    "v_mfma_f32_32x32x16_bf16 v[144:159], v[196:199], v[44:47], v[144:159]\n\t"
    "v_exp_f32 v131, v131\n\t"
    "v_exp_f32 v132, v132\n\t"
    "v_exp_f32 v133, v133\n\t"
    "v_smfmac_f32_32x32x32_bf16 v[96:111], v[216:219], v[176:183], v124\n\t"
    "buffer_load_dwordx4 %[vg], s[40:43], s52 offen offset:2048 sc1 lds\n\t"
    "buffer_load_dwordx4 %[vg], s[40:43], s52 offen offset:3072 sc1 lds\n\t"
    "v_exp_f32 v134, v134\n\t"
    "v_exp_f32 v135, v135\n\t"
    "v_exp_f32 v136, v136\n\t"
    "v_smfmac_f32_16x16x64_bf16 v[116:119], v[120:123], v[176:183], v124\n\t"
    "ds_read_b128 v[216:219], %[rb] offset:22528\n\t"
    "v_exp_f32 v137, v137\n\t"
    "v_exp_f32 v138, v138\n\t"
    "v_smfmac_f32_32x32x32_bf16 v[96:111], v[220:223], v[184:191], v124\n\t"
    "s_waitcnt lgkmcnt(1)\n\t"
    "v_exp_f32 v139, v139\n\t"
    "v_exp_f32 v140, v140\n\t"
    "v_exp_f32 v141, v141\n\t"
    "v_smfmac_f32_16x16x64_bf16 v[116:119], v[120:123], v[184:191], v124\n\t"
    "ds_read_b128 v[220:223], %[rb] offset:23552\n\t"
    "v_exp_f32 v142, v142\n\t"
    "v_exp_f32 v143, v143\n\t"
    "v_mfma_f32_32x32x16_bf16 v[160:175], v[200:203], v[32:35], v[48:63]\n\t"
    "s_add_u32 s52, %[krot], 65536\n\t"
    "v_exp_f32 v144, v144\n\t"
    "v_exp_f32 v145, v145\n\t"
    "v_exp_f32 v146, v146\n\t"
    "v_mfma_f32_32x32x16_bf16 v[160:175], v[204:207], v[36:39], v[160:175]\n\t"
    "s_add_u32 m0, s54, 0\n\t"
    "s_and_b32 s52, s52, 0x3ffff\n\t"
    "v_exp_f32 v147, v147\n\t"
    "v_exp_f32 v148, v148\n\t"
    "v_exp_f32 v149, v149\n\t"
    "v_smfmac_f32_32x32x32_bf16 v[80:95], v[208:211], v[128:135], v124\n\t"
    "buffer_load_dwordx4 %[vg], s[40:43], s52 offen offset:0 sc1 lds\n\t"
    "buffer_load_dwordx4 %[vg], s[40:43], s52 offen offset:1024 sc1 lds\n\t"
    "v_exp_f32 v150, v150\n\t"
    "v_exp_f32 v151, v151\n\t"
    "v_exp_f32 v152, v152\n\t"
    "v_smfmac_f32_16x16x64_bf16 v[112:115], v[120:123], v[128:135], v124\n\t"
    "ds_read_b128 v[192:195], %[rb] offset:24576\n\t"
    "v_exp_f32 v153, v153\n\t"
    "v_exp_f32 v154, v154\n\t"
    "v_smfmac_f32_32x32x32_bf16 v[80:95], v[212:215], v[136:143], v124\n\t"
    "v_exp_f32 v155, v155\n\t"
    "v_exp_f32 v156, v156\n\t"
    "v_exp_f32 v157, v157\n\t"
    "v_smfmac_f32_16x16x64_bf16 v[112:115], v[120:123], v[136:143], v124\n\t"
    "ds_read_b128 v[196:199], %[rb] offset:25600\n\t"
    "v_exp_f32 v158, v158\n\t"
    "v_exp_f32 v159, v159\n\t"
    "v_mfma_f32_32x32x16_bf16 v[176:191], v[200:203], v[40:43], v[64:79]\n\t"
    "v_exp_f32 v160, v160\n\t"
    "v_exp_f32 v161, v161\n\t"
    "v_exp_f32 v162, v162\n\t"
    "v_mfma_f32_32x32x16_bf16 v[176:191], v[204:207], v[44:47], v[176:191]\n\t"
    "v_exp_f32 v163, v163\n\t"
    "v_exp_f32 v164, v164\n\t"
    "v_exp_f32 v165, v165\n\t"
    "v_smfmac_f32_32x32x32_bf16 v[96:111], v[208:211], v[144:151], v124\n\t"
    "buffer_load_dwordx4 %[vg], s[40:43], s52 offen offset:2048 sc1 lds\n\t"
    "buffer_load_dwordx4 %[vg], s[40:43], s52 offen offset:3072 sc1 lds\n\t"
    "v_exp_f32 v166, v166\n\t"
    "v_exp_f32 v167, v167\n\t"
    "v_exp_f32 v168, v168\n\t"
    "v_smfmac_f32_16x16x64_bf16 v[116:119], v[120:123], v[144:151], v124\n\t"
    "ds_read_b128 v[208:211], %[rb] offset:26624\n\t"
    "v_exp_f32 v169, v169\n\t"
    "v_exp_f32 v170, v170\n\t"
    "v_smfmac_f32_32x32x32_bf16 v[96:111], v[212:215], v[152:159], v124\n\t"
    "s_waitcnt lgkmcnt(1)\n\t"
    "v_exp_f32 v171, v171\n\t"
    "v_exp_f32 v172, v172\n\t"
    "v_exp_f32 v173, v173\n\t"
    "v_smfmac_f32_16x16x64_bf16 v[116:119], v[120:123], v[152:159], v124\n\t"
    "ds_read_b128 v[212:215], %[rb] offset:27648\n\t"
    "v_exp_f32 v174, v174\n\t"
    "v_exp_f32 v175, v175\n\t"
    "v_mfma_f32_32x32x16_bf16 v[128:143], v[192:195], v[32:35], v[48:63]\n\t"
    "s_add_u32 m0, s54, 65536\n\t"
    "v_exp_f32 v176, v176\n\t"
    "v_exp_f32 v177, v177\n\t"
    "v_exp_f32 v178, v178\n\t"
    "v_mfma_f32_32x32x16_bf16 v[128:143], v[196:199], v[36:39], v[128:143]\n\t"
    "s_mov_b32 s53, 0\n\t"
    "v_exp_f32 v179, v179\n\t"
    "v_exp_f32 v180, v180\n\t"
    "v_exp_f32 v181, v181\n\t"
    "v_smfmac_f32_32x32x32_bf16 v[80:95], v[216:219], v[160:167], v124\n\t"
    "buffer_load_dwordx4 v224, s[44:47], s53 offen offset:0 lds\n\t"
    "buffer_load_dwordx4 v225, s[44:47], s53 offen offset:1024 lds\n\t"
    "buffer_load_dwordx4 v226, s[44:47], s53 offen offset:2048 lds\n\t"
    "buffer_load_dwordx4 v227, s[44:47], s53 offen offset:3072 lds\n\t"
    "v_exp_f32 v182, v182\n\t"
    "v_exp_f32 v183, v183\n\t"
    "v_exp_f32 v184, v184\n\t"
    "v_smfmac_f32_16x16x64_bf16 v[112:115], v[120:123], v[160:167], v124\n\t"
    "ds_read_b128 v[200:203], %[rb] offset:28672\n\t"
    "v_exp_f32 v185, v185\n\t"
    "v_exp_f32 v186, v186\n\t"
    "v_smfmac_f32_32x32x32_bf16 v[80:95], v[220:223], v[168:175], v124\n\t"
    "v_exp_f32 v187, v187\n\t"
    "v_exp_f32 v188, v188\n\t"
    "v_exp_f32 v189, v189\n\t"
    "v_smfmac_f32_16x16x64_bf16 v[112:115], v[120:123], v[168:175], v124\n\t"
    "ds_read_b128 v[204:207], %[rb] offset:29696\n\t"
    "v_exp_f32 v190, v190\n\t"
    "v_exp_f32 v191, v191\n\t"
    "v_mfma_f32_32x32x16_bf16 v[144:159], v[192:195], v[40:43], v[64:79]\n\t"
    "s_add_u32 m0, s54, 81920\n\t"
    "v_exp_f32 v128, v128\n\t"
    "v_exp_f32 v129, v129\n\t"
    "v_exp_f32 v130, v130\n\t"
    "v_mfma_f32_32x32x16_bf16 v[144:159], v[196:199], v[44:47], v[144:159]\n\t"
    "s_mov_b32 s53, 262144\n\t"
    "v_exp_f32 v131, v131\n\t"
    "v_exp_f32 v132, v132\n\t"
    "v_exp_f32 v133, v133\n\t"
    "v_smfmac_f32_32x32x32_bf16 v[96:111], v[216:219], v[176:183], v124\n\t"
    "buffer_load_dwordx4 v224, s[44:47], s53 offen offset:0 lds\n\t"
    "buffer_load_dwordx4 v225, s[44:47], s53 offen offset:1024 lds\n\t"
    "buffer_load_dwordx4 v226, s[44:47], s53 offen offset:2048 lds\n\t"
    "buffer_load_dwordx4 v227, s[44:47], s53 offen offset:3072 lds\n\t"
    "v_exp_f32 v134, v134\n\t"
    "v_exp_f32 v135, v135\n\t"
    "v_exp_f32 v136, v136\n\t"
    "v_smfmac_f32_16x16x64_bf16 v[116:119], v[120:123], v[176:183], v124\n\t"
    "ds_read_b128 v[216:219], %[rb] offset:30720\n\t"
    "v_exp_f32 v137, v137\n\t"
    "v_exp_f32 v138, v138\n\t"
    "v_smfmac_f32_32x32x32_bf16 v[96:111], v[220:223], v[184:191], v124\n\t"
    "s_waitcnt lgkmcnt(1)\n\t"
    "v_exp_f32 v139, v139\n\t"
    "v_exp_f32 v140, v140\n\t"
    "v_exp_f32 v141, v141\n\t"
    "v_smfmac_f32_16x16x64_bf16 v[116:119], v[120:123], v[184:191], v124\n\t"
    "ds_read_b128 v[220:223], %[rb] offset:31744\n\t"
    "v_exp_f32 v142, v142\n\t"
    "v_exp_f32 v143, v143\n\t"
    "v_mfma_f32_32x32x16_bf16 v[160:175], v[200:203], v[32:35], v[48:63]\n\t"
    "v_exp_f32 v144, v144\n\t"
    "v_exp_f32 v145, v145\n\t"
    "v_exp_f32 v146, v146\n\t"
    "v_mfma_f32_32x32x16_bf16 v[160:175], v[204:207], v[36:39], v[160:175]\n\t"
    "v_exp_f32 v147, v147\n\t"
    "v_exp_f32 v148, v148\n\t"
    "v_exp_f32 v149, v149\n\t"
    "v_smfmac_f32_32x32x32_bf16 v[80:95], v[208:211], v[128:135], v124\n\t"
    "s_waitcnt vmcnt(16) lgkmcnt(0)\n\t"
    "s_barrier\n\t"
    "s_mov_b64 exec, 1\n\t"
    "global_store_dword %[vf1], %[tag], %[pf] sc1\n\t"
    "s_mov_b64 exec, -1\n\t"
    "v_exp_f32 v150, v150\n\t"
    "v_exp_f32 v151, v151\n\t"
    "v_exp_f32 v152, v152\n\t"
    "v_smfmac_f32_16x16x64_bf16 v[112:115], v[120:123], v[128:135], v124\n\t"
    "ds_read_b128 v[192:195], %[rb] offset:32768\n\t"
    "v_exp_f32 v153, v153\n\t"
    "v_exp_f32 v154, v154\n\t"
    "v_smfmac_f32_32x32x32_bf16 v[80:95], v[212:215], v[136:143], v124\n\t"
    "v_exp_f32 v155, v155\n\t"
    "v_exp_f32 v156, v156\n\t"
    "v_exp_f32 v157, v157\n\t"
    "v_smfmac_f32_16x16x64_bf16 v[112:115], v[120:123], v[136:143], v124\n\t"
    "ds_read_b128 v[196:199], %[rb] offset:33792\n\t"
    "v_exp_f32 v158, v158\n\t"
    "v_exp_f32 v159, v159\n\t"
    "v_mfma_f32_32x32x16_bf16 v[176:191], v[200:203], v[40:43], v[64:79]\n\t"
    "v_exp_f32 v160, v160\n\t"
    "v_exp_f32 v161, v161\n\t"
    "v_exp_f32 v162, v162\n\t"
    "v_mfma_f32_32x32x16_bf16 v[176:191], v[204:207], v[44:47], v[176:191]\n\t"
    "v_exp_f32 v163, v163\n\t"
    "v_exp_f32 v164, v164\n\t"
    "v_exp_f32 v165, v165\n\t"
    "v_smfmac_f32_32x32x32_bf16 v[96:111], v[208:211], v[144:151], v124\n\t"
    "v_exp_f32 v166, v166\n\t"
    "v_exp_f32 v167, v167\n\t"
    "v_exp_f32 v168, v168\n\t"
    "v_smfmac_f32_16x16x64_bf16 v[116:119], v[120:123], v[144:151], v124\n\t"
    "ds_read_b128 v[208:211], %[rb] offset:34816\n\t"
    "v_exp_f32 v169, v169\n\t"
    "v_exp_f32 v170, v170\n\t"
    "v_smfmac_f32_32x32x32_bf16 v[96:111], v[212:215], v[152:159], v124\n\t"
    "s_waitcnt lgkmcnt(1)\n\t"
    "v_exp_f32 v171, v171\n\t"
    "v_exp_f32 v172, v172\n\t"
    "v_exp_f32 v173, v173\n\t"
    "v_smfmac_f32_16x16x64_bf16 v[116:119], v[120:123], v[152:159], v124\n\t"
    "ds_read_b128 v[212:215], %[rb] offset:35840\n\t"
    "v_exp_f32 v174, v174\n\t"
    "v_exp_f32 v175, v175\n\t"
    "v_mfma_f32_32x32x16_bf16 v[128:143], v[192:195], v[32:35], v[48:63]\n\t"
    "s_add_u32 s52, %[krot], 81920\n\t"
    "v_exp_f32 v176, v176\n\t"
    "v_exp_f32 v177, v177\n\t"
    "v_exp_f32 v178, v178\n\t"
    "v_mfma_f32_32x32x16_bf16 v[128:143], v[196:199], v[36:39], v[128:143]\n\t"
    "s_add_u32 m0, s54, 16384\n\t"
    "s_and_b32 s52, s52, 0x3ffff\n\t"
    "v_exp_f32 v179, v179\n\t"
    "v_exp_f32 v180, v180\n\t"
    "v_exp_f32 v181, v181\n\t"
    "v_smfmac_f32_32x32x32_bf16 v[80:95], v[216:219], v[160:167], v124\n\t"
    "buffer_load_dwordx4 %[vg], s[40:43], s52 offen offset:0 sc1 lds\n\t"
    "buffer_load_dwordx4 %[vg], s[40:43], s52 offen offset:1024 sc1 lds\n\t"
    "v_exp_f32 v182, v182\n\t"
    "v_exp_f32 v183, v183\n\t"
    "v_exp_f32 v184, v184\n\t"
    "v_smfmac_f32_16x16x64_bf16 v[112:115], v[120:123], v[160:167], v124\n\t"
    "ds_read_b128 v[200:203], %[rb] offset:36864\n\t"
    "v_exp_f32 v185, v185\n\t"
    "v_exp_f32 v186, v186\n\t"
    "v_smfmac_f32_32x32x32_bf16 v[80:95], v[220:223], v[168:175], v124\n\t"
    "v_exp_f32 v187, v187\n\t"
    "v_exp_f32 v188, v188\n\t"
    "v_exp_f32 v189, v189\n\t"
    "v_smfmac_f32_16x16x64_bf16 v[112:115], v[120:123], v[168:175], v124\n\t"
    "ds_read_b128 v[204:207], %[rb] offset:37888\n\t"
    "v_exp_f32 v190, v190\n\t"
    "v_exp_f32 v191, v191\n\t"
    "v_mfma_f32_32x32x16_bf16 v[144:159], v[192:195], v[40:43], v[64:79]\n\t"
    "v_exp_f32 v128, v128\n\t"
    "v_exp_f32 v129, v129\n\t"
    "v_exp_f32 v130, v130\n\t"
    "v_mfma_f32_32x32x16_bf16 v[144:159], v[196:199], v[44:47], v[144:159]\n\t"
    "v_exp_f32 v131, v131\n\t"
    "v_exp_f32 v132, v132\n\t"
    "v_exp_f32 v133, v133\n\t"
    "v_smfmac_f32_32x32x32_bf16 v[96:111], v[216:219], v[176:183], v124\n\t"
    "buffer_load_dwordx4 %[vg], s[40:43], s52 offen offset:2048 sc1 lds\n\t"
    "buffer_load_dwordx4 %[vg], s[40:43], s52 offen offset:3072 sc1 lds\n\t"
    "v_exp_f32 v134, v134\n\t"
    "v_exp_f32 v135, v135\n\t"
    "v_exp_f32 v136, v136\n\t"
    "v_smfmac_f32_16x16x64_bf16 v[116:119], v[120:123], v[176:183], v124\n\t"
    "ds_read_b128 v[216:219], %[rb] offset:38912\n\t"
    "v_exp_f32 v137, v137\n\t"
    "v_exp_f32 v138, v138\n\t"
    "v_smfmac_f32_32x32x32_bf16 v[96:111], v[220:223], v[184:191], v124\n\t"
    "s_waitcnt lgkmcnt(1)\n\t"
    "v_exp_f32 v139, v139\n\t"
    "v_exp_f32 v140, v140\n\t"
    "v_exp_f32 v141, v141\n\t"
    "v_smfmac_f32_16x16x64_bf16 v[116:119], v[120:123], v[184:191], v124\n\t"
    "ds_read_b128 v[220:223], %[rb] offset:39936\n\t"
    "v_exp_f32 v142, v142\n\t"
    "v_exp_f32 v143, v143\n\t"
    "v_mfma_f32_32x32x16_bf16 v[160:175], v[200:203], v[32:35], v[48:63]\n\t"
    "s_add_u32 m0, s54, 98304\n\t"
    "v_exp_f32 v144, v144\n\t"
    "v_exp_f32 v145, v145\n\t"
    "v_exp_f32 v146, v146\n\t"
    "v_mfma_f32_32x32x16_bf16 v[160:175], v[204:207], v[36:39], v[160:175]\n\t"
    "s_mov_b32 s53, 524288\n\t"
    "v_exp_f32 v147, v147\n\t"
    "v_exp_f32 v148, v148\n\t"
    "v_exp_f32 v149, v149\n\t"
    "v_smfmac_f32_32x32x32_bf16 v[80:95], v[208:211], v[128:135], v124\n\t"
    "buffer_load_dwordx4 v224, s[44:47], s53 offen offset:0 lds\n\t"
    "buffer_load_dwordx4 v225, s[44:47], s53 offen offset:1024 lds\n\t"
    "v_exp_f32 v150, v150\n\t"
    "v_exp_f32 v151, v151\n\t"
    "v_exp_f32 v152, v152\n\t"
    "v_smfmac_f32_16x16x64_bf16 v[112:115], v[120:123], v[128:135], v124\n\t"
    "ds_read_b128 v[192:195], %[rb] offset:40960\n\t"
    "v_exp_f32 v153, v153\n\t"
    "v_exp_f32 v154, v154\n\t"
    "v_smfmac_f32_32x32x32_bf16 v[80:95], v[212:215], v[136:143], v124\n\t"
    "v_exp_f32 v155, v155\n\t"
    "v_exp_f32 v156, v156\n\t"
    "v_exp_f32 v157, v157\n\t"
    "v_smfmac_f32_16x16x64_bf16 v[112:115], v[120:123], v[136:143], v124\n\t"
    "ds_read_b128 v[196:199], %[rb] offset:41984\n\t"
    "v_exp_f32 v158, v158\n\t"
    "v_exp_f32 v159, v159\n\t"
    "v_mfma_f32_32x32x16_bf16 v[176:191], v[200:203], v[40:43], v[64:79]\n\t"
    "v_exp_f32 v160, v160\n\t"
    "v_exp_f32 v161, v161\n\t"
    "v_exp_f32 v162, v162\n\t"
    "v_mfma_f32_32x32x16_bf16 v[176:191], v[204:207], v[44:47], v[176:191]\n\t"
    "v_exp_f32 v163, v163\n\t"
    "v_exp_f32 v164, v164\n\t"
    "v_exp_f32 v165, v165\n\t"
    "v_smfmac_f32_32x32x32_bf16 v[96:111], v[208:211], v[144:151], v124\n\t"
    "buffer_load_dwordx4 v226, s[44:47], s53 offen offset:2048 lds\n\t"
    "buffer_load_dwordx4 v227, s[44:47], s53 offen offset:3072 lds\n\t"
    "v_exp_f32 v166, v166\n\t"
    "v_exp_f32 v167, v167\n\t"
    "v_exp_f32 v168, v168\n\t"
    "v_smfmac_f32_16x16x64_bf16 v[116:119], v[120:123], v[144:151], v124\n\t"
    "ds_read_b128 v[208:211], %[rb] offset:43008\n\t"
    "v_exp_f32 v169, v169\n\t"
    "v_exp_f32 v170, v170\n\t"
    "v_smfmac_f32_32x32x32_bf16 v[96:111], v[212:215], v[152:159], v124\n\t"
    "s_waitcnt lgkmcnt(1)\n\t"
    "v_exp_f32 v171, v171\n\t"
    "v_exp_f32 v172, v172\n\t"
    "v_exp_f32 v173, v173\n\t"
    "v_smfmac_f32_16x16x64_bf16 v[116:119], v[120:123], v[152:159], v124\n\t"
    "ds_read_b128 v[212:215], %[rb] offset:44032\n\t"
    "v_exp_f32 v174, v174\n\t"
    "v_exp_f32 v175, v175\n\t"
    "v_mfma_f32_32x32x16_bf16 v[128:143], v[192:195], v[32:35], v[48:63]\n\t"
    "s_add_u32 m0, s54, 114688\n\t"
    "v_exp_f32 v176, v176\n\t"
    "v_exp_f32 v177, v177\n\t"
    "v_exp_f32 v178, v178\n\t"
    "v_mfma_f32_32x32x16_bf16 v[128:143], v[196:199], v[36:39], v[128:143]\n\t"
    "s_mov_b32 s53, 786432\n\t"
    "v_exp_f32 v179, v179\n\t"
    "v_exp_f32 v180, v180\n\t"
    "v_exp_f32 v181, v181\n\t"
    "v_smfmac_f32_32x32x32_bf16 v[80:95], v[216:219], v[160:167], v124\n\t"
    "buffer_load_dwordx4 v224, s[44:47], s53 offen offset:0 lds\n\t"
    "buffer_load_dwordx4 v225, s[44:47], s53 offen offset:1024 lds\n\t"
    "v_exp_f32 v182, v182\n\t"
    "v_exp_f32 v183, v183\n\t"
    "v_exp_f32 v184, v184\n\t"
    "v_smfmac_f32_16x16x64_bf16 v[112:115], v[120:123], v[160:167], v124\n\t"
    "ds_read_b128 v[200:203], %[rb] offset:45056\n\t"
    "v_exp_f32 v185, v185\n\t"
    "v_exp_f32 v186, v186\n\t"
    "v_smfmac_f32_32x32x32_bf16 v[80:95], v[220:223], v[168:175], v124\n\t"
    "v_exp_f32 v187, v187\n\t"
    "v_exp_f32 v188, v188\n\t"
    "v_exp_f32 v189, v189\n\t"
    "v_smfmac_f32_16x16x64_bf16 v[112:115], v[120:123], v[168:175], v124\n\t"
    "ds_read_b128 v[204:207], %[rb] offset:46080\n\t"
    "v_exp_f32 v190, v190\n\t"
    "v_exp_f32 v191, v191\n\t"
    "v_mfma_f32_32x32x16_bf16 v[144:159], v[192:195], v[40:43], v[64:79]\n\t"
    "v_exp_f32 v128, v128\n\t"
    "v_exp_f32 v129, v129\n\t"
    "v_exp_f32 v130, v130\n\t"
    "v_mfma_f32_32x32x16_bf16 v[144:159], v[196:199], v[44:47], v[144:159]\n\t"
    "v_exp_f32 v131, v131\n\t"
    "v_exp_f32 v132, v132\n\t"
    "v_exp_f32 v133, v133\n\t"
    "v_smfmac_f32_32x32x32_bf16 v[96:111], v[216:219], v[176:183], v124\n\t"
    "buffer_load_dwordx4 v226, s[44:47], s53 offen offset:2048 lds\n\t"
    "buffer_load_dwordx4 v227, s[44:47], s53 offen offset:3072 lds\n\t"
    "v_exp_f32 v134, v134\n\t"
    "v_exp_f32 v135, v135\n\t"
    "v_exp_f32 v136, v136\n\t"
    "v_smfmac_f32_16x16x64_bf16 v[116:119], v[120:123], v[176:183], v124\n\t"
    "ds_read_b128 v[216:219], %[rb] offset:47104\n\t"
    "v_exp_f32 v137, v137\n\t"
    "v_exp_f32 v138, v138\n\t"
    "v_smfmac_f32_32x32x32_bf16 v[96:111], v[220:223], v[184:191], v124\n\t"
    "s_waitcnt lgkmcnt(1)\n\t"
    "v_exp_f32 v139, v139\n\t"
    "v_exp_f32 v140, v140\n\t"
    "v_exp_f32 v141, v141\n\t"
    "v_smfmac_f32_16x16x64_bf16 v[116:119], v[120:123], v[184:191], v124\n\t"
    "ds_read_b128 v[220:223], %[rb] offset:48128\n\t"
    "v_exp_f32 v142, v142\n\t"
    "v_exp_f32 v143, v143\n\t"
    "v_mfma_f32_32x32x16_bf16 v[160:175], v[200:203], v[32:35], v[48:63]\n\t"
    "v_exp_f32 v144, v144\n\t"
    "v_exp_f32 v145, v145\n\t"
    "v_exp_f32 v146, v146\n\t"
    "v_mfma_f32_32x32x16_bf16 v[160:175], v[204:207], v[36:39], v[160:175]\n\t"
    "v_exp_f32 v147, v147\n\t"
    "v_exp_f32 v148, v148\n\t"
    "v_exp_f32 v149, v149\n\t"
    "v_smfmac_f32_32x32x32_bf16 v[80:95], v[208:211], v[128:135], v124\n\t"
    "s_waitcnt vmcnt(12) lgkmcnt(0)\n\t"
    "s_barrier\n\t"
    "v_exp_f32 v150, v150\n\t"
    "v_exp_f32 v151, v151\n\t"
    "v_exp_f32 v152, v152\n\t"
    "v_smfmac_f32_16x16x64_bf16 v[112:115], v[120:123], v[128:135], v124\n\t"
    "ds_read_b128 v[192:195], %[rb] offset:49152\n\t"
    "v_exp_f32 v153, v153\n\t"
    "v_exp_f32 v154, v154\n\t"
    "v_smfmac_f32_32x32x32_bf16 v[80:95], v[212:215], v[136:143], v124\n\t"
    "v_exp_f32 v155, v155\n\t"
    "v_exp_f32 v156, v156\n\t"
    "v_exp_f32 v157, v157\n\t"
    "v_smfmac_f32_16x16x64_bf16 v[112:115], v[120:123], v[136:143], v124\n\t"
    "ds_read_b128 v[196:199], %[rb] offset:50176\n\t"
    "v_exp_f32 v158, v158\n\t"
    "v_exp_f32 v159, v159\n\t"
    "v_mfma_f32_32x32x16_bf16 v[176:191], v[200:203], v[40:43], v[64:79]\n\t"
    "ds_read_b64_tr_b16 a[0:1], v228 offset:0\n\t"
    "ds_read_b64_tr_b16 a[2:3], v228 offset:2048\n\t"
    "s_add_u32 s52, %[krot], 98304\n\t"
    "v_exp_f32 v160, v160\n\t"
    "v_exp_f32 v161, v161\n\t"
    "v_exp_f32 v162, v162\n\t"
    "v_mfma_f32_32x32x16_bf16 v[176:191], v[204:207], v[44:47], v[176:191]\n\t"
    "s_add_u32 m0, s54, 32768\n\t"
    "s_and_b32 s52, s52, 0x3ffff\n\t"
    "v_exp_f32 v163, v163\n\t"
    "v_exp_f32 v164, v164\n\t"
    "v_exp_f32 v165, v165\n\t"
    "v_smfmac_f32_32x32x32_bf16 v[96:111], v[208:211], v[144:151], v124\n\t"
    "buffer_load_dwordx4 %[vg], s[40:43], s52 offen offset:0 sc1 lds\n\t"
    "buffer_load_dwordx4 %[vg], s[40:43], s52 offen offset:1024 sc1 lds\n\t"
    "v_exp_f32 v166, v166\n\t"
    "v_exp_f32 v167, v167\n\t"
    "v_exp_f32 v168, v168\n\t"
    "v_smfmac_f32_16x16x64_bf16 v[116:119], v[120:123], v[144:151], v124\n\t"
    "ds_read_b128 v[208:211], %[rb] offset:51200\n\t"
    "v_exp_f32 v169, v169\n\t"
    "v_exp_f32 v170, v170\n\t"
    "v_smfmac_f32_32x32x32_bf16 v[96:111], v[212:215], v[152:159], v124\n\t"
    "s_waitcnt lgkmcnt(3)\n\t"
    "v_exp_f32 v171, v171\n\t"
    "v_exp_f32 v172, v172\n\t"
    "v_exp_f32 v173, v173\n\t"
    "v_smfmac_f32_16x16x64_bf16 v[116:119], v[120:123], v[152:159], v124\n\t"
    "ds_read_b128 v[212:215], %[rb] offset:52224\n\t"
    "v_exp_f32 v174, v174\n\t"
    "v_exp_f32 v175, v175\n\t"
    "v_mfma_f32_32x32x16_bf16 v[128:143], v[192:195], v[32:35], v[48:63]\n\t"
    "ds_read_b64_tr_b16 a[4:5], v229 offset:0\n\t"
    "ds_read_b64_tr_b16 a[6:7], v229 offset:2048\n\t"
    "v_exp_f32 v176, v176\n\t"
    "v_exp_f32 v177, v177\n\t"
    "v_exp_f32 v178, v178\n\t"
    "v_mfma_f32_32x32x16_bf16 v[128:143], v[196:199], v[36:39], v[128:143]\n\t"
    "v_exp_f32 v179, v179\n\t"
    "v_exp_f32 v180, v180\n\t"
    "v_exp_f32 v181, v181\n\t"
    "v_smfmac_f32_32x32x32_bf16 v[80:95], v[216:219], v[160:167], v124\n\t"
    "v_exp_f32 v182, v182\n\t"
    "v_exp_f32 v183, v183\n\t"
    "v_exp_f32 v184, v184\n\t"
    "v_smfmac_f32_16x16x64_bf16 v[112:115], v[120:123], v[160:167], v124\n\t"
    "ds_read_b128 v[200:203], %[rb] offset:53248\n\t"
    "v_exp_f32 v185, v185\n\t"
    "v_exp_f32 v186, v186\n\t"
    "v_smfmac_f32_32x32x32_bf16 v[80:95], v[220:223], v[168:175], v124\n\t"
    "v_exp_f32 v187, v187\n\t"
    "v_exp_f32 v188, v188\n\t"
    "v_exp_f32 v189, v189\n\t"
    "v_smfmac_f32_16x16x64_bf16 v[112:115], v[120:123], v[168:175], v124\n\t"
    "ds_read_b128 v[204:207], %[rb] offset:54272\n\t"
    "v_exp_f32 v190, v190\n\t"
    "v_exp_f32 v191, v191\n\t"
    "v_mfma_f32_32x32x16_bf16 v[144:159], v[192:195], v[40:43], v[64:79]\n\t"
    "ds_read_b64_tr_b16 a[8:9], v228 offset:4096\n\t"
    "ds_read_b64_tr_b16 a[10:11], v228 offset:6144\n\t"
    "v_exp_f32 v128, v128\n\t"
    "v_exp_f32 v129, v129\n\t"
    "v_exp_f32 v130, v130\n\t"
    "v_mfma_f32_32x32x16_bf16 v[144:159], v[196:199], v[44:47], v[144:159]\n\t"
    "v_exp_f32 v131, v131\n\t"
    "v_exp_f32 v132, v132\n\t"
    "v_exp_f32 v133, v133\n\t"
    "v_smfmac_f32_32x32x32_bf16 v[96:111], v[216:219], v[176:183], v124\n\t"
    "buffer_load_dwordx4 %[vg], s[40:43], s52 offen offset:2048 sc1 lds\n\t"
    "buffer_load_dwordx4 %[vg], s[40:43], s52 offen offset:3072 sc1 lds\n\t"
    "v_exp_f32 v134, v134\n\t"
    "v_exp_f32 v135, v135\n\t"
    "v_exp_f32 v136, v136\n\t"
    "v_smfmac_f32_16x16x64_bf16 v[116:119], v[120:123], v[176:183], v124\n\t"
    "ds_read_b128 v[216:219], %[rb] offset:55296\n\t"
    "v_exp_f32 v137, v137\n\t"
    "v_exp_f32 v138, v138\n\t"
    "v_smfmac_f32_32x32x32_bf16 v[96:111], v[220:223], v[184:191], v124\n\t"
    "s_waitcnt lgkmcnt(3)\n\t"
    "global_load_dwordx4 v[250:253], v248, s[62:63]\n\t"
    "v_exp_f32 v139, v139\n\t"
    "v_exp_f32 v140, v140\n\t"
    "v_exp_f32 v141, v141\n\t"
    "v_smfmac_f32_16x16x64_bf16 v[116:119], v[120:123], v[184:191], v124\n\t"
    "ds_read_b128 v[220:223], %[rb] offset:56320\n\t"
    "v_exp_f32 v142, v142\n\t"
    "v_exp_f32 v143, v143\n\t"
    "v_mfma_f32_32x32x16_bf16 v[160:175], v[200:203], v[32:35], v[48:63]\n\t"
    "ds_read_b64_tr_b16 a[12:13], v229 offset:4096\n\t"
    "ds_read_b64_tr_b16 a[14:15], v229 offset:6144\n\t"
    "ds_read_b64_tr_b16 a[16:17], v228 offset:16384\n\t"
    "v_exp_f32 v144, v144\n\t"
    "v_exp_f32 v145, v145\n\t"
    "v_exp_f32 v146, v146\n\t"
    "v_mfma_f32_32x32x16_bf16 v[160:175], v[204:207], v[36:39], v[160:175]\n\t"
    "v_exp_f32 v147, v147\n\t"
    "v_exp_f32 v148, v148\n\t"
    "v_exp_f32 v149, v149\n\t"
    "v_smfmac_f32_32x32x32_bf16 v[80:95], v[208:211], v[128:135], v124\n\t"
    "v_exp_f32 v150, v150\n\t"
    "v_exp_f32 v151, v151\n\t"
    "v_exp_f32 v152, v152\n\t"
    "v_smfmac_f32_16x16x64_bf16 v[112:115], v[120:123], v[128:135], v124\n\t"
    "ds_read_b128 v[192:195], %[rb] offset:57344\n\t"
    "v_exp_f32 v153, v153\n\t"
    "v_exp_f32 v154, v154\n\t"
    "v_smfmac_f32_32x32x32_bf16 v[80:95], v[212:215], v[136:143], v124\n\t"
    "v_exp_f32 v155, v155\n\t"
    "v_exp_f32 v156, v156\n\t"
    "v_exp_f32 v157, v157\n\t"
    "v_smfmac_f32_16x16x64_bf16 v[112:115], v[120:123], v[136:143], v124\n\t"
    "ds_read_b128 v[196:199], %[rb] offset:58368\n\t"
    "v_exp_f32 v158, v158\n\t"
    "v_exp_f32 v159, v159\n\t"
    "v_mfma_f32_32x32x16_bf16 v[176:191], v[200:203], v[40:43], v[64:79]\n\t"
    "ds_read_b64_tr_b16 a[18:19], v228 offset:18432\n\t"
    "ds_read_b64_tr_b16 a[20:21], v229 offset:16384\n\t"
    "v_exp_f32 v160, v160\n\t"
    "v_exp_f32 v161, v161\n\t"
    "v_exp_f32 v162, v162\n\t"
    "v_mfma_f32_32x32x16_bf16 v[176:191], v[204:207], v[44:47], v[176:191]\n\t"
    "v_exp_f32 v163, v163\n\t"
    "v_exp_f32 v164, v164\n\t"
    "v_exp_f32 v165, v165\n\t"
    "v_smfmac_f32_32x32x32_bf16 v[96:111], v[208:211], v[144:151], v124\n\t"
    "v_exp_f32 v166, v166\n\t"
    "v_exp_f32 v167, v167\n\t"
    "v_exp_f32 v168, v168\n\t"
    "v_smfmac_f32_16x16x64_bf16 v[116:119], v[120:123], v[144:151], v124\n\t"
    "ds_read_b128 v[208:211], %[rb] offset:59392\n\t"
    "v_exp_f32 v169, v169\n\t"
    "v_exp_f32 v170, v170\n\t"
    "v_smfmac_f32_32x32x32_bf16 v[96:111], v[212:215], v[152:159], v124\n\t"
    "s_waitcnt lgkmcnt(3)\n\t"
    "v_exp_f32 v171, v171\n\t"
    "v_exp_f32 v172, v172\n\t"
    "v_exp_f32 v173, v173\n\t"
    "v_smfmac_f32_16x16x64_bf16 v[116:119], v[120:123], v[152:159], v124\n\t"
    "ds_read_b128 v[212:215], %[rb] offset:60416\n\t"
    "v_exp_f32 v174, v174\n\t"
    "v_exp_f32 v175, v175\n\t"
    "v_mfma_f32_32x32x16_bf16 v[128:143], v[192:195], v[32:35], v[48:63]\n\t"
    "ds_read_b64_tr_b16 a[22:23], v229 offset:18432\n\t"
    "ds_read_b64_tr_b16 a[24:25], v228 offset:20480\n\t"
    "v_exp_f32 v176, v176\n\t"
    "v_exp_f32 v177, v177\n\t"
    "v_exp_f32 v178, v178\n\t"
    "v_mfma_f32_32x32x16_bf16 v[128:143], v[196:199], v[36:39], v[128:143]\n\t"
    "v_exp_f32 v179, v179\n\t"
    "v_exp_f32 v180, v180\n\t"
    "v_exp_f32 v181, v181\n\t"
    "v_smfmac_f32_32x32x32_bf16 v[80:95], v[216:219], v[160:167], v124\n\t"
    "v_exp_f32 v182, v182\n\t"
    "v_exp_f32 v183, v183\n\t"
    "v_exp_f32 v184, v184\n\t"
    "v_smfmac_f32_16x16x64_bf16 v[112:115], v[120:123], v[160:167], v124\n\t"
    "ds_read_b128 v[200:203], %[rb] offset:61440\n\t"
    "v_exp_f32 v185, v185\n\t"
    "v_exp_f32 v186, v186\n\t"
    "v_smfmac_f32_32x32x32_bf16 v[80:95], v[220:223], v[168:175], v124\n\t"
    "v_exp_f32 v187, v187\n\t"
    "v_exp_f32 v188, v188\n\t"
    "v_exp_f32 v189, v189\n\t"
    "v_smfmac_f32_16x16x64_bf16 v[112:115], v[120:123], v[168:175], v124\n\t"
    "ds_read_b128 v[204:207], %[rb] offset:62464\n\t"
    "v_exp_f32 v190, v190\n\t"
    "v_exp_f32 v191, v191\n\t"
    "v_mfma_f32_32x32x16_bf16 v[144:159], v[192:195], v[40:43], v[64:79]\n\t"
    "ds_read_b64_tr_b16 a[26:27], v228 offset:22528\n\t"
    "ds_read_b64_tr_b16 a[28:29], v229 offset:20480\n\t"
    "ds_read_b64_tr_b16 a[30:31], v229 offset:22528\n\t"
    "v_exp_f32 v128, v128\n\t"
    "v_exp_f32 v129, v129\n\t"
    "v_exp_f32 v130, v130\n\t"
    "v_mfma_f32_32x32x16_bf16 v[144:159], v[196:199], v[44:47], v[144:159]\n\t"
    "v_exp_f32 v131, v131\n\t"
    "v_exp_f32 v132, v132\n\t"
    "v_exp_f32 v133, v133\n\t"
    "v_smfmac_f32_32x32x32_bf16 v[96:111], v[216:219], v[176:183], v124\n\t"
    "v_exp_f32 v134, v134\n\t"
    "v_exp_f32 v135, v135\n\t"
    "v_exp_f32 v136, v136\n\t"
    "v_smfmac_f32_16x16x64_bf16 v[116:119], v[120:123], v[176:183], v124\n\t"
    "ds_read_b128 v[216:219], %[rb] offset:63488\n\t"
    "v_exp_f32 v137, v137\n\t"
    "v_exp_f32 v138, v138\n\t"
    "v_smfmac_f32_32x32x32_bf16 v[96:111], v[220:223], v[184:191], v124\n\t"
    "s_waitcnt lgkmcnt(4)\n\t"
    "v_exp_f32 v139, v139\n\t"
    "v_exp_f32 v140, v140\n\t"
    "v_exp_f32 v141, v141\n\t"
    "v_smfmac_f32_16x16x64_bf16 v[116:119], v[120:123], v[184:191], v124\n\t"
    "ds_read_b128 v[220:223], %[rb] offset:64512\n\t"
    "v_exp_f32 v142, v142\n\t"
    "v_exp_f32 v143, v143\n\t"
    "v_mfma_f32_32x32x16_bf16 v[160:175], v[200:203], v[32:35], v[48:63]\n\t"
    "v_exp_f32 v144, v144\n\t"
    "v_exp_f32 v145, v145\n\t"
    "v_exp_f32 v146, v146\n\t"
    "v_mfma_f32_32x32x16_bf16 v[160:175], v[204:207], v[36:39], v[160:175]\n\t"
    "v_exp_f32 v147, v147\n\t"
    "v_exp_f32 v148, v148\n\t"
    "v_exp_f32 v149, v149\n\t"
    "v_smfmac_f32_32x32x32_bf16 v[80:95], v[208:211], v[128:135], v124\n\t"
    "s_waitcnt vmcnt(5) lgkmcnt(0)\n\t"
    "s_barrier\n\t"
    "v_exp_f32 v150, v150\n\t"
    "v_exp_f32 v151, v151\n\t"
    "v_exp_f32 v152, v152\n\t"
    "v_smfmac_f32_16x16x64_bf16 v[112:115], v[120:123], v[128:135], v124\n\t"
    "ds_read_b128 v[192:195], %[rb] offset:0\n\t"
    "v_exp_f32 v153, v153\n\t"
    "v_exp_f32 v154, v154\n\t"
    "v_smfmac_f32_32x32x32_bf16 v[80:95], v[212:215], v[136:143], v124\n\t"
    "v_exp_f32 v155, v155\n\t"
    "v_exp_f32 v156, v156\n\t"
    "v_exp_f32 v157, v157\n\t"
    "v_smfmac_f32_16x16x64_bf16 v[112:115], v[120:123], v[136:143], v124\n\t"
    "ds_read_b128 v[196:199], %[rb] offset:1024\n\t"
    "v_exp_f32 v158, v158\n\t"
    "v_exp_f32 v159, v159\n\t"
    "v_mfma_f32_32x32x16_bf16 v[176:191], v[200:203], v[40:43], v[64:79]\n\t"
    "ds_read_b64_tr_b16 a[32:33], v228 offset:32768\n\t"
    "ds_read_b64_tr_b16 a[34:35], v228 offset:34816\n\t"
    "v_exp_f32 v160, v160\n\t"
    "v_exp_f32 v161, v161\n\t"
    "v_exp_f32 v162, v162\n\t"
    "v_mfma_f32_32x32x16_bf16 v[176:191], v[204:207], v[44:47], v[176:191]\n\t"
    "v_exp_f32 v163, v163\n\t"
    "v_exp_f32 v164, v164\n\t"
    "v_exp_f32 v165, v165\n\t"
    "v_smfmac_f32_32x32x32_bf16 v[96:111], v[208:211], v[144:151], v124\n\t"
    "v_exp_f32 v166, v166\n\t"
    "v_exp_f32 v167, v167\n\t"
    "v_exp_f32 v168, v168\n\t"
    "v_smfmac_f32_16x16x64_bf16 v[116:119], v[120:123], v[144:151], v124\n\t"
    "ds_read_b128 v[208:211], %[rb] offset:2048\n\t"
    "v_exp_f32 v169, v169\n\t"
    "v_exp_f32 v170, v170\n\t"
    "v_smfmac_f32_32x32x32_bf16 v[96:111], v[212:215], v[152:159], v124\n\t"
    "s_waitcnt lgkmcnt(3)\n\t"
    "v_exp_f32 v171, v171\n\t"
    "v_exp_f32 v172, v172\n\t"
    "v_exp_f32 v173, v173\n\t"
    "v_smfmac_f32_16x16x64_bf16 v[116:119], v[120:123], v[152:159], v124\n\t"
    "ds_read_b128 v[212:215], %[rb] offset:3072\n\t"
    "v_exp_f32 v174, v174\n\t"
    "v_exp_f32 v175, v175\n\t"
    "v_mfma_f32_32x32x16_bf16 v[128:143], v[192:195], v[32:35], v[48:63]\n\t"
    "ds_read_b64_tr_b16 a[36:37], v229 offset:32768\n\t"
    "ds_read_b64_tr_b16 a[38:39], v229 offset:34816\n\t"
    "s_add_u32 s52, %[krot], 114688\n\t"
    "v_exp_f32 v176, v176\n\t"
    "v_exp_f32 v177, v177\n\t"
    "v_exp_f32 v178, v178\n\t"
    "v_mfma_f32_32x32x16_bf16 v[128:143], v[196:199], v[36:39], v[128:143]\n\t"
    "s_add_u32 m0, s54, 49152\n\t"
    "s_and_b32 s52, s52, 0x3ffff\n\t"
    "v_exp_f32 v179, v179\n\t"
    "v_exp_f32 v180, v180\n\t"
    "v_exp_f32 v181, v181\n\t"
    "v_smfmac_f32_32x32x32_bf16 v[80:95], v[216:219], v[160:167], v124\n\t"
    "buffer_load_dwordx4 %[vg], s[40:43], s52 offen offset:0 sc1 lds\n\t"
    "buffer_load_dwordx4 %[vg], s[40:43], s52 offen offset:1024 sc1 lds\n\t"
    "v_exp_f32 v182, v182\n\t"
    "v_exp_f32 v183, v183\n\t"
    "v_exp_f32 v184, v184\n\t"
    "v_smfmac_f32_16x16x64_bf16 v[112:115], v[120:123], v[160:167], v124\n\t"
    "ds_read_b128 v[200:203], %[rb] offset:4096\n\t"
    "v_exp_f32 v185, v185\n\t"
    "v_exp_f32 v186, v186\n\t"
    "v_smfmac_f32_32x32x32_bf16 v[80:95], v[220:223], v[168:175], v124\n\t"
    "v_exp_f32 v187, v187\n\t"
    "v_exp_f32 v188, v188\n\t"
    "v_exp_f32 v189, v189\n\t"
    "v_smfmac_f32_16x16x64_bf16 v[112:115], v[120:123], v[168:175], v124\n\t"
    "ds_read_b128 v[204:207], %[rb] offset:5120\n\t"
    "v_exp_f32 v190, v190\n\t"
    "v_exp_f32 v191, v191\n\t"
    "v_mfma_f32_32x32x16_bf16 v[144:159], v[192:195], v[40:43], v[64:79]\n\t"
    "ds_read_b64_tr_b16 a[40:41], v228 offset:36864\n\t"
    "ds_read_b64_tr_b16 a[42:43], v228 offset:38912\n\t"
    "v_exp_f32 v128, v128\n\t"
    "v_exp_f32 v129, v129\n\t"
    "v_exp_f32 v130, v130\n\t"
    "v_mfma_f32_32x32x16_bf16 v[144:159], v[196:199], v[44:47], v[144:159]\n\t"
    "v_exp_f32 v131, v131\n\t"
    "v_exp_f32 v132, v132\n\t"
    "v_exp_f32 v133, v133\n\t"
    "v_smfmac_f32_32x32x32_bf16 v[96:111], v[216:219], v[176:183], v124\n\t"
    "buffer_load_dwordx4 %[vg], s[40:43], s52 offen offset:2048 sc1 lds\n\t"
    "buffer_load_dwordx4 %[vg], s[40:43], s52 offen offset:3072 sc1 lds\n\t"
    "v_exp_f32 v134, v134\n\t"
    "v_exp_f32 v135, v135\n\t"
    "v_exp_f32 v136, v136\n\t"
    "v_smfmac_f32_16x16x64_bf16 v[116:119], v[120:123], v[176:183], v124\n\t"
    "ds_read_b128 v[216:219], %[rb] offset:6144\n\t"
    "v_exp_f32 v137, v137\n\t"
    "v_exp_f32 v138, v138\n\t"
    "v_smfmac_f32_32x32x32_bf16 v[96:111], v[220:223], v[184:191], v124\n\t"
    "s_waitcnt lgkmcnt(3)\n\t"
    "v_exp_f32 v139, v139\n\t"
    "v_exp_f32 v140, v140\n\t"
    "v_exp_f32 v141, v141\n\t"
    "v_smfmac_f32_16x16x64_bf16 v[116:119], v[120:123], v[184:191], v124\n\t"
    "ds_read_b128 v[220:223], %[rb] offset:7168\n\t"
    "v_exp_f32 v142, v142\n\t"
    "v_exp_f32 v143, v143\n\t"
    "v_mfma_f32_32x32x16_bf16 v[160:175], v[200:203], v[32:35], v[48:63]\n\t"
    "ds_read_b64_tr_b16 a[44:45], v229 offset:36864\n\t"
    "ds_read_b64_tr_b16 a[46:47], v229 offset:38912\n\t"
    "ds_read_b64_tr_b16 a[48:49], v228 offset:49152\n\t"
    "s_add_u32 m0, s54, 65536\n\t"
    "v_exp_f32 v144, v144\n\t"
    "v_exp_f32 v145, v145\n\t"
    "v_exp_f32 v146, v146\n\t"
    "v_mfma_f32_32x32x16_bf16 v[160:175], v[204:207], v[36:39], v[160:175]\n\t"
    "s_mov_b32 s53, 1048576\n\t"
    "v_exp_f32 v147, v147\n\t"
    "v_exp_f32 v148, v148\n\t"
    "v_exp_f32 v149, v149\n\t"
    "v_smfmac_f32_32x32x32_bf16 v[80:95], v[208:211], v[128:135], v124\n\t"
    "buffer_load_dwordx4 v224, s[44:47], s53 offen offset:0 lds\n\t"
    "buffer_load_dwordx4 v225, s[44:47], s53 offen offset:1024 lds\n\t"
    "v_exp_f32 v150, v150\n\t"
    "v_exp_f32 v151, v151\n\t"
    "v_exp_f32 v152, v152\n\t"
    "v_smfmac_f32_16x16x64_bf16 v[112:115], v[120:123], v[128:135], v124\n\t"
    "ds_read_b128 v[192:195], %[rb] offset:8192\n\t"
    "v_exp_f32 v153, v153\n\t"
    "v_exp_f32 v154, v154\n\t"
    "v_smfmac_f32_32x32x32_bf16 v[80:95], v[212:215], v[136:143], v124\n\t"
    "v_exp_f32 v155, v155\n\t"
    "v_exp_f32 v156, v156\n\t"
    "v_exp_f32 v157, v157\n\t"
    "v_smfmac_f32_16x16x64_bf16 v[112:115], v[120:123], v[136:143], v124\n\t"
    "ds_read_b128 v[196:199], %[rb] offset:9216\n\t"
    "v_exp_f32 v158, v158\n\t"
    "v_exp_f32 v159, v159\n\t"
    "v_mfma_f32_32x32x16_bf16 v[176:191], v[200:203], v[40:43], v[64:79]\n\t"
    "ds_read_b64_tr_b16 a[50:51], v228 offset:51200\n\t"
    "ds_read_b64_tr_b16 a[52:53], v229 offset:49152\n\t"
    "v_exp_f32 v160, v160\n\t"
    "v_exp_f32 v161, v161\n\t"
    "v_exp_f32 v162, v162\n\t"
    "v_mfma_f32_32x32x16_bf16 v[176:191], v[204:207], v[44:47], v[176:191]\n\t"
    "v_exp_f32 v163, v163\n\t"
    "v_exp_f32 v164, v164\n\t"
    "v_exp_f32 v165, v165\n\t"
    "v_smfmac_f32_32x32x32_bf16 v[96:111], v[208:211], v[144:151], v124\n\t"
    "buffer_load_dwordx4 v226, s[44:47], s53 offen offset:2048 lds\n\t"
    "buffer_load_dwordx4 v227, s[44:47], s53 offen offset:3072 lds\n\t"
    "v_exp_f32 v166, v166\n\t"
    "v_exp_f32 v167, v167\n\t"
    "v_exp_f32 v168, v168\n\t"
    "v_smfmac_f32_16x16x64_bf16 v[116:119], v[120:123], v[144:151], v124\n\t"
    "ds_read_b128 v[208:211], %[rb] offset:10240\n\t"
    "v_exp_f32 v169, v169\n\t"
    "v_exp_f32 v170, v170\n\t"
    "v_smfmac_f32_32x32x32_bf16 v[96:111], v[212:215], v[152:159], v124\n\t"
    "s_waitcnt lgkmcnt(3)\n\t"
    "v_exp_f32 v171, v171\n\t"
    "v_exp_f32 v172, v172\n\t"
    "v_exp_f32 v173, v173\n\t"
    "v_smfmac_f32_16x16x64_bf16 v[116:119], v[120:123], v[152:159], v124\n\t"
    "ds_read_b128 v[212:215], %[rb] offset:11264\n\t"
    "v_exp_f32 v174, v174\n\t"
    "v_exp_f32 v175, v175\n\t"
    "v_mfma_f32_32x32x16_bf16 v[128:143], v[192:195], v[32:35], v[48:63]\n\t"
    "ds_read_b64_tr_b16 a[54:55], v229 offset:51200\n\t"
    "ds_read_b64_tr_b16 a[56:57], v228 offset:53248\n\t"
    "s_add_u32 m0, s54, 81920\n\t"
    "v_exp_f32 v176, v176\n\t"
    "v_exp_f32 v177, v177\n\t"
    "v_exp_f32 v178, v178\n\t"
    "v_mfma_f32_32x32x16_bf16 v[128:143], v[196:199], v[36:39], v[128:143]\n\t"
    "s_mov_b32 s53, 1310720\n\t"
    "v_exp_f32 v179, v179\n\t"
    "v_exp_f32 v180, v180\n\t"
    "v_exp_f32 v181, v181\n\t"
    "v_smfmac_f32_32x32x32_bf16 v[80:95], v[216:219], v[160:167], v124\n\t"
    "buffer_load_dwordx4 v224, s[44:47], s53 offen offset:0 lds\n\t"
    "buffer_load_dwordx4 v225, s[44:47], s53 offen offset:1024 lds\n\t"
    "v_exp_f32 v182, v182\n\t"
    "v_exp_f32 v183, v183\n\t"
    "v_exp_f32 v184, v184\n\t"
    "v_smfmac_f32_16x16x64_bf16 v[112:115], v[120:123], v[160:167], v124\n\t"
    "ds_read_b128 v[200:203], %[rb] offset:12288\n\t"
    "v_exp_f32 v185, v185\n\t"
    "v_exp_f32 v186, v186\n\t"
    "v_smfmac_f32_32x32x32_bf16 v[80:95], v[220:223], v[168:175], v124\n\t"
    "v_exp_f32 v187, v187\n\t"
    "v_exp_f32 v188, v188\n\t"
    "v_exp_f32 v189, v189\n\t"
    "v_smfmac_f32_16x16x64_bf16 v[112:115], v[120:123], v[168:175], v124\n\t"
    "ds_read_b128 v[204:207], %[rb] offset:13312\n\t"
    "v_exp_f32 v190, v190\n\t"
    "v_exp_f32 v191, v191\n\t"
    "v_mfma_f32_32x32x16_bf16 v[144:159], v[192:195], v[40:43], v[64:79]\n\t"
    "ds_read_b64_tr_b16 a[58:59], v228 offset:55296\n\t"
    "ds_read_b64_tr_b16 a[60:61], v229 offset:53248\n\t"
    "ds_read_b64_tr_b16 a[62:63], v229 offset:55296\n\t"
    "v_exp_f32 v128, v128\n\t"
    "v_exp_f32 v129, v129\n\t"
    "v_exp_f32 v130, v130\n\t"
    "v_mfma_f32_32x32x16_bf16 v[144:159], v[196:199], v[44:47], v[144:159]\n\t"
    "v_exp_f32 v131, v131\n\t"
    "v_exp_f32 v132, v132\n\t"
    "v_exp_f32 v133, v133\n\t"
    "v_smfmac_f32_32x32x32_bf16 v[96:111], v[216:219], v[176:183], v124\n\t"
    "buffer_load_dwordx4 v226, s[44:47], s53 offen offset:2048 lds\n\t"
    "buffer_load_dwordx4 v227, s[44:47], s53 offen offset:3072 lds\n\t"
    "v_exp_f32 v134, v134\n\t"
    "v_exp_f32 v135, v135\n\t"
    "v_exp_f32 v136, v136\n\t"
    "v_smfmac_f32_16x16x64_bf16 v[116:119], v[120:123], v[176:183], v124\n\t"
    "ds_read_b128 v[216:219], %[rb] offset:14336\n\t"
    "v_exp_f32 v137, v137\n\t"
    "v_exp_f32 v138, v138\n\t"
    "v_smfmac_f32_32x32x32_bf16 v[96:111], v[220:223], v[184:191], v124\n\t"
    "s_waitcnt lgkmcnt(4)\n\t"
    "v_exp_f32 v139, v139\n\t"
    "v_exp_f32 v140, v140\n\t"
    "v_exp_f32 v141, v141\n\t"
    "v_smfmac_f32_16x16x64_bf16 v[116:119], v[120:123], v[184:191], v124\n\t"
    "ds_read_b128 v[220:223], %[rb] offset:15360\n\t"
    "v_exp_f32 v142, v142\n\t"
    "v_exp_f32 v143, v143\n\t"
    "v_mfma_f32_32x32x16_bf16 v[160:175], v[200:203], v[32:35], v[48:63]\n\t"
    "v_exp_f32 v144, v144\n\t"
    "v_exp_f32 v145, v145\n\t"
    "v_exp_f32 v146, v146\n\t"
    "v_mfma_f32_32x32x16_bf16 v[160:175], v[204:207], v[36:39], v[160:175]\n\t"
    "v_exp_f32 v147, v147\n\t"
    "v_exp_f32 v148, v148\n\t"
    "v_exp_f32 v149, v149\n\t"
    "v_smfmac_f32_32x32x32_bf16 v[80:95], v[208:211], v[128:135], v124\n\t"
    "s_waitcnt lgkmcnt(0)\n\t"
    "s_barrier\n\t"
    "v_exp_f32 v150, v150\n\t"
    "v_exp_f32 v151, v151\n\t"
    "v_exp_f32 v152, v152\n\t"
    "v_smfmac_f32_16x16x64_bf16 v[112:115], v[120:123], v[128:135], v124\n\t"
    "ds_read_b128 v[192:195], %[rb] offset:16384\n\t"
    "v_exp_f32 v153, v153\n\t"
    "v_exp_f32 v154, v154\n\t"
    "v_smfmac_f32_32x32x32_bf16 v[80:95], v[212:215], v[136:143], v124\n\t"
    "v_exp_f32 v155, v155\n\t"
    "v_exp_f32 v156, v156\n\t"
    "v_exp_f32 v157, v157\n\t"
    "v_smfmac_f32_16x16x64_bf16 v[112:115], v[120:123], v[136:143], v124\n\t"
    "ds_read_b128 v[196:199], %[rb] offset:17408\n\t"
    "v_exp_f32 v158, v158\n\t"
    "v_exp_f32 v159, v159\n\t"
    "v_mfma_f32_32x32x16_bf16 v[176:191], v[200:203], v[40:43], v[64:79]\n\t"
    "v_exp_f32 v160, v160\n\t"
    "v_exp_f32 v161, v161\n\t"
    "v_exp_f32 v162, v162\n\t"
    "v_mfma_f32_32x32x16_bf16 v[176:191], v[204:207], v[44:47], v[176:191]\n\t"
    "v_exp_f32 v163, v163\n\t"
    "v_exp_f32 v164, v164\n\t"
    "v_exp_f32 v165, v165\n\t"
    "v_smfmac_f32_32x32x32_bf16 v[96:111], v[208:211], v[144:151], v124\n\t"
    "v_exp_f32 v166, v166\n\t"
    "v_exp_f32 v167, v167\n\t"
    "v_exp_f32 v168, v168\n\t"
    "v_smfmac_f32_16x16x64_bf16 v[116:119], v[120:123], v[144:151], v124\n\t"
    "ds_read_b128 v[208:211], %[rb] offset:18432\n\t"
    "v_exp_f32 v169, v169\n\t"
    "v_exp_f32 v170, v170\n\t"
    "v_smfmac_f32_32x32x32_bf16 v[96:111], v[212:215], v[152:159], v124\n\t"
    "s_waitcnt lgkmcnt(1)\n\t"
    "v_exp_f32 v171, v171\n\t"
    "v_exp_f32 v172, v172\n\t"
    "v_exp_f32 v173, v173\n\t"
    "v_smfmac_f32_16x16x64_bf16 v[116:119], v[120:123], v[152:159], v124\n\t"
    "ds_read_b128 v[212:215], %[rb] offset:19456\n\t"
    "v_exp_f32 v174, v174\n\t"
    "v_exp_f32 v175, v175\n\t"
    "v_mfma_f32_32x32x16_bf16 v[128:143], v[192:195], v[32:35], v[48:63]\n\t"
    "s_add_u32 s52, %[krot], 131072\n\t"
    "v_exp_f32 v176, v176\n\t"
    "v_exp_f32 v177, v177\n\t"
    "v_exp_f32 v178, v178\n\t"
    "v_mfma_f32_32x32x16_bf16 v[128:143], v[196:199], v[36:39], v[128:143]\n\t"
    "s_add_u32 m0, s54, 0\n\t"
    "s_and_b32 s52, s52, 0x3ffff\n\t"
    "v_exp_f32 v179, v179\n\t"
    "v_exp_f32 v180, v180\n\t"
    "v_exp_f32 v181, v181\n\t"
    "v_smfmac_f32_32x32x32_bf16 v[80:95], v[216:219], v[160:167], v124\n\t"
    "buffer_load_dwordx4 %[vg], s[40:43], s52 offen offset:0 sc1 lds\n\t"
    "buffer_load_dwordx4 %[vg], s[40:43], s52 offen offset:1024 sc1 lds\n\t"
    "v_exp_f32 v182, v182\n\t"
    "v_exp_f32 v183, v183\n\t"
    "v_exp_f32 v184, v184\n\t"
    "v_smfmac_f32_16x16x64_bf16 v[112:115], v[120:123], v[160:167], v124\n\t"
    "ds_read_b128 v[200:203], %[rb] offset:20480\n\t"
    "v_exp_f32 v185, v185\n\t"
    "v_exp_f32 v186, v186\n\t"
    "v_smfmac_f32_32x32x32_bf16 v[80:95], v[220:223], v[168:175], v124\n\t"
    "v_exp_f32 v187, v187\n\t"
    "v_exp_f32 v188, v188\n\t"
    "v_exp_f32 v189, v189\n\t"
    "v_smfmac_f32_16x16x64_bf16 v[112:115], v[120:123], v[168:175], v124\n\t"
    "ds_read_b128 v[204:207], %[rb] offset:21504\n\t"
    "v_exp_f32 v190, v190\n\t"
    "v_exp_f32 v191, v191\n\t"
    "v_mfma_f32_32x32x16_bf16 v[144:159], v[192:195], v[40:43], v[64:79]\n\t"
    "v_exp_f32 v128, v128\n\t"
    "v_exp_f32 v129, v129\n\t"
    "v_exp_f32 v130, v130\n\t"
    "v_mfma_f32_32x32x16_bf16 v[144:159], v[196:199], v[44:47], v[144:159]\n\t"
    "v_exp_f32 v131, v131\n\t"
    "v_exp_f32 v132, v132\n\t"
    "v_exp_f32 v133, v133\n\t"
    "v_smfmac_f32_32x32x32_bf16 v[96:111], v[216:219], v[176:183], v124\n\t"
    "buffer_load_dwordx4 %[vg], s[40:43], s52 offen offset:2048 sc1 lds\n\t"
    "buffer_load_dwordx4 %[vg], s[40:43], s52 offen offset:3072 sc1 lds\n\t"
    "v_exp_f32 v134, v134\n\t"
    "v_exp_f32 v135, v135\n\t"
    "v_exp_f32 v136, v136\n\t"
    "v_smfmac_f32_16x16x64_bf16 v[116:119], v[120:123], v[176:183], v124\n\t"
    "ds_read_b128 v[216:219], %[rb] offset:22528\n\t"
    "v_exp_f32 v137, v137\n\t"
    "v_exp_f32 v138, v138\n\t"
    "v_smfmac_f32_32x32x32_bf16 v[96:111], v[220:223], v[184:191], v124\n\t"
    "s_waitcnt lgkmcnt(1)\n\t"
    "v_exp_f32 v139, v139\n\t"
    "v_exp_f32 v140, v140\n\t"
    "v_exp_f32 v141, v141\n\t"
    "v_smfmac_f32_16x16x64_bf16 v[116:119], v[120:123], v[184:191], v124\n\t"
    "ds_read_b128 v[220:223], %[rb] offset:23552\n\t"
    "v_exp_f32 v142, v142\n\t"
    "v_exp_f32 v143, v143\n\t"
    "v_mfma_f32_32x32x16_bf16 v[160:175], v[200:203], v[32:35], v[48:63]\n\t"
    "s_add_u32 m0, s54, 98304\n\t"
    "v_exp_f32 v144, v144\n\t"
    "v_exp_f32 v145, v145\n\t"
    "v_exp_f32 v146, v146\n\t"
    "v_mfma_f32_32x32x16_bf16 v[160:175], v[204:207], v[36:39], v[160:175]\n\t"
    "s_mov_b32 s53, 1572864\n\t"
    "v_exp_f32 v147, v147\n\t"
    "v_exp_f32 v148, v148\n\t"
    "v_exp_f32 v149, v149\n\t"
    "v_smfmac_f32_32x32x32_bf16 v[80:95], v[208:211], v[128:135], v124\n\t"
    "buffer_load_dwordx4 v224, s[44:47], s53 offen offset:0 lds\n\t"
    "buffer_load_dwordx4 v225, s[44:47], s53 offen offset:1024 lds\n\t"
    "v_exp_f32 v150, v150\n\t"
    "v_exp_f32 v151, v151\n\t"
    "v_exp_f32 v152, v152\n\t"
    "v_smfmac_f32_16x16x64_bf16 v[112:115], v[120:123], v[128:135], v124\n\t"
    "ds_read_b128 v[192:195], %[rb] offset:24576\n\t"
    "v_exp_f32 v153, v153\n\t"
    "v_exp_f32 v154, v154\n\t"
    "v_smfmac_f32_32x32x32_bf16 v[80:95], v[212:215], v[136:143], v124\n\t"
    "v_exp_f32 v155, v155\n\t"
    "v_exp_f32 v156, v156\n\t"
    "v_exp_f32 v157, v157\n\t"
    "v_smfmac_f32_16x16x64_bf16 v[112:115], v[120:123], v[136:143], v124\n\t"
    "ds_read_b128 v[196:199], %[rb] offset:25600\n\t"
    "v_exp_f32 v158, v158\n\t"
    "v_exp_f32 v159, v159\n\t"
    "v_mfma_f32_32x32x16_bf16 v[176:191], v[200:203], v[40:43], v[64:79]\n\t"
    "v_exp_f32 v160, v160\n\t"
    "v_exp_f32 v161, v161\n\t"
    "v_exp_f32 v162, v162\n\t"
    "v_mfma_f32_32x32x16_bf16 v[176:191], v[204:207], v[44:47], v[176:191]\n\t"
    "v_exp_f32 v163, v163\n\t"
    "v_exp_f32 v164, v164\n\t"
    "v_exp_f32 v165, v165\n\t"
    "v_smfmac_f32_32x32x32_bf16 v[96:111], v[208:211], v[144:151], v124\n\t"
    "buffer_load_dwordx4 v226, s[44:47], s53 offen offset:2048 lds\n\t"
    "buffer_load_dwordx4 v227, s[44:47], s53 offen offset:3072 lds\n\t"
    "v_exp_f32 v166, v166\n\t"
    "v_exp_f32 v167, v167\n\t"
    "v_exp_f32 v168, v168\n\t"
    "v_smfmac_f32_16x16x64_bf16 v[116:119], v[120:123], v[144:151], v124\n\t"
    "ds_read_b128 v[208:211], %[rb] offset:26624\n\t"
    "v_exp_f32 v169, v169\n\t"
    "v_exp_f32 v170, v170\n\t"
    "v_smfmac_f32_32x32x32_bf16 v[96:111], v[212:215], v[152:159], v124\n\t"
    "s_waitcnt lgkmcnt(1)\n\t"
    "v_exp_f32 v171, v171\n\t"
    "v_exp_f32 v172, v172\n\t"
    "v_exp_f32 v173, v173\n\t"
    "v_smfmac_f32_16x16x64_bf16 v[116:119], v[120:123], v[152:159], v124\n\t"
    "ds_read_b128 v[212:215], %[rb] offset:27648\n\t"
    "v_exp_f32 v174, v174\n\t"
    "v_exp_f32 v175, v175\n\t"
    "v_mfma_f32_32x32x16_bf16 v[128:143], v[192:195], v[32:35], v[48:63]\n\t"
    "s_add_u32 m0, s54, 114688\n\t"
    "v_exp_f32 v176, v176\n\t"
    "v_exp_f32 v177, v177\n\t"
    "v_exp_f32 v178, v178\n\t"
    "v_mfma_f32_32x32x16_bf16 v[128:143], v[196:199], v[36:39], v[128:143]\n\t"
    "s_mov_b32 s53, 1835008\n\t"
    "v_exp_f32 v179, v179\n\t"
    "v_exp_f32 v180, v180\n\t"
    "v_exp_f32 v181, v181\n\t"
    "v_smfmac_f32_32x32x32_bf16 v[80:95], v[216:219], v[160:167], v124\n\t"
    "buffer_load_dwordx4 v224, s[44:47], s53 offen offset:0 lds\n\t"
    "buffer_load_dwordx4 v225, s[44:47], s53 offen offset:1024 lds\n\t"
    "v_exp_f32 v182, v182\n\t"
    "v_exp_f32 v183, v183\n\t"
    "v_exp_f32 v184, v184\n\t"
    "v_smfmac_f32_16x16x64_bf16 v[112:115], v[120:123], v[160:167], v124\n\t"
    "ds_read_b128 v[200:203], %[rb] offset:28672\n\t"
    "v_exp_f32 v185, v185\n\t"
    "v_exp_f32 v186, v186\n\t"
    "v_smfmac_f32_32x32x32_bf16 v[80:95], v[220:223], v[168:175], v124\n\t"
    "v_exp_f32 v187, v187\n\t"
    "v_exp_f32 v188, v188\n\t"
    "v_exp_f32 v189, v189\n\t"
    "v_smfmac_f32_16x16x64_bf16 v[112:115], v[120:123], v[168:175], v124\n\t"
    "ds_read_b128 v[204:207], %[rb] offset:29696\n\t"
    "v_exp_f32 v190, v190\n\t"
    "v_exp_f32 v191, v191\n\t"
    "v_mfma_f32_32x32x16_bf16 v[144:159], v[192:195], v[40:43], v[64:79]\n\t"
    "v_exp_f32 v128, v128\n\t"
    "v_exp_f32 v129, v129\n\t"
    "v_exp_f32 v130, v130\n\t"
    "v_mfma_f32_32x32x16_bf16 v[144:159], v[196:199], v[44:47], v[144:159]\n\t"
    "v_exp_f32 v131, v131\n\t"
    "v_exp_f32 v132, v132\n\t"
    "v_exp_f32 v133, v133\n\t"
    "v_smfmac_f32_32x32x32_bf16 v[96:111], v[216:219], v[176:183], v124\n\t"
    "buffer_load_dwordx4 v226, s[44:47], s53 offen offset:2048 lds\n\t"
    "buffer_load_dwordx4 v227, s[44:47], s53 offen offset:3072 lds\n\t"
    "v_exp_f32 v134, v134\n\t"
    "v_exp_f32 v135, v135\n\t"
    "v_exp_f32 v136, v136\n\t"
    "v_smfmac_f32_16x16x64_bf16 v[116:119], v[120:123], v[176:183], v124\n\t"
    "ds_read_b128 v[216:219], %[rb] offset:30720\n\t"
    "v_exp_f32 v137, v137\n\t"
    "v_exp_f32 v138, v138\n\t"
    "v_smfmac_f32_32x32x32_bf16 v[96:111], v[220:223], v[184:191], v124\n\t"
    "s_waitcnt lgkmcnt(1)\n\t"
    "v_exp_f32 v139, v139\n\t"
    "v_exp_f32 v140, v140\n\t"
    "v_exp_f32 v141, v141\n\t"
    "v_smfmac_f32_16x16x64_bf16 v[116:119], v[120:123], v[184:191], v124\n\t"
    "ds_read_b128 v[220:223], %[rb] offset:31744\n\t"
    "v_exp_f32 v142, v142\n\t"
    "v_exp_f32 v143, v143\n\t"
    "v_mfma_f32_32x32x16_bf16 v[160:175], v[200:203], v[32:35], v[48:63]\n\t"
    "v_exp_f32 v144, v144\n\t"
    "v_exp_f32 v145, v145\n\t"
    "v_exp_f32 v146, v146\n\t"
    "v_mfma_f32_32x32x16_bf16 v[160:175], v[204:207], v[36:39], v[160:175]\n\t"
    "v_exp_f32 v147, v147\n\t"
    "v_exp_f32 v148, v148\n\t"
    "v_exp_f32 v149, v149\n\t"
    "v_smfmac_f32_32x32x32_bf16 v[80:95], v[208:211], v[128:135], v124\n\t"
    "s_waitcnt vmcnt(12) lgkmcnt(0)\n\t"
    "s_barrier\n\t"
    "v_exp_f32 v150, v150\n\t"
    "v_exp_f32 v151, v151\n\t"
    "v_exp_f32 v152, v152\n\t"
    "v_smfmac_f32_16x16x64_bf16 v[112:115], v[120:123], v[128:135], v124\n\t"
    "ds_read_b128 v[192:195], %[rb] offset:32768\n\t"
    "v_exp_f32 v153, v153\n\t"
    "v_exp_f32 v154, v154\n\t"
    "v_smfmac_f32_32x32x32_bf16 v[80:95], v[212:215], v[136:143], v124\n\t"
    "v_exp_f32 v155, v155\n\t"
    "v_exp_f32 v156, v156\n\t"
    "v_exp_f32 v157, v157\n\t"
    "v_smfmac_f32_16x16x64_bf16 v[112:115], v[120:123], v[136:143], v124\n\t"
    "ds_read_b128 v[196:199], %[rb] offset:33792\n\t"
    "v_exp_f32 v158, v158\n\t"
    "v_exp_f32 v159, v159\n\t"
    "v_mfma_f32_32x32x16_bf16 v[176:191], v[200:203], v[40:43], v[64:79]\n\t"
    "ds_read_b64_tr_b16 a[64:65], v228 offset:0\n\t"
    "ds_read_b64_tr_b16 a[66:67], v228 offset:2048\n\t"
    "s_add_u32 s52, %[krot], 147456\n\t"
    "v_exp_f32 v160, v160\n\t"
    "v_exp_f32 v161, v161\n\t"
    "v_exp_f32 v162, v162\n\t"
    "v_mfma_f32_32x32x16_bf16 v[176:191], v[204:207], v[44:47], v[176:191]\n\t"
    "s_add_u32 m0, s54, 16384\n\t"
    "s_and_b32 s52, s52, 0x3ffff\n\t"
    "v_exp_f32 v163, v163\n\t"
    "v_exp_f32 v164, v164\n\t"
    "v_exp_f32 v165, v165\n\t"
    "v_smfmac_f32_32x32x32_bf16 v[96:111], v[208:211], v[144:151], v124\n\t"
    "buffer_load_dwordx4 %[vg], s[40:43], s52 offen offset:0 sc1 lds\n\t"
    "buffer_load_dwordx4 %[vg], s[40:43], s52 offen offset:1024 sc1 lds\n\t"
    "v_exp_f32 v166, v166\n\t"
    "v_exp_f32 v167, v167\n\t"
    "v_exp_f32 v168, v168\n\t"
    "v_smfmac_f32_16x16x64_bf16 v[116:119], v[120:123], v[144:151], v124\n\t"
    "ds_read_b128 v[208:211], %[rb] offset:34816\n\t"
    "v_exp_f32 v169, v169\n\t"
    "v_exp_f32 v170, v170\n\t"
    "v_smfmac_f32_32x32x32_bf16 v[96:111], v[212:215], v[152:159], v124\n\t"
    "s_waitcnt lgkmcnt(3)\n\t"
    "v_exp_f32 v171, v171\n\t"
    "v_exp_f32 v172, v172\n\t"
    "v_exp_f32 v173, v173\n\t"
    "v_smfmac_f32_16x16x64_bf16 v[116:119], v[120:123], v[152:159], v124\n\t"
    "ds_read_b128 v[212:215], %[rb] offset:35840\n\t"
    "v_exp_f32 v174, v174\n\t"
    "v_exp_f32 v175, v175\n\t"
    "v_mfma_f32_32x32x16_bf16 v[128:143], v[192:195], v[32:35], v[48:63]\n\t"
    "ds_read_b64_tr_b16 a[68:69], v229 offset:0\n\t"
    "ds_read_b64_tr_b16 a[70:71], v229 offset:2048\n\t"
    "v_exp_f32 v176, v176\n\t"
    "v_exp_f32 v177, v177\n\t"
    "v_exp_f32 v178, v178\n\t"
    "v_mfma_f32_32x32x16_bf16 v[128:143], v[196:199], v[36:39], v[128:143]\n\t"
    "v_exp_f32 v179, v179\n\t"
    "v_exp_f32 v180, v180\n\t"
    "v_exp_f32 v181, v181\n\t"
    "v_smfmac_f32_32x32x32_bf16 v[80:95], v[216:219], v[160:167], v124\n\t"
    "v_exp_f32 v182, v182\n\t"
    "v_exp_f32 v183, v183\n\t"
    "v_exp_f32 v184, v184\n\t"
    "v_smfmac_f32_16x16x64_bf16 v[112:115], v[120:123], v[160:167], v124\n\t"
    "ds_read_b128 v[200:203], %[rb] offset:36864\n\t"
    "v_exp_f32 v185, v185\n\t"
    "v_exp_f32 v186, v186\n\t"
    "v_smfmac_f32_32x32x32_bf16 v[80:95], v[220:223], v[168:175], v124\n\t"
    "v_exp_f32 v187, v187\n\t"
    "v_exp_f32 v188, v188\n\t"
    "v_exp_f32 v189, v189\n\t"
    "v_smfmac_f32_16x16x64_bf16 v[112:115], v[120:123], v[168:175], v124\n\t"
    "ds_read_b128 v[204:207], %[rb] offset:37888\n\t"
    "v_exp_f32 v190, v190\n\t"
    "v_exp_f32 v191, v191\n\t"
    "v_mfma_f32_32x32x16_bf16 v[144:159], v[192:195], v[40:43], v[64:79]\n\t"
    "ds_read_b64_tr_b16 a[72:73], v228 offset:4096\n\t"
    "ds_read_b64_tr_b16 a[74:75], v228 offset:6144\n\t"
    "v_exp_f32 v128, v128\n\t"
    "v_exp_f32 v129, v129\n\t"
    "v_exp_f32 v130, v130\n\t"
    "v_mfma_f32_32x32x16_bf16 v[144:159], v[196:199], v[44:47], v[144:159]\n\t"
    "v_exp_f32 v131, v131\n\t"
    "v_exp_f32 v132, v132\n\t"
    "v_exp_f32 v133, v133\n\t"
    "v_smfmac_f32_32x32x32_bf16 v[96:111], v[216:219], v[176:183], v124\n\t"
    "buffer_load_dwordx4 %[vg], s[40:43], s52 offen offset:2048 sc1 lds\n\t"
    "buffer_load_dwordx4 %[vg], s[40:43], s52 offen offset:3072 sc1 lds\n\t"
    "v_exp_f32 v134, v134\n\t"
    "v_exp_f32 v135, v135\n\t"
    "v_exp_f32 v136, v136\n\t"
    "v_smfmac_f32_16x16x64_bf16 v[116:119], v[120:123], v[176:183], v124\n\t"
    "ds_read_b128 v[216:219], %[rb] offset:38912\n\t"
    "v_exp_f32 v137, v137\n\t"
    "v_exp_f32 v138, v138\n\t"
    "v_smfmac_f32_32x32x32_bf16 v[96:111], v[220:223], v[184:191], v124\n\t"
    "s_waitcnt lgkmcnt(3)\n\t"
    "global_load_dwordx4 v[250:253], v248, s[62:63]\n\t"
    "v_exp_f32 v139, v139\n\t"
    "v_exp_f32 v140, v140\n\t"
    "v_exp_f32 v141, v141\n\t"
    "v_smfmac_f32_16x16x64_bf16 v[116:119], v[120:123], v[184:191], v124\n\t"
    "ds_read_b128 v[220:223], %[rb] offset:39936\n\t"
    "v_exp_f32 v142, v142\n\t"
    "v_exp_f32 v143, v143\n\t"
    "v_mfma_f32_32x32x16_bf16 v[160:175], v[200:203], v[32:35], v[48:63]\n\t"
    "ds_read_b64_tr_b16 a[76:77], v229 offset:4096\n\t"
    "ds_read_b64_tr_b16 a[78:79], v229 offset:6144\n\t"
    "ds_read_b64_tr_b16 a[80:81], v228 offset:16384\n\t"
    "v_exp_f32 v144, v144\n\t"
    "v_exp_f32 v145, v145\n\t"
    "v_exp_f32 v146, v146\n\t"
    "v_mfma_f32_32x32x16_bf16 v[160:175], v[204:207], v[36:39], v[160:175]\n\t"
    "v_exp_f32 v147, v147\n\t"
    "v_exp_f32 v148, v148\n\t"
    "v_exp_f32 v149, v149\n\t"
    "v_smfmac_f32_32x32x32_bf16 v[80:95], v[208:211], v[128:135], v124\n\t"
    "v_exp_f32 v150, v150\n\t"
    "v_exp_f32 v151, v151\n\t"
    "v_exp_f32 v152, v152\n\t"
    "v_smfmac_f32_16x16x64_bf16 v[112:115], v[120:123], v[128:135], v124\n\t"
    "ds_read_b128 v[192:195], %[rb] offset:40960\n\t"
    "v_exp_f32 v153, v153\n\t"
    "v_exp_f32 v154, v154\n\t"
    "v_smfmac_f32_32x32x32_bf16 v[80:95], v[212:215], v[136:143], v124\n\t"
    "v_exp_f32 v155, v155\n\t"
    "v_exp_f32 v156, v156\n\t"
    "v_exp_f32 v157, v157\n\t"
    "v_smfmac_f32_16x16x64_bf16 v[112:115], v[120:123], v[136:143], v124\n\t"
    "ds_read_b128 v[196:199], %[rb] offset:41984\n\t"
    "v_exp_f32 v158, v158\n\t"
    "v_exp_f32 v159, v159\n\t"
    "v_mfma_f32_32x32x16_bf16 v[176:191], v[200:203], v[40:43], v[64:79]\n\t"
    "ds_read_b64_tr_b16 a[82:83], v228 offset:18432\n\t"
    "ds_read_b64_tr_b16 a[84:85], v229 offset:16384\n\t"
    "v_exp_f32 v160, v160\n\t"
    "v_exp_f32 v161, v161\n\t"
    "v_exp_f32 v162, v162\n\t"
    "v_mfma_f32_32x32x16_bf16 v[176:191], v[204:207], v[44:47], v[176:191]\n\t"
    "v_exp_f32 v163, v163\n\t"
    "v_exp_f32 v164, v164\n\t"
    "v_exp_f32 v165, v165\n\t"
    "v_smfmac_f32_32x32x32_bf16 v[96:111], v[208:211], v[144:151], v124\n\t"
    "v_exp_f32 v166, v166\n\t"
    "v_exp_f32 v167, v167\n\t"
    "v_exp_f32 v168, v168\n\t"
    "v_smfmac_f32_16x16x64_bf16 v[116:119], v[120:123], v[144:151], v124\n\t"
    "ds_read_b128 v[208:211], %[rb] offset:43008\n\t"
    "v_exp_f32 v169, v169\n\t"
    "v_exp_f32 v170, v170\n\t"
    "v_smfmac_f32_32x32x32_bf16 v[96:111], v[212:215], v[152:159], v124\n\t"
    "s_waitcnt lgkmcnt(3)\n\t"
    "v_exp_f32 v171, v171\n\t"
    "v_exp_f32 v172, v172\n\t"
    "v_exp_f32 v173, v173\n\t"
    "v_smfmac_f32_16x16x64_bf16 v[116:119], v[120:123], v[152:159], v124\n\t"
    "ds_read_b128 v[212:215], %[rb] offset:44032\n\t"
    "v_exp_f32 v174, v174\n\t"
    "v_exp_f32 v175, v175\n\t"
    "v_mfma_f32_32x32x16_bf16 v[128:143], v[192:195], v[32:35], v[48:63]\n\t"
    "ds_read_b64_tr_b16 a[86:87], v229 offset:18432\n\t"
    "ds_read_b64_tr_b16 a[88:89], v228 offset:20480\n\t"
    "v_exp_f32 v176, v176\n\t"
    "v_exp_f32 v177, v177\n\t"
    "v_exp_f32 v178, v178\n\t"
    "v_mfma_f32_32x32x16_bf16 v[128:143], v[196:199], v[36:39], v[128:143]\n\t"
    "v_exp_f32 v179, v179\n\t"
    "v_exp_f32 v180, v180\n\t"
    "v_exp_f32 v181, v181\n\t"
    "v_smfmac_f32_32x32x32_bf16 v[80:95], v[216:219], v[160:167], v124\n\t"
    "v_exp_f32 v182, v182\n\t"
    "v_exp_f32 v183, v183\n\t"
    "v_exp_f32 v184, v184\n\t"
    "v_smfmac_f32_16x16x64_bf16 v[112:115], v[120:123], v[160:167], v124\n\t"
    "ds_read_b128 v[200:203], %[rb] offset:45056\n\t"
    "v_exp_f32 v185, v185\n\t"
    "v_exp_f32 v186, v186\n\t"
    "v_smfmac_f32_32x32x32_bf16 v[80:95], v[220:223], v[168:175], v124\n\t"
    "v_exp_f32 v187, v187\n\t"
    "v_exp_f32 v188, v188\n\t"
    "v_exp_f32 v189, v189\n\t"
    "v_smfmac_f32_16x16x64_bf16 v[112:115], v[120:123], v[168:175], v124\n\t"
    "ds_read_b128 v[204:207], %[rb] offset:46080\n\t"
    "v_exp_f32 v190, v190\n\t"
    "v_exp_f32 v191, v191\n\t"
    "v_mfma_f32_32x32x16_bf16 v[144:159], v[192:195], v[40:43], v[64:79]\n\t"
    "ds_read_b64_tr_b16 a[90:91], v228 offset:22528\n\t"
    "ds_read_b64_tr_b16 a[92:93], v229 offset:20480\n\t"
    "ds_read_b64_tr_b16 a[94:95], v229 offset:22528\n\t"
    "v_exp_f32 v128, v128\n\t"
    "v_exp_f32 v129, v129\n\t"
    "v_exp_f32 v130, v130\n\t"
    "v_mfma_f32_32x32x16_bf16 v[144:159], v[196:199], v[44:47], v[144:159]\n\t"
    "v_exp_f32 v131, v131\n\t"
    "v_exp_f32 v132, v132\n\t"
    "v_exp_f32 v133, v133\n\t"
    "v_smfmac_f32_32x32x32_bf16 v[96:111], v[216:219], v[176:183], v124\n\t"
    "v_exp_f32 v134, v134\n\t"
    "v_exp_f32 v135, v135\n\t"
    "v_exp_f32 v136, v136\n\t"
    "v_smfmac_f32_16x16x64_bf16 v[116:119], v[120:123], v[176:183], v124\n\t"
    "ds_read_b128 v[216:219], %[rb] offset:47104\n\t"
    "v_exp_f32 v137, v137\n\t"
    "v_exp_f32 v138, v138\n\t"
    "v_smfmac_f32_32x32x32_bf16 v[96:111], v[220:223], v[184:191], v124\n\t"
    "s_waitcnt lgkmcnt(4)\n\t"
    "v_exp_f32 v139, v139\n\t"
    "v_exp_f32 v140, v140\n\t"
    "v_exp_f32 v141, v141\n\t"
    "v_smfmac_f32_16x16x64_bf16 v[116:119], v[120:123], v[184:191], v124\n\t"
    "ds_read_b128 v[220:223], %[rb] offset:48128\n\t"
    "v_exp_f32 v142, v142\n\t"
    "v_exp_f32 v143, v143\n\t"
    "v_mfma_f32_32x32x16_bf16 v[160:175], v[200:203], v[32:35], v[48:63]\n\t"
    "v_exp_f32 v144, v144\n\t"
    "v_exp_f32 v145, v145\n\t"
    "v_exp_f32 v146, v146\n\t"
    "v_mfma_f32_32x32x16_bf16 v[160:175], v[204:207], v[36:39], v[160:175]\n\t"
    "v_exp_f32 v147, v147\n\t"
    "v_exp_f32 v148, v148\n\t"
    "v_exp_f32 v149, v149\n\t"
    "v_smfmac_f32_32x32x32_bf16 v[80:95], v[208:211], v[128:135], v124\n\t"
    "s_waitcnt vmcnt(9) lgkmcnt(0)\n\t"
    "s_barrier\n\t"
    "v_exp_f32 v150, v150\n\t"
    "v_exp_f32 v151, v151\n\t"
    "v_exp_f32 v152, v152\n\t"
    "v_smfmac_f32_16x16x64_bf16 v[112:115], v[120:123], v[128:135], v124\n\t"
    "ds_read_b128 v[192:195], %[rb] offset:49152\n\t"
    "v_exp_f32 v153, v153\n\t"
    "v_exp_f32 v154, v154\n\t"
    "v_smfmac_f32_32x32x32_bf16 v[80:95], v[212:215], v[136:143], v124\n\t"
    "v_exp_f32 v155, v155\n\t"
    "v_exp_f32 v156, v156\n\t"
    "v_exp_f32 v157, v157\n\t"
    "v_smfmac_f32_16x16x64_bf16 v[112:115], v[120:123], v[136:143], v124\n\t"
    "ds_read_b128 v[196:199], %[rb] offset:50176\n\t"
    "v_exp_f32 v158, v158\n\t"
    "v_exp_f32 v159, v159\n\t"
    "v_mfma_f32_32x32x16_bf16 v[176:191], v[200:203], v[40:43], v[64:79]\n\t"
    "ds_read_b64_tr_b16 a[96:97], v228 offset:32768\n\t"
    "v_exp_f32 v160, v160\n\t"
    "v_exp_f32 v161, v161\n\t"
    "v_exp_f32 v162, v162\n\t"
    "v_mfma_f32_32x32x16_bf16 v[176:191], v[204:207], v[44:47], v[176:191]\n\t"
    "v_exp_f32 v163, v163\n\t"
    "v_exp_f32 v164, v164\n\t"
    "v_exp_f32 v165, v165\n\t"
    "v_smfmac_f32_32x32x32_bf16 v[96:111], v[208:211], v[144:151], v124\n\t"
    "v_exp_f32 v166, v166\n\t"
    "v_exp_f32 v167, v167\n\t"
    "v_exp_f32 v168, v168\n\t"
    "v_smfmac_f32_16x16x64_bf16 v[116:119], v[120:123], v[144:151], v124\n\t"
    "ds_read_b128 v[208:211], %[rb] offset:51200\n\t"
    "v_exp_f32 v169, v169\n\t"
    "v_exp_f32 v170, v170\n\t"
    "v_smfmac_f32_32x32x32_bf16 v[96:111], v[212:215], v[152:159], v124\n\t"
    "s_waitcnt lgkmcnt(2)\n\t"
    "v_exp_f32 v171, v171\n\t"
    "v_exp_f32 v172, v172\n\t"
    "v_exp_f32 v173, v173\n\t"
    "v_smfmac_f32_16x16x64_bf16 v[116:119], v[120:123], v[152:159], v124\n\t"
    "ds_read_b128 v[212:215], %[rb] offset:52224\n\t"
    "v_exp_f32 v174, v174\n\t"
    "v_exp_f32 v175, v175\n\t"
    "v_mfma_f32_32x32x16_bf16 v[128:143], v[192:195], v[32:35], v[48:63]\n\t"
    "ds_read_b64_tr_b16 a[98:99], v228 offset:34816\n\t"
    "s_add_u32 s52, %[krot], 163840\n\t"
    "v_exp_f32 v176, v176\n\t"
    "v_exp_f32 v177, v177\n\t"
    "v_exp_f32 v178, v178\n\t"
    "v_mfma_f32_32x32x16_bf16 v[128:143], v[196:199], v[36:39], v[128:143]\n\t"
    "s_add_u32 m0, s54, 32768\n\t"
    "s_and_b32 s52, s52, 0x3ffff\n\t"
    "v_exp_f32 v179, v179\n\t"
    "v_exp_f32 v180, v180\n\t"
    "v_exp_f32 v181, v181\n\t"
    "v_smfmac_f32_32x32x32_bf16 v[80:95], v[216:219], v[160:167], v124\n\t"
    "buffer_load_dwordx4 %[vg], s[40:43], s52 offen offset:0 sc1 lds\n\t"
    "buffer_load_dwordx4 %[vg], s[40:43], s52 offen offset:1024 sc1 lds\n\t"
    "v_exp_f32 v182, v182\n\t"
    "v_exp_f32 v183, v183\n\t"
    "v_exp_f32 v184, v184\n\t"
    "v_smfmac_f32_16x16x64_bf16 v[112:115], v[120:123], v[160:167], v124\n\t"
    "ds_read_b128 v[200:203], %[rb] offset:53248\n\t"
    "v_exp_f32 v185, v185\n\t"
    "v_exp_f32 v186, v186\n\t"
    "v_smfmac_f32_32x32x32_bf16 v[80:95], v[220:223], v[168:175], v124\n\t"
    "v_exp_f32 v187, v187\n\t"
    "v_exp_f32 v188, v188\n\t"
    "v_exp_f32 v189, v189\n\t"
    "v_smfmac_f32_16x16x64_bf16 v[112:115], v[120:123], v[168:175], v124\n\t"
    "ds_read_b128 v[204:207], %[rb] offset:54272\n\t"
    "v_exp_f32 v190, v190\n\t"
    "v_exp_f32 v191, v191\n\t"
    "v_mfma_f32_32x32x16_bf16 v[144:159], v[192:195], v[40:43], v[64:79]\n\t"
    "ds_read_b64_tr_b16 a[100:101], v229 offset:32768\n\t"
    "v_exp_f32 v128, v128\n\t"
    "v_exp_f32 v129, v129\n\t"
    "v_exp_f32 v130, v130\n\t"
    "v_mfma_f32_32x32x16_bf16 v[144:159], v[196:199], v[44:47], v[144:159]\n\t"
    "v_exp_f32 v131, v131\n\t"
    "v_exp_f32 v132, v132\n\t"
    "v_exp_f32 v133, v133\n\t"
    "v_smfmac_f32_32x32x32_bf16 v[96:111], v[216:219], v[176:183], v124\n\t"
    "buffer_load_dwordx4 %[vg], s[40:43], s52 offen offset:2048 sc1 lds\n\t"
    "buffer_load_dwordx4 %[vg], s[40:43], s52 offen offset:3072 sc1 lds\n\t"
    "v_exp_f32 v134, v134\n\t"
    "v_exp_f32 v135, v135\n\t"
    "v_exp_f32 v136, v136\n\t"
    "v_smfmac_f32_16x16x64_bf16 v[116:119], v[120:123], v[176:183], v124\n\t"
    "ds_read_b128 v[216:219], %[rb] offset:55296\n\t"
    "v_exp_f32 v137, v137\n\t"
    "v_exp_f32 v138, v138\n\t"
    "v_smfmac_f32_32x32x32_bf16 v[96:111], v[220:223], v[184:191], v124\n\t"
    "s_waitcnt lgkmcnt(2)\n\t"
    "v_exp_f32 v139, v139\n\t"
    "v_exp_f32 v140, v140\n\t"
    "v_exp_f32 v141, v141\n\t"
    "v_smfmac_f32_16x16x64_bf16 v[116:119], v[120:123], v[184:191], v124\n\t"
    "ds_read_b128 v[220:223], %[rb] offset:56320\n\t"
    "v_exp_f32 v142, v142\n\t"
    "v_exp_f32 v143, v143\n\t"
    "v_mfma_f32_32x32x16_bf16 v[160:175], v[200:203], v[32:35], v[48:63]\n\t"
    "ds_read_b64_tr_b16 a[102:103], v229 offset:34816\n\t"
    "s_mov_b64 s[44:45], %[pwa]\n\t"
    "v_exp_f32 v144, v144\n\t"
    "v_exp_f32 v145, v145\n\t"
    "v_exp_f32 v146, v146\n\t"
    "v_mfma_f32_32x32x16_bf16 v[160:175], v[204:207], v[36:39], v[160:175]\n\t"
    "s_add_u32 m0, s54, 65536\n\t"
    "s_mov_b32 s53, 0\n\t"
    "v_exp_f32 v147, v147\n\t"
    "v_exp_f32 v148, v148\n\t"
    "v_exp_f32 v149, v149\n\t"
    "v_smfmac_f32_32x32x32_bf16 v[80:95], v[208:211], v[128:135], v124\n\t"
    "buffer_load_dwordx4 v224, s[44:47], s53 offen offset:0 lds\n\t"
    "buffer_load_dwordx4 v225, s[44:47], s53 offen offset:1024 lds\n\t"
    "v_exp_f32 v150, v150\n\t"
    "v_exp_f32 v151, v151\n\t"
    "v_exp_f32 v152, v152\n\t"
    "v_smfmac_f32_16x16x64_bf16 v[112:115], v[120:123], v[128:135], v124\n\t"
    "ds_read_b128 v[192:195], %[rb] offset:57344\n\t"
    "v_exp_f32 v153, v153\n\t"
    "v_exp_f32 v154, v154\n\t"
    "v_smfmac_f32_32x32x32_bf16 v[80:95], v[212:215], v[136:143], v124\n\t"
    "v_exp_f32 v155, v155\n\t"
    "v_exp_f32 v156, v156\n\t"
    "v_exp_f32 v157, v157\n\t"
    "v_smfmac_f32_16x16x64_bf16 v[112:115], v[120:123], v[136:143], v124\n\t"
    "ds_read_b128 v[196:199], %[rb] offset:58368\n\t"
    "v_exp_f32 v158, v158\n\t"
    "v_exp_f32 v159, v159\n\t"
    "v_mfma_f32_32x32x16_bf16 v[176:191], v[200:203], v[40:43], v[64:79]\n\t"
    "ds_read_b64_tr_b16 a[104:105], v228 offset:36864\n\t"
    "v_exp_f32 v160, v160\n\t"
    "v_exp_f32 v161, v161\n\t"
    "v_exp_f32 v162, v162\n\t"
    "v_mfma_f32_32x32x16_bf16 v[176:191], v[204:207], v[44:47], v[176:191]\n\t"
    "v_exp_f32 v163, v163\n\t"
    "v_exp_f32 v164, v164\n\t"
    "v_exp_f32 v165, v165\n\t"
    "v_smfmac_f32_32x32x32_bf16 v[96:111], v[208:211], v[144:151], v124\n\t"
    "buffer_load_dwordx4 v226, s[44:47], s53 offen offset:2048 lds\n\t"
    "buffer_load_dwordx4 v227, s[44:47], s53 offen offset:3072 lds\n\t"
    "v_exp_f32 v166, v166\n\t"
    "v_exp_f32 v167, v167\n\t"
    "v_exp_f32 v168, v168\n\t"
    "v_smfmac_f32_16x16x64_bf16 v[116:119], v[120:123], v[144:151], v124\n\t"
    "ds_read_b128 v[208:211], %[rb] offset:59392\n\t"
    "v_exp_f32 v169, v169\n\t"
    "v_exp_f32 v170, v170\n\t"
    "v_smfmac_f32_32x32x32_bf16 v[96:111], v[212:215], v[152:159], v124\n\t"
    "s_waitcnt lgkmcnt(2)\n\t"
    "v_exp_f32 v171, v171\n\t"
    "v_exp_f32 v172, v172\n\t"
    "v_exp_f32 v173, v173\n\t"
    "v_smfmac_f32_16x16x64_bf16 v[116:119], v[120:123], v[152:159], v124\n\t"
    "ds_read_b128 v[212:215], %[rb] offset:60416\n\t"
    "v_exp_f32 v174, v174\n\t"
    "v_exp_f32 v175, v175\n\t"
    "v_mfma_f32_32x32x16_bf16 v[128:143], v[192:195], v[32:35], v[48:63]\n\t"
    "ds_read_b64_tr_b16 a[106:107], v228 offset:38912\n\t"
    "s_add_u32 m0, s54, 81920\n\t"
    "v_exp_f32 v176, v176\n\t"
    "v_exp_f32 v177, v177\n\t"
    "v_exp_f32 v178, v178\n\t"
    "v_mfma_f32_32x32x16_bf16 v[128:143], v[196:199], v[36:39], v[128:143]\n\t"
    "s_mov_b32 s53, 262144\n\t"
    "v_exp_f32 v179, v179\n\t"
    "v_exp_f32 v180, v180\n\t"
    "v_exp_f32 v181, v181\n\t"
    "v_smfmac_f32_32x32x32_bf16 v[80:95], v[216:219], v[160:167], v124\n\t"
    "buffer_load_dwordx4 v224, s[44:47], s53 offen offset:0 lds\n\t"
    "buffer_load_dwordx4 v225, s[44:47], s53 offen offset:1024 lds\n\t"
    "v_exp_f32 v182, v182\n\t"
    "v_exp_f32 v183, v183\n\t"
    "v_exp_f32 v184, v184\n\t"
    "v_smfmac_f32_16x16x64_bf16 v[112:115], v[120:123], v[160:167], v124\n\t"
    "ds_read_b128 v[200:203], %[rb] offset:61440\n\t"
    "v_exp_f32 v185, v185\n\t"
    "v_exp_f32 v186, v186\n\t"
    "v_smfmac_f32_32x32x32_bf16 v[80:95], v[220:223], v[168:175], v124\n\t"
    "v_exp_f32 v187, v187\n\t"
    "v_exp_f32 v188, v188\n\t"
    "v_exp_f32 v189, v189\n\t"
    "v_smfmac_f32_16x16x64_bf16 v[112:115], v[120:123], v[168:175], v124\n\t"
    "ds_read_b128 v[204:207], %[rb] offset:62464\n\t"
    "v_exp_f32 v190, v190\n\t"
    "v_exp_f32 v191, v191\n\t"
    "v_mfma_f32_32x32x16_bf16 v[144:159], v[192:195], v[40:43], v[64:79]\n\t"
    "ds_read_b64_tr_b16 a[108:109], v229 offset:36864\n\t"
    "ds_read_b64_tr_b16 a[110:111], v229 offset:38912\n\t"
    "v_exp_f32 v128, v128\n\t"
    "v_exp_f32 v129, v129\n\t"
    "v_exp_f32 v130, v130\n\t"
    "v_mfma_f32_32x32x16_bf16 v[144:159], v[196:199], v[44:47], v[144:159]\n\t"
    "v_exp_f32 v131, v131\n\t"
    "v_exp_f32 v132, v132\n\t"
    "v_exp_f32 v133, v133\n\t"
    "v_smfmac_f32_32x32x32_bf16 v[96:111], v[216:219], v[176:183], v124\n\t"
    "buffer_load_dwordx4 v226, s[44:47], s53 offen offset:2048 lds\n\t"
    "buffer_load_dwordx4 v227, s[44:47], s53 offen offset:3072 lds\n\t"
    "v_exp_f32 v134, v134\n\t"
    "v_exp_f32 v135, v135\n\t"
    "v_exp_f32 v136, v136\n\t"
    "v_smfmac_f32_16x16x64_bf16 v[116:119], v[120:123], v[176:183], v124\n\t"
    "ds_read_b128 v[216:219], %[rb] offset:63488\n\t"
    "v_exp_f32 v137, v137\n\t"
    "v_exp_f32 v138, v138\n\t"
    "v_smfmac_f32_32x32x32_bf16 v[96:111], v[220:223], v[184:191], v124\n\t"
    "s_waitcnt lgkmcnt(3)\n\t"
    "v_exp_f32 v139, v139\n\t"
    "v_exp_f32 v140, v140\n\t"
    "v_exp_f32 v141, v141\n\t"
    "v_smfmac_f32_16x16x64_bf16 v[116:119], v[120:123], v[184:191], v124\n\t"
    "ds_read_b128 v[220:223], %[rb] offset:64512\n\t"
    "v_exp_f32 v142, v142\n\t"
    "v_exp_f32 v143, v143\n\t"
    "v_mfma_f32_32x32x16_bf16 v[160:175], v[200:203], v[32:35], v[48:63]\n\t"
    "v_exp_f32 v144, v144\n\t"
    "v_exp_f32 v145, v145\n\t"
    "v_exp_f32 v146, v146\n\t"
    "v_mfma_f32_32x32x16_bf16 v[160:175], v[204:207], v[36:39], v[160:175]\n\t"
    "v_exp_f32 v147, v147\n\t"
    "v_exp_f32 v148, v148\n\t"
    "v_exp_f32 v149, v149\n\t"
    "v_smfmac_f32_32x32x32_bf16 v[80:95], v[208:211], v[128:135], v124\n\t"
    "s_waitcnt vmcnt(17) lgkmcnt(0)\n\t"
    "s_barrier\n\t"
    "v_exp_f32 v150, v150\n\t"
    "v_exp_f32 v151, v151\n\t"
    "v_exp_f32 v152, v152\n\t"
    "v_smfmac_f32_16x16x64_bf16 v[112:115], v[120:123], v[128:135], v124\n\t"
    "ds_read_b128 v[192:195], %[rb] offset:0\n\t"
    "v_exp_f32 v153, v153\n\t"
    "v_exp_f32 v154, v154\n\t"
    "v_smfmac_f32_32x32x32_bf16 v[80:95], v[212:215], v[136:143], v124\n\t"
    "v_exp_f32 v155, v155\n\t"
    "v_exp_f32 v156, v156\n\t"
    "v_exp_f32 v157, v157\n\t"
    "v_smfmac_f32_16x16x64_bf16 v[112:115], v[120:123], v[136:143], v124\n\t"
    "ds_read_b128 v[196:199], %[rb] offset:1024\n\t"
    "v_exp_f32 v158, v158\n\t"
    "v_exp_f32 v159, v159\n\t"
    "v_mfma_f32_32x32x16_bf16 v[176:191], v[200:203], v[40:43], v[64:79]\n\t"
    "ds_read_b64_tr_b16 a[112:113], v228 offset:49152\n\t"
    "s_add_u32 s52, %[krot], 180224\n\t"
    "v_exp_f32 v160, v160\n\t"
    "v_exp_f32 v161, v161\n\t"
    "v_exp_f32 v162, v162\n\t"
    "v_mfma_f32_32x32x16_bf16 v[176:191], v[204:207], v[44:47], v[176:191]\n\t"
    "s_add_u32 m0, s54, 49152\n\t"
    "s_and_b32 s52, s52, 0x3ffff\n\t"
    "v_exp_f32 v163, v163\n\t"
    "v_exp_f32 v164, v164\n\t"
    "v_exp_f32 v165, v165\n\t"
    "v_smfmac_f32_32x32x32_bf16 v[96:111], v[208:211], v[144:151], v124\n\t"
    "buffer_load_dwordx4 %[vg], s[40:43], s52 offen offset:0 sc1 lds\n\t"
    "buffer_load_dwordx4 %[vg], s[40:43], s52 offen offset:1024 sc1 lds\n\t"
    "v_exp_f32 v166, v166\n\t"
    "v_exp_f32 v167, v167\n\t"
    "v_exp_f32 v168, v168\n\t"
    "v_smfmac_f32_16x16x64_bf16 v[116:119], v[120:123], v[144:151], v124\n\t"
    "ds_read_b128 v[208:211], %[rb] offset:2048\n\t"
    "v_exp_f32 v169, v169\n\t"
    "v_exp_f32 v170, v170\n\t"
    "v_smfmac_f32_32x32x32_bf16 v[96:111], v[212:215], v[152:159], v124\n\t"
    "s_waitcnt lgkmcnt(2)\n\t"
    "global_load_dword v232, %[vpb], %[pf] sc1\n\t"
    "v_exp_f32 v171, v171\n\t"
    "v_exp_f32 v172, v172\n\t"
    "v_exp_f32 v173, v173\n\t"
    "v_smfmac_f32_16x16x64_bf16 v[116:119], v[120:123], v[152:159], v124\n\t"
    "ds_read_b128 v[212:215], %[rb] offset:3072\n\t"
    "v_exp_f32 v174, v174\n\t"
    "v_exp_f32 v175, v175\n\t"
    "v_mfma_f32_32x32x16_bf16 v[128:143], v[192:195], v[32:35], v[48:63]\n\t"
    "ds_read_b64_tr_b16 a[114:115], v228 offset:51200\n\t"
    "v_exp_f32 v176, v176\n\t"
    "v_exp_f32 v177, v177\n\t"
    "v_exp_f32 v178, v178\n\t"
    "v_mfma_f32_32x32x16_bf16 v[128:143], v[196:199], v[36:39], v[128:143]\n\t"
    "v_exp_f32 v179, v179\n\t"
    "v_exp_f32 v180, v180\n\t"
    "v_exp_f32 v181, v181\n\t"
    "v_smfmac_f32_32x32x32_bf16 v[80:95], v[216:219], v[160:167], v124\n\t"
    "v_exp_f32 v182, v182\n\t"
    "v_exp_f32 v183, v183\n\t"
    "v_exp_f32 v184, v184\n\t"
    "v_smfmac_f32_16x16x64_bf16 v[112:115], v[120:123], v[160:167], v124\n\t"
    "ds_read_b128 v[200:203], %[rb] offset:4096\n\t"
    "v_exp_f32 v185, v185\n\t"
    "v_exp_f32 v186, v186\n\t"
    "v_smfmac_f32_32x32x32_bf16 v[80:95], v[220:223], v[168:175], v124\n\t"
    "v_exp_f32 v187, v187\n\t"
    "v_exp_f32 v188, v188\n\t"
    "v_exp_f32 v189, v189\n\t"
    "v_smfmac_f32_16x16x64_bf16 v[112:115], v[120:123], v[168:175], v124\n\t"
    "ds_read_b128 v[204:207], %[rb] offset:5120\n\t"
    "v_exp_f32 v190, v190\n\t"
    "v_exp_f32 v191, v191\n\t"
    "v_mfma_f32_32x32x16_bf16 v[144:159], v[192:195], v[40:43], v[64:79]\n\t"
    "ds_read_b64_tr_b16 a[116:117], v229 offset:49152\n\t"
    "v_exp_f32 v128, v128\n\t"
    "v_exp_f32 v129, v129\n\t"
    "v_exp_f32 v130, v130\n\t"
    "v_mfma_f32_32x32x16_bf16 v[144:159], v[196:199], v[44:47], v[144:159]\n\t"
    "v_exp_f32 v131, v131\n\t"
    "v_exp_f32 v132, v132\n\t"
    "v_exp_f32 v133, v133\n\t"
    "v_smfmac_f32_32x32x32_bf16 v[96:111], v[216:219], v[176:183], v124\n\t"
    "buffer_load_dwordx4 %[vg], s[40:43], s52 offen offset:2048 sc1 lds\n\t"
    "buffer_load_dwordx4 %[vg], s[40:43], s52 offen offset:3072 sc1 lds\n\t"
    "v_exp_f32 v134, v134\n\t"
    "v_exp_f32 v135, v135\n\t"
    "v_exp_f32 v136, v136\n\t"
    "v_smfmac_f32_16x16x64_bf16 v[116:119], v[120:123], v[176:183], v124\n\t"
    "ds_read_b128 v[216:219], %[rb] offset:6144\n\t"
    "v_exp_f32 v137, v137\n\t"
    "v_exp_f32 v138, v138\n\t"
    "v_smfmac_f32_32x32x32_bf16 v[96:111], v[220:223], v[184:191], v124\n\t"
    "s_waitcnt lgkmcnt(2)\n\t"
    "v_exp_f32 v139, v139\n\t"
    "v_exp_f32 v140, v140\n\t"
    "v_exp_f32 v141, v141\n\t"
    "v_smfmac_f32_16x16x64_bf16 v[116:119], v[120:123], v[184:191], v124\n\t"
    "ds_read_b128 v[220:223], %[rb] offset:7168\n\t"
    "v_exp_f32 v142, v142\n\t"
    "v_exp_f32 v143, v143\n\t"
    "v_mfma_f32_32x32x16_bf16 v[160:175], v[200:203], v[32:35], v[48:63]\n\t"
    "ds_read_b64_tr_b16 a[118:119], v229 offset:51200\n\t"
    "v_exp_f32 v144, v144\n\t"
    "v_exp_f32 v145, v145\n\t"
    "v_exp_f32 v146, v146\n\t"
    "v_mfma_f32_32x32x16_bf16 v[160:175], v[204:207], v[36:39], v[160:175]\n\t"
    "v_exp_f32 v147, v147\n\t"
    "v_exp_f32 v148, v148\n\t"
    "v_exp_f32 v149, v149\n\t"
    "v_smfmac_f32_32x32x32_bf16 v[80:95], v[208:211], v[128:135], v124\n\t"
    "v_exp_f32 v150, v150\n\t"
    "v_exp_f32 v151, v151\n\t"
    "v_exp_f32 v152, v152\n\t"
    "v_smfmac_f32_16x16x64_bf16 v[112:115], v[120:123], v[128:135], v124\n\t"
    "ds_read_b128 v[192:195], %[rb] offset:8192\n\t"
    "v_exp_f32 v153, v153\n\t"
    "v_exp_f32 v154, v154\n\t"
    "v_smfmac_f32_32x32x32_bf16 v[80:95], v[212:215], v[136:143], v124\n\t"
    "v_exp_f32 v155, v155\n\t"
    "v_exp_f32 v156, v156\n\t"
    "v_exp_f32 v157, v157\n\t"
    "v_smfmac_f32_16x16x64_bf16 v[112:115], v[120:123], v[136:143], v124\n\t"
    "ds_read_b128 v[196:199], %[rb] offset:9216\n\t"
    "v_exp_f32 v158, v158\n\t"
    "v_exp_f32 v159, v159\n\t"
    "v_mfma_f32_32x32x16_bf16 v[176:191], v[200:203], v[40:43], v[64:79]\n\t"
    "ds_read_b64_tr_b16 a[120:121], v228 offset:53248\n\t"
    "s_add_u32 m0, s54, 98304\n\t"
    "v_exp_f32 v160, v160\n\t"
    "v_exp_f32 v161, v161\n\t"
    "v_exp_f32 v162, v162\n\t"
    "v_mfma_f32_32x32x16_bf16 v[176:191], v[204:207], v[44:47], v[176:191]\n\t"
    "s_mov_b32 s53, 524288\n\t"
    "v_exp_f32 v163, v163\n\t"
    "v_exp_f32 v164, v164\n\t"
    "v_exp_f32 v165, v165\n\t"
    "v_smfmac_f32_32x32x32_bf16 v[96:111], v[208:211], v[144:151], v124\n\t"
    "buffer_load_dwordx4 v224, s[44:47], s53 offen offset:0 lds\n\t"
    "buffer_load_dwordx4 v225, s[44:47], s53 offen offset:1024 lds\n\t"
    "v_exp_f32 v166, v166\n\t"
    "v_exp_f32 v167, v167\n\t"
    "v_exp_f32 v168, v168\n\t"
    "v_smfmac_f32_16x16x64_bf16 v[116:119], v[120:123], v[144:151], v124\n\t"
    "ds_read_b128 v[208:211], %[rb] offset:10240\n\t"
    "v_exp_f32 v169, v169\n\t"
    "v_exp_f32 v170, v170\n\t"
    "v_smfmac_f32_32x32x32_bf16 v[96:111], v[212:215], v[152:159], v124\n\t"
    "s_waitcnt lgkmcnt(2)\n\t"
    "v_exp_f32 v171, v171\n\t"
    "v_exp_f32 v172, v172\n\t"
    "v_exp_f32 v173, v173\n\t"
    "v_smfmac_f32_16x16x64_bf16 v[116:119], v[120:123], v[152:159], v124\n\t"
    "ds_read_b128 v[212:215], %[rb] offset:11264\n\t"
    "v_exp_f32 v174, v174\n\t"
    "v_exp_f32 v175, v175\n\t"
    "v_mfma_f32_32x32x16_bf16 v[128:143], v[192:195], v[32:35], v[48:63]\n\t"
    "ds_read_b64_tr_b16 a[122:123], v228 offset:55296\n\t"
    "v_exp_f32 v176, v176\n\t"
    "v_exp_f32 v177, v177\n\t"
    "v_exp_f32 v178, v178\n\t"
    "v_mfma_f32_32x32x16_bf16 v[128:143], v[196:199], v[36:39], v[128:143]\n\t"
    "v_exp_f32 v179, v179\n\t"
    "v_exp_f32 v180, v180\n\t"
    "v_exp_f32 v181, v181\n\t"
    "v_smfmac_f32_32x32x32_bf16 v[80:95], v[216:219], v[160:167], v124\n\t"
    "v_exp_f32 v182, v182\n\t"
    "v_exp_f32 v183, v183\n\t"
    "v_exp_f32 v184, v184\n\t"
    "v_smfmac_f32_16x16x64_bf16 v[112:115], v[120:123], v[160:167], v124\n\t"
    "ds_read_b128 v[200:203], %[rb] offset:12288\n\t"
    "v_exp_f32 v185, v185\n\t"
    "v_exp_f32 v186, v186\n\t"
    "v_smfmac_f32_32x32x32_bf16 v[80:95], v[220:223], v[168:175], v124\n\t"
    "v_exp_f32 v187, v187\n\t"
    "v_exp_f32 v188, v188\n\t"
    "v_exp_f32 v189, v189\n\t"
    "v_smfmac_f32_16x16x64_bf16 v[112:115], v[120:123], v[168:175], v124\n\t"
    "ds_read_b128 v[204:207], %[rb] offset:13312\n\t"
    "v_exp_f32 v190, v190\n\t"
    "v_exp_f32 v191, v191\n\t"
    "v_mfma_f32_32x32x16_bf16 v[144:159], v[192:195], v[40:43], v[64:79]\n\t"
    "ds_read_b64_tr_b16 a[124:125], v229 offset:53248\n\t"
    "ds_read_b64_tr_b16 a[126:127], v229 offset:55296\n\t"
    "v_exp_f32 v128, v128\n\t"
    "v_exp_f32 v129, v129\n\t"
    "v_exp_f32 v130, v130\n\t"
    "v_mfma_f32_32x32x16_bf16 v[144:159], v[196:199], v[44:47], v[144:159]\n\t"
    "v_exp_f32 v131, v131\n\t"
    "v_exp_f32 v132, v132\n\t"
    "v_exp_f32 v133, v133\n\t"
    "v_smfmac_f32_32x32x32_bf16 v[96:111], v[216:219], v[176:183], v124\n\t"
    "buffer_load_dwordx4 v226, s[44:47], s53 offen offset:2048 lds\n\t"
    "buffer_load_dwordx4 v227, s[44:47], s53 offen offset:3072 lds\n\t"
    "v_exp_f32 v134, v134\n\t"
    "v_exp_f32 v135, v135\n\t"
    "v_exp_f32 v136, v136\n\t"
    "v_smfmac_f32_16x16x64_bf16 v[116:119], v[120:123], v[176:183], v124\n\t"
    "ds_read_b128 v[216:219], %[rb] offset:14336\n\t"
    "v_exp_f32 v137, v137\n\t"
    "v_exp_f32 v138, v138\n\t"
    "v_smfmac_f32_32x32x32_bf16 v[96:111], v[220:223], v[184:191], v124\n\t"
    "s_waitcnt lgkmcnt(3)\n\t"
    "v_exp_f32 v139, v139\n\t"
    "v_exp_f32 v140, v140\n\t"
    "v_exp_f32 v141, v141\n\t"
    "v_smfmac_f32_16x16x64_bf16 v[116:119], v[120:123], v[184:191], v124\n\t"
    "ds_read_b128 v[220:223], %[rb] offset:15360\n\t"
    "v_exp_f32 v142, v142\n\t"
    "v_exp_f32 v143, v143\n\t"
    "v_mfma_f32_32x32x16_bf16 v[160:175], v[200:203], v[32:35], v[48:63]\n\t"
    "v_exp_f32 v144, v144\n\t"
    "v_exp_f32 v145, v145\n\t"
    "v_exp_f32 v146, v146\n\t"
    "v_mfma_f32_32x32x16_bf16 v[160:175], v[204:207], v[36:39], v[160:175]\n\t"
    "v_exp_f32 v147, v147\n\t"
    "v_exp_f32 v148, v148\n\t"
    "v_exp_f32 v149, v149\n\t"
    "v_smfmac_f32_32x32x32_bf16 v[80:95], v[208:211], v[128:135], v124\n\t"
    "s_waitcnt vmcnt(13) lgkmcnt(0)\n\t"
    "s_barrier\n\t"
    "v_exp_f32 v150, v150\n\t"
    "v_exp_f32 v151, v151\n\t"
    "v_exp_f32 v152, v152\n\t"
    "v_smfmac_f32_16x16x64_bf16 v[112:115], v[120:123], v[128:135], v124\n\t"
    "ds_read_b128 v[192:195], %[rb] offset:16384\n\t"
    "v_exp_f32 v153, v153\n\t"
    "v_exp_f32 v154, v154\n\t"
    "v_smfmac_f32_32x32x32_bf16 v[80:95], v[212:215], v[136:143], v124\n\t"
    "v_exp_f32 v155, v155\n\t"
    "v_exp_f32 v156, v156\n\t"
    "v_exp_f32 v157, v157\n\t"
    "v_smfmac_f32_16x16x64_bf16 v[112:115], v[120:123], v[136:143], v124\n\t"
    "ds_read_b128 v[196:199], %[rb] offset:17408\n\t"
    "v_exp_f32 v158, v158\n\t"
    "v_exp_f32 v159, v159\n\t"
    "v_mfma_f32_32x32x16_bf16 v[176:191], v[200:203], v[40:43], v[64:79]\n\t"
    "ds_read_b64_tr_b16 a[128:129], v228 offset:0\n\t"
    "s_add_u32 s52, %[krot], 196608\n\t"
    "v_exp_f32 v160, v160\n\t"
    "v_exp_f32 v161, v161\n\t"
    "v_exp_f32 v162, v162\n\t"
    "v_mfma_f32_32x32x16_bf16 v[176:191], v[204:207], v[44:47], v[176:191]\n\t"
    "s_add_u32 m0, s54, 0\n\t"
    "s_and_b32 s52, s52, 0x3ffff\n\t"
    "v_exp_f32 v163, v163\n\t"
    "v_exp_f32 v164, v164\n\t"
    "v_exp_f32 v165, v165\n\t"
    "v_smfmac_f32_32x32x32_bf16 v[96:111], v[208:211], v[144:151], v124\n\t"
    "buffer_load_dwordx4 %[vg], s[40:43], s52 offen offset:0 sc1 lds\n\t"
    "buffer_load_dwordx4 %[vg], s[40:43], s52 offen offset:1024 sc1 lds\n\t"
    "v_exp_f32 v166, v166\n\t"
    "v_exp_f32 v167, v167\n\t"
    "v_exp_f32 v168, v168\n\t"
    "v_smfmac_f32_16x16x64_bf16 v[116:119], v[120:123], v[144:151], v124\n\t"
    "ds_read_b128 v[208:211], %[rb] offset:18432\n\t"
    "v_exp_f32 v169, v169\n\t"
    "v_exp_f32 v170, v170\n\t"
    "v_smfmac_f32_32x32x32_bf16 v[96:111], v[212:215], v[152:159], v124\n\t"
    "s_waitcnt lgkmcnt(2)\n\t"
    "v_exp_f32 v171, v171\n\t"
    "v_exp_f32 v172, v172\n\t"
    "v_exp_f32 v173, v173\n\t"
    "v_smfmac_f32_16x16x64_bf16 v[116:119], v[120:123], v[152:159], v124\n\t"
    "ds_read_b128 v[212:215], %[rb] offset:19456\n\t"
    "v_exp_f32 v174, v174\n\t"
    "v_exp_f32 v175, v175\n\t"
    "v_mfma_f32_32x32x16_bf16 v[128:143], v[192:195], v[32:35], v[48:63]\n\t"
    "ds_read_b64_tr_b16 a[130:131], v228 offset:2048\n\t"
    "v_exp_f32 v176, v176\n\t"
    "v_exp_f32 v177, v177\n\t"
    "v_exp_f32 v178, v178\n\t"
    "v_mfma_f32_32x32x16_bf16 v[128:143], v[196:199], v[36:39], v[128:143]\n\t"
    "v_exp_f32 v179, v179\n\t"
    "v_exp_f32 v180, v180\n\t"
    "v_exp_f32 v181, v181\n\t"
    "v_smfmac_f32_32x32x32_bf16 v[80:95], v[216:219], v[160:167], v124\n\t"
    "v_exp_f32 v182, v182\n\t"
    "v_exp_f32 v183, v183\n\t"
    "v_exp_f32 v184, v184\n\t"
    "v_smfmac_f32_16x16x64_bf16 v[112:115], v[120:123], v[160:167], v124\n\t"
    "ds_read_b128 v[200:203], %[rb] offset:20480\n\t"
    "v_exp_f32 v185, v185\n\t"
    "v_exp_f32 v186, v186\n\t"
    "v_smfmac_f32_32x32x32_bf16 v[80:95], v[220:223], v[168:175], v124\n\t"
    "v_exp_f32 v187, v187\n\t"
    "v_exp_f32 v188, v188\n\t"
    "v_exp_f32 v189, v189\n\t"
    "v_smfmac_f32_16x16x64_bf16 v[112:115], v[120:123], v[168:175], v124\n\t"
    "ds_read_b128 v[204:207], %[rb] offset:21504\n\t"
    "v_exp_f32 v190, v190\n\t"
    "v_exp_f32 v191, v191\n\t"
    "v_mfma_f32_32x32x16_bf16 v[144:159], v[192:195], v[40:43], v[64:79]\n\t"
    "ds_read_b64_tr_b16 a[132:133], v229 offset:0\n\t"
    "v_exp_f32 v128, v128\n\t"
    "v_exp_f32 v129, v129\n\t"
    "v_exp_f32 v130, v130\n\t"
    "v_mfma_f32_32x32x16_bf16 v[144:159], v[196:199], v[44:47], v[144:159]\n\t"
    "v_exp_f32 v131, v131\n\t"
    "v_exp_f32 v132, v132\n\t"
    "v_exp_f32 v133, v133\n\t"
    "v_smfmac_f32_32x32x32_bf16 v[96:111], v[216:219], v[176:183], v124\n\t"
    "buffer_load_dwordx4 %[vg], s[40:43], s52 offen offset:2048 sc1 lds\n\t"
    "buffer_load_dwordx4 %[vg], s[40:43], s52 offen offset:3072 sc1 lds\n\t"
    "v_exp_f32 v134, v134\n\t"
    "v_exp_f32 v135, v135\n\t"
    "v_exp_f32 v136, v136\n\t"
    "v_smfmac_f32_16x16x64_bf16 v[116:119], v[120:123], v[176:183], v124\n\t"
    "ds_read_b128 v[216:219], %[rb] offset:22528\n\t"
    "v_exp_f32 v137, v137\n\t"
    "v_exp_f32 v138, v138\n\t"
    "v_smfmac_f32_32x32x32_bf16 v[96:111], v[220:223], v[184:191], v124\n\t"
    "s_waitcnt lgkmcnt(2)\n\t"
    "global_load_dwordx4 v[250:253], v248, s[62:63]\n\t"
    "v_exp_f32 v139, v139\n\t"
    "v_exp_f32 v140, v140\n\t"
    "v_exp_f32 v141, v141\n\t"
    "v_smfmac_f32_16x16x64_bf16 v[116:119], v[120:123], v[184:191], v124\n\t"
    "ds_read_b128 v[220:223], %[rb] offset:23552\n\t"
    "v_exp_f32 v142, v142\n\t"
    "v_exp_f32 v143, v143\n\t"
    "v_mfma_f32_32x32x16_bf16 v[160:175], v[200:203], v[32:35], v[48:63]\n\t"
    "ds_read_b64_tr_b16 a[134:135], v229 offset:2048\n\t"
    "v_exp_f32 v144, v144\n\t"
    "v_exp_f32 v145, v145\n\t"
    "v_exp_f32 v146, v146\n\t"
    "v_mfma_f32_32x32x16_bf16 v[160:175], v[204:207], v[36:39], v[160:175]\n\t"
    "v_exp_f32 v147, v147\n\t"
    "v_exp_f32 v148, v148\n\t"
    "v_exp_f32 v149, v149\n\t"
    "v_smfmac_f32_32x32x32_bf16 v[80:95], v[208:211], v[128:135], v124\n\t"
    "v_exp_f32 v150, v150\n\t"
    "v_exp_f32 v151, v151\n\t"
    "v_exp_f32 v152, v152\n\t"
    "v_smfmac_f32_16x16x64_bf16 v[112:115], v[120:123], v[128:135], v124\n\t"
    "ds_read_b128 v[192:195], %[rb] offset:24576\n\t"
    "v_exp_f32 v153, v153\n\t"
    "v_exp_f32 v154, v154\n\t"
    "v_smfmac_f32_32x32x32_bf16 v[80:95], v[212:215], v[136:143], v124\n\t"
    "v_exp_f32 v155, v155\n\t"
    "v_exp_f32 v156, v156\n\t"
    "v_exp_f32 v157, v157\n\t"
    "v_smfmac_f32_16x16x64_bf16 v[112:115], v[120:123], v[136:143], v124\n\t"
    "ds_read_b128 v[196:199], %[rb] offset:25600\n\t"
    "v_exp_f32 v158, v158\n\t"
    "v_exp_f32 v159, v159\n\t"
    "v_mfma_f32_32x32x16_bf16 v[176:191], v[200:203], v[40:43], v[64:79]\n\t"
    "ds_read_b64_tr_b16 a[136:137], v228 offset:4096\n\t"
    "s_add_u32 m0, s54, 114688\n\t"
    "v_exp_f32 v160, v160\n\t"
    "v_exp_f32 v161, v161\n\t"
    "v_exp_f32 v162, v162\n\t"
    "v_mfma_f32_32x32x16_bf16 v[176:191], v[204:207], v[44:47], v[176:191]\n\t"
    "s_mov_b32 s53, 786432\n\t"
    "v_exp_f32 v163, v163\n\t"
    "v_exp_f32 v164, v164\n\t"
    "v_exp_f32 v165, v165\n\t"
    "v_smfmac_f32_32x32x32_bf16 v[96:111], v[208:211], v[144:151], v124\n\t"
    "buffer_load_dwordx4 v224, s[44:47], s53 offen offset:0 lds\n\t"
    "buffer_load_dwordx4 v225, s[44:47], s53 offen offset:1024 lds\n\t"
    "v_exp_f32 v166, v166\n\t"
    "v_exp_f32 v167, v167\n\t"
    "v_exp_f32 v168, v168\n\t"
    "v_smfmac_f32_16x16x64_bf16 v[116:119], v[120:123], v[144:151], v124\n\t"
    "ds_read_b128 v[208:211], %[rb] offset:26624\n\t"
    "v_exp_f32 v169, v169\n\t"
    "v_exp_f32 v170, v170\n\t"
    "v_smfmac_f32_32x32x32_bf16 v[96:111], v[212:215], v[152:159], v124\n\t"
    "s_waitcnt lgkmcnt(2)\n\t"
    "v_exp_f32 v171, v171\n\t"
    "v_exp_f32 v172, v172\n\t"
    "v_exp_f32 v173, v173\n\t"
    "v_smfmac_f32_16x16x64_bf16 v[116:119], v[120:123], v[152:159], v124\n\t"
    "ds_read_b128 v[212:215], %[rb] offset:27648\n\t"
    "v_exp_f32 v174, v174\n\t"
    "v_exp_f32 v175, v175\n\t"
    "v_mfma_f32_32x32x16_bf16 v[128:143], v[192:195], v[32:35], v[48:63]\n\t"
    "ds_read_b64_tr_b16 a[138:139], v228 offset:6144\n\t"
    "v_exp_f32 v176, v176\n\t"
    "v_exp_f32 v177, v177\n\t"
    "v_exp_f32 v178, v178\n\t"
    "v_mfma_f32_32x32x16_bf16 v[128:143], v[196:199], v[36:39], v[128:143]\n\t"
    "v_exp_f32 v179, v179\n\t"
    "v_exp_f32 v180, v180\n\t"
    "v_exp_f32 v181, v181\n\t"
    "v_smfmac_f32_32x32x32_bf16 v[80:95], v[216:219], v[160:167], v124\n\t"
    "v_exp_f32 v182, v182\n\t"
    "v_exp_f32 v183, v183\n\t"
    "v_exp_f32 v184, v184\n\t"
    "v_smfmac_f32_16x16x64_bf16 v[112:115], v[120:123], v[160:167], v124\n\t"
    "ds_read_b128 v[200:203], %[rb] offset:28672\n\t"
    "v_exp_f32 v185, v185\n\t"
    "v_exp_f32 v186, v186\n\t"
    "v_smfmac_f32_32x32x32_bf16 v[80:95], v[220:223], v[168:175], v124\n\t"
    "v_exp_f32 v187, v187\n\t"
    "v_exp_f32 v188, v188\n\t"
    "v_exp_f32 v189, v189\n\t"
    "v_smfmac_f32_16x16x64_bf16 v[112:115], v[120:123], v[168:175], v124\n\t"
    "ds_read_b128 v[204:207], %[rb] offset:29696\n\t"
    "v_exp_f32 v190, v190\n\t"
    "v_exp_f32 v191, v191\n\t"
    "v_mfma_f32_32x32x16_bf16 v[144:159], v[192:195], v[40:43], v[64:79]\n\t"
    "ds_read_b64_tr_b16 a[140:141], v229 offset:4096\n\t"
    "ds_read_b64_tr_b16 a[142:143], v229 offset:6144\n\t"
    "v_exp_f32 v128, v128\n\t"
    "v_exp_f32 v129, v129\n\t"
    "v_exp_f32 v130, v130\n\t"
    "v_mfma_f32_32x32x16_bf16 v[144:159], v[196:199], v[44:47], v[144:159]\n\t"
    "v_exp_f32 v131, v131\n\t"
    "v_exp_f32 v132, v132\n\t"
    "v_exp_f32 v133, v133\n\t"
    "v_smfmac_f32_32x32x32_bf16 v[96:111], v[216:219], v[176:183], v124\n\t"
    "buffer_load_dwordx4 v226, s[44:47], s53 offen offset:2048 lds\n\t"
    "buffer_load_dwordx4 v227, s[44:47], s53 offen offset:3072 lds\n\t"
    "v_exp_f32 v134, v134\n\t"
    "v_exp_f32 v135, v135\n\t"
    "v_exp_f32 v136, v136\n\t"
    "v_smfmac_f32_16x16x64_bf16 v[116:119], v[120:123], v[176:183], v124\n\t"
    "ds_read_b128 v[216:219], %[rb] offset:30720\n\t"
    "v_exp_f32 v137, v137\n\t"
    "v_exp_f32 v138, v138\n\t"
    "v_smfmac_f32_32x32x32_bf16 v[96:111], v[220:223], v[184:191], v124\n\t"
    "s_waitcnt lgkmcnt(3)\n\t"
    "v_exp_f32 v139, v139\n\t"
    "v_exp_f32 v140, v140\n\t"
    "v_exp_f32 v141, v141\n\t"
    "v_smfmac_f32_16x16x64_bf16 v[116:119], v[120:123], v[184:191], v124\n\t"
    "ds_read_b128 v[220:223], %[rb] offset:31744\n\t"
    "v_exp_f32 v142, v142\n\t"
    "v_exp_f32 v143, v143\n\t"
    "v_mfma_f32_32x32x16_bf16 v[160:175], v[200:203], v[32:35], v[48:63]\n\t"
    "v_exp_f32 v144, v144\n\t"
    "v_exp_f32 v145, v145\n\t"
    "v_exp_f32 v146, v146\n\t"
    "v_mfma_f32_32x32x16_bf16 v[160:175], v[204:207], v[36:39], v[160:175]\n\t"
    "v_exp_f32 v147, v147\n\t"
    "v_exp_f32 v148, v148\n\t"
    "v_exp_f32 v149, v149\n\t"
    "v_smfmac_f32_32x32x32_bf16 v[80:95], v[208:211], v[128:135], v124\n\t"
    "s_waitcnt vmcnt(18) lgkmcnt(0)\n\t"
    "s_barrier\n\t"
    "v_exp_f32 v150, v150\n\t"
    "v_exp_f32 v151, v151\n\t"
    "v_exp_f32 v152, v152\n\t"
    "v_smfmac_f32_16x16x64_bf16 v[112:115], v[120:123], v[128:135], v124\n\t"
    "ds_read_b128 v[192:195], %[rb] offset:32768\n\t"
    "v_exp_f32 v153, v153\n\t"
    "v_exp_f32 v154, v154\n\t"
    "v_smfmac_f32_32x32x32_bf16 v[80:95], v[212:215], v[136:143], v124\n\t"
    "v_exp_f32 v155, v155\n\t"
    "v_exp_f32 v156, v156\n\t"
    "v_exp_f32 v157, v157\n\t"
    "v_smfmac_f32_16x16x64_bf16 v[112:115], v[120:123], v[136:143], v124\n\t"
    "ds_read_b128 v[196:199], %[rb] offset:33792\n\t"
    "v_exp_f32 v158, v158\n\t"
    "v_exp_f32 v159, v159\n\t"
    "v_mfma_f32_32x32x16_bf16 v[176:191], v[200:203], v[40:43], v[64:79]\n\t"
    "ds_read_b64_tr_b16 a[144:145], v228 offset:16384\n\t"
    "s_add_u32 s52, %[krot], 212992\n\t"
    "v_exp_f32 v160, v160\n\t"
    "v_exp_f32 v161, v161\n\t"
    "v_exp_f32 v162, v162\n\t"
    "v_mfma_f32_32x32x16_bf16 v[176:191], v[204:207], v[44:47], v[176:191]\n\t"
    "s_add_u32 m0, s54, 16384\n\t"
    "s_and_b32 s52, s52, 0x3ffff\n\t"
    "v_exp_f32 v163, v163\n\t"
    "v_exp_f32 v164, v164\n\t"
    "v_exp_f32 v165, v165\n\t"
    "v_smfmac_f32_32x32x32_bf16 v[96:111], v[208:211], v[144:151], v124\n\t"
    "buffer_load_dwordx4 %[vg], s[40:43], s52 offen offset:0 sc1 lds\n\t"
    "buffer_load_dwordx4 %[vg], s[40:43], s52 offen offset:1024 sc1 lds\n\t"
    "v_exp_f32 v166, v166\n\t"
    "v_exp_f32 v167, v167\n\t"
    "v_exp_f32 v168, v168\n\t"
    "v_smfmac_f32_16x16x64_bf16 v[116:119], v[120:123], v[144:151], v124\n\t"
    "ds_read_b128 v[208:211], %[rb] offset:34816\n\t"
    "v_exp_f32 v169, v169\n\t"
    "v_exp_f32 v170, v170\n\t"
    "v_smfmac_f32_32x32x32_bf16 v[96:111], v[212:215], v[152:159], v124\n\t"
    "s_waitcnt lgkmcnt(2)\n\t"
    "v_exp_f32 v171, v171\n\t"
    "v_exp_f32 v172, v172\n\t"
    "v_exp_f32 v173, v173\n\t"
    "v_smfmac_f32_16x16x64_bf16 v[116:119], v[120:123], v[152:159], v124\n\t"
    "ds_read_b128 v[212:215], %[rb] offset:35840\n\t"
    "v_exp_f32 v174, v174\n\t"
    "v_exp_f32 v175, v175\n\t"
    "v_mfma_f32_32x32x16_bf16 v[128:143], v[192:195], v[32:35], v[48:63]\n\t"
    "ds_read_b64_tr_b16 a[146:147], v228 offset:18432\n\t"
    "v_exp_f32 v176, v176\n\t"
    "v_exp_f32 v177, v177\n\t"
    "v_exp_f32 v178, v178\n\t"
    "v_mfma_f32_32x32x16_bf16 v[128:143], v[196:199], v[36:39], v[128:143]\n\t"
    "v_exp_f32 v179, v179\n\t"
    "v_exp_f32 v180, v180\n\t"
    "v_exp_f32 v181, v181\n\t"
    "v_smfmac_f32_32x32x32_bf16 v[80:95], v[216:219], v[160:167], v124\n\t"
    "v_exp_f32 v182, v182\n\t"
    "v_exp_f32 v183, v183\n\t"
    "v_exp_f32 v184, v184\n\t"
    "v_smfmac_f32_16x16x64_bf16 v[112:115], v[120:123], v[160:167], v124\n\t"
    "ds_read_b128 v[200:203], %[rb] offset:36864\n\t"
    "v_exp_f32 v185, v185\n\t"
    "v_exp_f32 v186, v186\n\t"
    "v_smfmac_f32_32x32x32_bf16 v[80:95], v[220:223], v[168:175], v124\n\t"
    "v_exp_f32 v187, v187\n\t"
    "v_exp_f32 v188, v188\n\t"
    "v_exp_f32 v189, v189\n\t"
    "v_smfmac_f32_16x16x64_bf16 v[112:115], v[120:123], v[168:175], v124\n\t"
    "ds_read_b128 v[204:207], %[rb] offset:37888\n\t"
    "v_exp_f32 v190, v190\n\t"
    "v_exp_f32 v191, v191\n\t"
    "v_mfma_f32_32x32x16_bf16 v[144:159], v[192:195], v[40:43], v[64:79]\n\t"
    "ds_read_b64_tr_b16 a[148:149], v229 offset:16384\n\t"
    "v_exp_f32 v128, v128\n\t"
    "v_exp_f32 v129, v129\n\t"
    "v_exp_f32 v130, v130\n\t"
    "v_mfma_f32_32x32x16_bf16 v[144:159], v[196:199], v[44:47], v[144:159]\n\t"
    "v_exp_f32 v131, v131\n\t"
    "v_exp_f32 v132, v132\n\t"
    "v_exp_f32 v133, v133\n\t"
    "v_smfmac_f32_32x32x32_bf16 v[96:111], v[216:219], v[176:183], v124\n\t"
    "buffer_load_dwordx4 %[vg], s[40:43], s52 offen offset:2048 sc1 lds\n\t"
    "buffer_load_dwordx4 %[vg], s[40:43], s52 offen offset:3072 sc1 lds\n\t"
    "v_exp_f32 v134, v134\n\t"
    "v_exp_f32 v135, v135\n\t"
    "v_exp_f32 v136, v136\n\t"
    "v_smfmac_f32_16x16x64_bf16 v[116:119], v[120:123], v[176:183], v124\n\t"
    "ds_read_b128 v[216:219], %[rb] offset:38912\n\t"
    "v_exp_f32 v137, v137\n\t"
    "v_exp_f32 v138, v138\n\t"
    "v_smfmac_f32_32x32x32_bf16 v[96:111], v[220:223], v[184:191], v124\n\t"
    "s_waitcnt lgkmcnt(2)\n\t"
    "v_exp_f32 v139, v139\n\t"
    "v_exp_f32 v140, v140\n\t"
    "v_exp_f32 v141, v141\n\t"
    "v_smfmac_f32_16x16x64_bf16 v[116:119], v[120:123], v[184:191], v124\n\t"
    "ds_read_b128 v[220:223], %[rb] offset:39936\n\t"
    "v_exp_f32 v142, v142\n\t"
    "v_exp_f32 v143, v143\n\t"
    "v_mfma_f32_32x32x16_bf16 v[160:175], v[200:203], v[32:35], v[48:63]\n\t"
    "ds_read_b64_tr_b16 a[150:151], v229 offset:18432\n\t"
    "v_exp_f32 v144, v144\n\t"
    "v_exp_f32 v145, v145\n\t"
    "v_exp_f32 v146, v146\n\t"
    "v_mfma_f32_32x32x16_bf16 v[160:175], v[204:207], v[36:39], v[160:175]\n\t"
    "v_exp_f32 v147, v147\n\t"
    "v_exp_f32 v148, v148\n\t"
    "v_exp_f32 v149, v149\n\t"
    "v_smfmac_f32_32x32x32_bf16 v[80:95], v[208:211], v[128:135], v124\n\t"
    "v_exp_f32 v150, v150\n\t"
    "v_exp_f32 v151, v151\n\t"
    "v_exp_f32 v152, v152\n\t"
    "v_smfmac_f32_16x16x64_bf16 v[112:115], v[120:123], v[128:135], v124\n\t"
    "ds_read_b128 v[192:195], %[rb] offset:40960\n\t"
    "v_exp_f32 v153, v153\n\t"
    "v_exp_f32 v154, v154\n\t"
    "v_smfmac_f32_32x32x32_bf16 v[80:95], v[212:215], v[136:143], v124\n\t"
    "v_exp_f32 v155, v155\n\t"
    "v_exp_f32 v156, v156\n\t"
    "v_exp_f32 v157, v157\n\t"
    "v_smfmac_f32_16x16x64_bf16 v[112:115], v[120:123], v[136:143], v124\n\t"
    "ds_read_b128 v[196:199], %[rb] offset:41984\n\t"
    "v_exp_f32 v158, v158\n\t"
    "v_exp_f32 v159, v159\n\t"
    "v_mfma_f32_32x32x16_bf16 v[176:191], v[200:203], v[40:43], v[64:79]\n\t"
    "ds_read_b64_tr_b16 a[152:153], v228 offset:20480\n\t"
    "s_add_u32 m0, s54, 65536\n\t"
    "v_exp_f32 v160, v160\n\t"
    "v_exp_f32 v161, v161\n\t"
    "v_exp_f32 v162, v162\n\t"
    "v_mfma_f32_32x32x16_bf16 v[176:191], v[204:207], v[44:47], v[176:191]\n\t"
    "s_mov_b32 s53, 1048576\n\t"
    "v_exp_f32 v163, v163\n\t"
    "v_exp_f32 v164, v164\n\t"
    "v_exp_f32 v165, v165\n\t"
    "v_smfmac_f32_32x32x32_bf16 v[96:111], v[208:211], v[144:151], v124\n\t"
    "buffer_load_dwordx4 v224, s[44:47], s53 offen offset:0 lds\n\t"
    "buffer_load_dwordx4 v225, s[44:47], s53 offen offset:1024 lds\n\t"
    "v_exp_f32 v166, v166\n\t"
    "v_exp_f32 v167, v167\n\t"
    "v_exp_f32 v168, v168\n\t"
    "v_smfmac_f32_16x16x64_bf16 v[116:119], v[120:123], v[144:151], v124\n\t"
    "ds_read_b128 v[208:211], %[rb] offset:43008\n\t"
    "v_exp_f32 v169, v169\n\t"
    "v_exp_f32 v170, v170\n\t"
    "v_smfmac_f32_32x32x32_bf16 v[96:111], v[212:215], v[152:159], v124\n\t"
    "s_waitcnt lgkmcnt(2)\n\t"
    "v_exp_f32 v171, v171\n\t"
    "v_exp_f32 v172, v172\n\t"
    "v_exp_f32 v173, v173\n\t"
    "v_smfmac_f32_16x16x64_bf16 v[116:119], v[120:123], v[152:159], v124\n\t"
    "ds_read_b128 v[212:215], %[rb] offset:44032\n\t"
    "v_exp_f32 v174, v174\n\t"
    "v_exp_f32 v175, v175\n\t"
    "v_mfma_f32_32x32x16_bf16 v[128:143], v[192:195], v[32:35], v[48:63]\n\t"
    "ds_read_b64_tr_b16 a[154:155], v228 offset:22528\n\t"
    "v_exp_f32 v176, v176\n\t"
    "v_exp_f32 v177, v177\n\t"
    "v_exp_f32 v178, v178\n\t"
    "v_mfma_f32_32x32x16_bf16 v[128:143], v[196:199], v[36:39], v[128:143]\n\t"
    "v_exp_f32 v179, v179\n\t"
    "v_exp_f32 v180, v180\n\t"
    "v_exp_f32 v181, v181\n\t"
    "v_smfmac_f32_32x32x32_bf16 v[80:95], v[216:219], v[160:167], v124\n\t"
    "v_exp_f32 v182, v182\n\t"
    "v_exp_f32 v183, v183\n\t"
    "v_exp_f32 v184, v184\n\t"
    "v_smfmac_f32_16x16x64_bf16 v[112:115], v[120:123], v[160:167], v124\n\t"
    "ds_read_b128 v[200:203], %[rb] offset:45056\n\t"
    "v_exp_f32 v185, v185\n\t"
    "v_exp_f32 v186, v186\n\t"
    "v_smfmac_f32_32x32x32_bf16 v[80:95], v[220:223], v[168:175], v124\n\t"
    "v_exp_f32 v187, v187\n\t"
    "v_exp_f32 v188, v188\n\t"
    "v_exp_f32 v189, v189\n\t"
    "v_smfmac_f32_16x16x64_bf16 v[112:115], v[120:123], v[168:175], v124\n\t"
    "ds_read_b128 v[204:207], %[rb] offset:46080\n\t"
    "v_exp_f32 v190, v190\n\t"
    "v_exp_f32 v191, v191\n\t"
    "v_mfma_f32_32x32x16_bf16 v[144:159], v[192:195], v[40:43], v[64:79]\n\t"
    "ds_read_b64_tr_b16 a[156:157], v229 offset:20480\n\t"
    "ds_read_b64_tr_b16 a[158:159], v229 offset:22528\n\t"
    "v_exp_f32 v128, v128\n\t"
    "v_exp_f32 v129, v129\n\t"
    "v_exp_f32 v130, v130\n\t"
    "v_mfma_f32_32x32x16_bf16 v[144:159], v[196:199], v[44:47], v[144:159]\n\t"
    "v_exp_f32 v131, v131\n\t"
    "v_exp_f32 v132, v132\n\t"
    "v_exp_f32 v133, v133\n\t"
    "v_smfmac_f32_32x32x32_bf16 v[96:111], v[216:219], v[176:183], v124\n\t"
    "buffer_load_dwordx4 v226, s[44:47], s53 offen offset:2048 lds\n\t"
    "buffer_load_dwordx4 v227, s[44:47], s53 offen offset:3072 lds\n\t"
    "v_exp_f32 v134, v134\n\t"
    "v_exp_f32 v135, v135\n\t"
    "v_exp_f32 v136, v136\n\t"
    "v_smfmac_f32_16x16x64_bf16 v[116:119], v[120:123], v[176:183], v124\n\t"
    "ds_read_b128 v[216:219], %[rb] offset:47104\n\t"
    "v_exp_f32 v137, v137\n\t"
    "v_exp_f32 v138, v138\n\t"
    "v_smfmac_f32_32x32x32_bf16 v[96:111], v[220:223], v[184:191], v124\n\t"
    "s_waitcnt lgkmcnt(3)\n\t"
    "v_exp_f32 v139, v139\n\t"
    "v_exp_f32 v140, v140\n\t"
    "v_exp_f32 v141, v141\n\t"
    "v_smfmac_f32_16x16x64_bf16 v[116:119], v[120:123], v[184:191], v124\n\t"
    "ds_read_b128 v[220:223], %[rb] offset:48128\n\t"
    "v_exp_f32 v142, v142\n\t"
    "v_exp_f32 v143, v143\n\t"
    "v_mfma_f32_32x32x16_bf16 v[160:175], v[200:203], v[32:35], v[48:63]\n\t"
    "v_exp_f32 v144, v144\n\t"
    "v_exp_f32 v145, v145\n\t"
    "v_exp_f32 v146, v146\n\t"
    "v_mfma_f32_32x32x16_bf16 v[160:175], v[204:207], v[36:39], v[160:175]\n\t"
    "v_exp_f32 v147, v147\n\t"
    "v_exp_f32 v148, v148\n\t"
    "v_exp_f32 v149, v149\n\t"
    "v_smfmac_f32_32x32x32_bf16 v[80:95], v[208:211], v[128:135], v124\n\t"
    "s_waitcnt vmcnt(17) lgkmcnt(0)\n\t"
    "s_barrier\n\t"
    "v_exp_f32 v150, v150\n\t"
    "v_exp_f32 v151, v151\n\t"
    "v_exp_f32 v152, v152\n\t"
    "v_smfmac_f32_16x16x64_bf16 v[112:115], v[120:123], v[128:135], v124\n\t"
    "ds_read_b128 v[192:195], %[rb] offset:49152\n\t"
    "v_exp_f32 v153, v153\n\t"
    "v_exp_f32 v154, v154\n\t"
    "v_smfmac_f32_32x32x32_bf16 v[80:95], v[212:215], v[136:143], v124\n\t"
    "v_exp_f32 v155, v155\n\t"
    "v_exp_f32 v156, v156\n\t"
    "v_exp_f32 v157, v157\n\t"
    "v_smfmac_f32_16x16x64_bf16 v[112:115], v[120:123], v[136:143], v124\n\t"
    "ds_read_b128 v[196:199], %[rb] offset:50176\n\t"
    "v_exp_f32 v158, v158\n\t"
    "v_exp_f32 v159, v159\n\t"
    "v_mfma_f32_32x32x16_bf16 v[176:191], v[200:203], v[40:43], v[64:79]\n\t"
    "ds_read_b64_tr_b16 a[160:161], v228 offset:32768\n\t"
    "s_add_u32 s52, %[krot], 229376\n\t"
    "v_exp_f32 v160, v160\n\t"
    "v_exp_f32 v161, v161\n\t"
    "v_exp_f32 v162, v162\n\t"
    "v_mfma_f32_32x32x16_bf16 v[176:191], v[204:207], v[44:47], v[176:191]\n\t"
    "s_add_u32 m0, s54, 32768\n\t"
    "s_and_b32 s52, s52, 0x3ffff\n\t"
    "v_exp_f32 v163, v163\n\t"
    "v_exp_f32 v164, v164\n\t"
    "v_exp_f32 v165, v165\n\t"
    "v_smfmac_f32_32x32x32_bf16 v[96:111], v[208:211], v[144:151], v124\n\t"
    "buffer_load_dwordx4 %[vg], s[40:43], s52 offen offset:0 sc1 lds\n\t"
    "buffer_load_dwordx4 %[vg], s[40:43], s52 offen offset:1024 sc1 lds\n\t"
    "s_mov_b32 s56, 0x40000\n\t"
    "WBP_%=:\n"
    "v_cmp_ne_u32 vcc, v232, %[tag]\n\t"
    "s_nop 4\n\t"
    "s_cbranch_vccz WBS_%=\n\t"
    "s_sleep 4\n\t"
    "global_load_dword v232, %[vpb], %[pf] sc1\n\t"
    "s_sub_u32 s56, s56, 1\n\t"
    "s_cmp_lg_u32 s56, 0\n\t"
    "s_waitcnt vmcnt(0)\n\t"
    "s_cbranch_scc1 WBP_%=\n\t"
    "WBS_%=:\n"
    "v_exp_f32 v166, v166\n\t"
    "v_exp_f32 v167, v167\n\t"
    "v_exp_f32 v168, v168\n\t"
    "v_smfmac_f32_16x16x64_bf16 v[116:119], v[120:123], v[144:151], v124\n\t"
    "ds_read_b128 v[208:211], %[rb] offset:51200\n\t"
    "v_exp_f32 v169, v169\n\t"
    "v_exp_f32 v170, v170\n\t"
    "v_smfmac_f32_32x32x32_bf16 v[96:111], v[212:215], v[152:159], v124\n\t"
    "s_waitcnt lgkmcnt(2)\n\t"
    "v_exp_f32 v171, v171\n\t"
    "v_exp_f32 v172, v172\n\t"
    "v_exp_f32 v173, v173\n\t"
    "v_smfmac_f32_16x16x64_bf16 v[116:119], v[120:123], v[152:159], v124\n\t"
    "ds_read_b128 v[212:215], %[rb] offset:52224\n\t"
    "v_exp_f32 v174, v174\n\t"
    "v_exp_f32 v175, v175\n\t"
    "v_mfma_f32_32x32x16_bf16 v[128:143], v[192:195], v[32:35], v[48:63]\n\t"
    "ds_read_b64_tr_b16 a[162:163], v228 offset:34816\n\t"
    "v_exp_f32 v176, v176\n\t"
    "v_exp_f32 v177, v177\n\t"
    "v_exp_f32 v178, v178\n\t"
    "v_mfma_f32_32x32x16_bf16 v[128:143], v[196:199], v[36:39], v[128:143]\n\t"
    "v_exp_f32 v179, v179\n\t"
    "v_exp_f32 v180, v180\n\t"
    "v_exp_f32 v181, v181\n\t"
    "v_smfmac_f32_32x32x32_bf16 v[80:95], v[216:219], v[160:167], v124\n\t"
    "v_exp_f32 v182, v182\n\t"
    "v_exp_f32 v183, v183\n\t"
    "v_exp_f32 v184, v184\n\t"
    "v_smfmac_f32_16x16x64_bf16 v[112:115], v[120:123], v[160:167], v124\n\t"
    "ds_read_b128 v[200:203], %[rb] offset:53248\n\t"
    "v_exp_f32 v185, v185\n\t"
    "v_exp_f32 v186, v186\n\t"
    "v_smfmac_f32_32x32x32_bf16 v[80:95], v[220:223], v[168:175], v124\n\t"
    "v_exp_f32 v187, v187\n\t"
    "v_exp_f32 v188, v188\n\t"
    "v_exp_f32 v189, v189\n\t"
    "v_smfmac_f32_16x16x64_bf16 v[112:115], v[120:123], v[168:175], v124\n\t"
    "ds_read_b128 v[204:207], %[rb] offset:54272\n\t"
    "v_exp_f32 v190, v190\n\t"
    "v_exp_f32 v191, v191\n\t"
    "v_mfma_f32_32x32x16_bf16 v[144:159], v[192:195], v[40:43], v[64:79]\n\t"
    "ds_read_b64_tr_b16 a[164:165], v229 offset:32768\n\t"
    "v_exp_f32 v128, v128\n\t"
    "v_exp_f32 v129, v129\n\t"
    "v_exp_f32 v130, v130\n\t"
    "v_mfma_f32_32x32x16_bf16 v[144:159], v[196:199], v[44:47], v[144:159]\n\t"
    "v_exp_f32 v131, v131\n\t"
    "v_exp_f32 v132, v132\n\t"
    "v_exp_f32 v133, v133\n\t"
    "v_smfmac_f32_32x32x32_bf16 v[96:111], v[216:219], v[176:183], v124\n\t"
    "buffer_load_dwordx4 %[vg], s[40:43], s52 offen offset:2048 sc1 lds\n\t"
    "buffer_load_dwordx4 %[vg], s[40:43], s52 offen offset:3072 sc1 lds\n\t"
    "v_exp_f32 v134, v134\n\t"
    "v_exp_f32 v135, v135\n\t"
    "v_exp_f32 v136, v136\n\t"
    "v_smfmac_f32_16x16x64_bf16 v[116:119], v[120:123], v[176:183], v124\n\t"
    "ds_read_b128 v[216:219], %[rb] offset:55296\n\t"
    "v_exp_f32 v137, v137\n\t"
    "v_exp_f32 v138, v138\n\t"
    "v_smfmac_f32_32x32x32_bf16 v[96:111], v[220:223], v[184:191], v124\n\t"
    "s_waitcnt lgkmcnt(2)\n\t"
    "v_exp_f32 v139, v139\n\t"
    "v_exp_f32 v140, v140\n\t"
    "v_exp_f32 v141, v141\n\t"
    "v_smfmac_f32_16x16x64_bf16 v[116:119], v[120:123], v[184:191], v124\n\t"
    "ds_read_b128 v[220:223], %[rb] offset:56320\n\t"
    "v_exp_f32 v142, v142\n\t"
    "v_exp_f32 v143, v143\n\t"
    "v_mfma_f32_32x32x16_bf16 v[160:175], v[200:203], v[32:35], v[48:63]\n\t"
    "ds_read_b64_tr_b16 a[166:167], v229 offset:34816\n\t"
    "v_exp_f32 v144, v144\n\t"
    "v_exp_f32 v145, v145\n\t"
    "v_exp_f32 v146, v146\n\t"
    "v_mfma_f32_32x32x16_bf16 v[160:175], v[204:207], v[36:39], v[160:175]\n\t"
    "v_exp_f32 v147, v147\n\t"
    "v_exp_f32 v148, v148\n\t"
    "v_exp_f32 v149, v149\n\t"
    "v_smfmac_f32_32x32x32_bf16 v[80:95], v[208:211], v[128:135], v124\n\t"
    "v_exp_f32 v150, v150\n\t"
    "v_exp_f32 v151, v151\n\t"
    "v_exp_f32 v152, v152\n\t"
    "v_smfmac_f32_16x16x64_bf16 v[112:115], v[120:123], v[128:135], v124\n\t"
    "ds_read_b128 v[192:195], %[rb] offset:57344\n\t"
    "v_exp_f32 v153, v153\n\t"
    "v_exp_f32 v154, v154\n\t"
    "v_smfmac_f32_32x32x32_bf16 v[80:95], v[212:215], v[136:143], v124\n\t"
    "v_exp_f32 v155, v155\n\t"
    "v_exp_f32 v156, v156\n\t"
    "v_exp_f32 v157, v157\n\t"
    "v_smfmac_f32_16x16x64_bf16 v[112:115], v[120:123], v[136:143], v124\n\t"
    "ds_read_b128 v[196:199], %[rb] offset:58368\n\t"
    "v_exp_f32 v158, v158\n\t"
    "v_exp_f32 v159, v159\n\t"
    "v_mfma_f32_32x32x16_bf16 v[176:191], v[200:203], v[40:43], v[64:79]\n\t"
    "ds_read_b64_tr_b16 a[168:169], v228 offset:36864\n\t"
    "s_add_u32 m0, s54, 81920\n\t"
    "v_exp_f32 v160, v160\n\t"
    "v_exp_f32 v161, v161\n\t"
    "v_exp_f32 v162, v162\n\t"
    "v_mfma_f32_32x32x16_bf16 v[176:191], v[204:207], v[44:47], v[176:191]\n\t"
    "s_mov_b32 s53, 1310720\n\t"
    "v_exp_f32 v163, v163\n\t"
    "v_exp_f32 v164, v164\n\t"
    "v_exp_f32 v165, v165\n\t"
    "v_smfmac_f32_32x32x32_bf16 v[96:111], v[208:211], v[144:151], v124\n\t"
    "buffer_load_dwordx4 v224, s[44:47], s53 offen offset:0 lds\n\t"
    "buffer_load_dwordx4 v225, s[44:47], s53 offen offset:1024 lds\n\t"
    "v_exp_f32 v166, v166\n\t"
    "v_exp_f32 v167, v167\n\t"
    "v_exp_f32 v168, v168\n\t"
    "v_smfmac_f32_16x16x64_bf16 v[116:119], v[120:123], v[144:151], v124\n\t"
    "ds_read_b128 v[208:211], %[rb] offset:59392\n\t"
    "v_exp_f32 v169, v169\n\t"
    "v_exp_f32 v170, v170\n\t"
    "v_smfmac_f32_32x32x32_bf16 v[96:111], v[212:215], v[152:159], v124\n\t"
    "s_waitcnt lgkmcnt(2)\n\t"
    "v_exp_f32 v171, v171\n\t"
    "v_exp_f32 v172, v172\n\t"
    "v_exp_f32 v173, v173\n\t"
    "v_smfmac_f32_16x16x64_bf16 v[116:119], v[120:123], v[152:159], v124\n\t"
    "ds_read_b128 v[212:215], %[rb] offset:60416\n\t"
    "v_exp_f32 v174, v174\n\t"
    "v_exp_f32 v175, v175\n\t"
    "v_mfma_f32_32x32x16_bf16 v[128:143], v[192:195], v[32:35], v[48:63]\n\t"
    "ds_read_b64_tr_b16 a[170:171], v228 offset:38912\n\t"
    "v_exp_f32 v176, v176\n\t"
    "v_exp_f32 v177, v177\n\t"
    "v_exp_f32 v178, v178\n\t"
    "v_mfma_f32_32x32x16_bf16 v[128:143], v[196:199], v[36:39], v[128:143]\n\t"
    "v_exp_f32 v179, v179\n\t"
    "v_exp_f32 v180, v180\n\t"
    "v_exp_f32 v181, v181\n\t"
    "v_smfmac_f32_32x32x32_bf16 v[80:95], v[216:219], v[160:167], v124\n\t"
    "v_exp_f32 v182, v182\n\t"
    "v_exp_f32 v183, v183\n\t"
    "v_exp_f32 v184, v184\n\t"
    "v_smfmac_f32_16x16x64_bf16 v[112:115], v[120:123], v[160:167], v124\n\t"
    "ds_read_b128 v[200:203], %[rb] offset:61440\n\t"
    "v_exp_f32 v185, v185\n\t"
    "v_exp_f32 v186, v186\n\t"
    "v_smfmac_f32_32x32x32_bf16 v[80:95], v[220:223], v[168:175], v124\n\t"
    "v_exp_f32 v187, v187\n\t"
    "v_exp_f32 v188, v188\n\t"
    "v_exp_f32 v189, v189\n\t"
    "v_smfmac_f32_16x16x64_bf16 v[112:115], v[120:123], v[168:175], v124\n\t"
    "ds_read_b128 v[204:207], %[rb] offset:62464\n\t"
    "v_exp_f32 v190, v190\n\t"
    "v_exp_f32 v191, v191\n\t"
    "v_mfma_f32_32x32x16_bf16 v[144:159], v[192:195], v[40:43], v[64:79]\n\t"
    "ds_read_b64_tr_b16 a[172:173], v229 offset:36864\n\t"
    "ds_read_b64_tr_b16 a[174:175], v229 offset:38912\n\t"
    "v_exp_f32 v128, v128\n\t"
    "v_exp_f32 v129, v129\n\t"
    "v_exp_f32 v130, v130\n\t"
    "v_mfma_f32_32x32x16_bf16 v[144:159], v[196:199], v[44:47], v[144:159]\n\t"
    "v_exp_f32 v131, v131\n\t"
    "v_exp_f32 v132, v132\n\t"
    "v_exp_f32 v133, v133\n\t"
    "v_smfmac_f32_32x32x32_bf16 v[96:111], v[216:219], v[176:183], v124\n\t"
    "buffer_load_dwordx4 v226, s[44:47], s53 offen offset:2048 lds\n\t"
    "buffer_load_dwordx4 v227, s[44:47], s53 offen offset:3072 lds\n\t"
    "v_exp_f32 v134, v134\n\t"
    "v_exp_f32 v135, v135\n\t"
    "v_exp_f32 v136, v136\n\t"
    "v_smfmac_f32_16x16x64_bf16 v[116:119], v[120:123], v[176:183], v124\n\t"
    "ds_read_b128 v[216:219], %[rb] offset:63488\n\t"
    "v_exp_f32 v137, v137\n\t"
    "v_exp_f32 v138, v138\n\t"
    "v_smfmac_f32_32x32x32_bf16 v[96:111], v[220:223], v[184:191], v124\n\t"
    "s_waitcnt lgkmcnt(3)\n\t"
    "v_exp_f32 v139, v139\n\t"
    "v_exp_f32 v140, v140\n\t"
    "v_exp_f32 v141, v141\n\t"
    "v_smfmac_f32_16x16x64_bf16 v[116:119], v[120:123], v[184:191], v124\n\t"
    "ds_read_b128 v[220:223], %[rb] offset:64512\n\t"
    "v_exp_f32 v142, v142\n\t"
    "v_exp_f32 v143, v143\n\t"
    "v_mfma_f32_32x32x16_bf16 v[160:175], v[200:203], v[32:35], v[48:63]\n\t"
    "v_exp_f32 v144, v144\n\t"
    "v_exp_f32 v145, v145\n\t"
    "v_exp_f32 v146, v146\n\t"
    "v_mfma_f32_32x32x16_bf16 v[160:175], v[204:207], v[36:39], v[160:175]\n\t"
    "v_exp_f32 v147, v147\n\t"
    "v_exp_f32 v148, v148\n\t"
    "v_exp_f32 v149, v149\n\t"
    "v_smfmac_f32_32x32x32_bf16 v[80:95], v[208:211], v[128:135], v124\n\t"
    "s_waitcnt vmcnt(16) lgkmcnt(0)\n\t"
    "s_barrier\n\t"
    "v_exp_f32 v150, v150\n\t"
    "v_exp_f32 v151, v151\n\t"
    "v_exp_f32 v152, v152\n\t"
    "v_smfmac_f32_16x16x64_bf16 v[112:115], v[120:123], v[128:135], v124\n\t"
    "ds_read_b128 v[192:195], %[rb] offset:0\n\t"
    "v_exp_f32 v153, v153\n\t"
    "v_exp_f32 v154, v154\n\t"
    "v_smfmac_f32_32x32x32_bf16 v[80:95], v[212:215], v[136:143], v124\n\t"
    "v_exp_f32 v155, v155\n\t"
    "v_exp_f32 v156, v156\n\t"
    "v_exp_f32 v157, v157\n\t"
    "v_smfmac_f32_16x16x64_bf16 v[112:115], v[120:123], v[136:143], v124\n\t"
    "ds_read_b128 v[196:199], %[rb] offset:1024\n\t"
    "v_exp_f32 v158, v158\n\t"
    "v_exp_f32 v159, v159\n\t"
    "v_mfma_f32_32x32x16_bf16 v[176:191], v[200:203], v[40:43], v[64:79]\n\t"
    "ds_read_b64_tr_b16 a[176:177], v228 offset:49152\n\t"
    "s_add_u32 s52, %[krot], 245760\n\t"
    "v_exp_f32 v160, v160\n\t"
    "v_exp_f32 v161, v161\n\t"
    "v_exp_f32 v162, v162\n\t"
    "v_mfma_f32_32x32x16_bf16 v[176:191], v[204:207], v[44:47], v[176:191]\n\t"
    "s_add_u32 m0, s54, 49152\n\t"
    "s_and_b32 s52, s52, 0x3ffff\n\t"
    "v_exp_f32 v163, v163\n\t"
    "v_exp_f32 v164, v164\n\t"
    "v_exp_f32 v165, v165\n\t"
    "v_smfmac_f32_32x32x32_bf16 v[96:111], v[208:211], v[144:151], v124\n\t"
    "buffer_load_dwordx4 %[vg], s[40:43], s52 offen offset:0 sc1 lds\n\t"
    "buffer_load_dwordx4 %[vg], s[40:43], s52 offen offset:1024 sc1 lds\n\t"
    "v_exp_f32 v166, v166\n\t"
    "v_exp_f32 v167, v167\n\t"
    "v_exp_f32 v168, v168\n\t"
    "v_smfmac_f32_16x16x64_bf16 v[116:119], v[120:123], v[144:151], v124\n\t"
    "ds_read_b128 v[208:211], %[rb] offset:2048\n\t"
    "v_exp_f32 v169, v169\n\t"
    "v_exp_f32 v170, v170\n\t"
    "v_smfmac_f32_32x32x32_bf16 v[96:111], v[212:215], v[152:159], v124\n\t"
    "s_waitcnt lgkmcnt(2)\n\t"
    "v_exp_f32 v171, v171\n\t"
    "v_exp_f32 v172, v172\n\t"
    "v_exp_f32 v173, v173\n\t"
    "v_smfmac_f32_16x16x64_bf16 v[116:119], v[120:123], v[152:159], v124\n\t"
    "ds_read_b128 v[212:215], %[rb] offset:3072\n\t"
    "v_exp_f32 v174, v174\n\t"
    "v_exp_f32 v175, v175\n\t"
    "v_mfma_f32_32x32x16_bf16 v[128:143], v[192:195], v[32:35], v[48:63]\n\t"
    "ds_read_b64_tr_b16 a[178:179], v228 offset:51200\n\t"
    "v_exp_f32 v176, v176\n\t"
    "v_exp_f32 v177, v177\n\t"
    "v_exp_f32 v178, v178\n\t"
    "v_mfma_f32_32x32x16_bf16 v[128:143], v[196:199], v[36:39], v[128:143]\n\t"
    "v_exp_f32 v179, v179\n\t"
    "v_exp_f32 v180, v180\n\t"
    "v_exp_f32 v181, v181\n\t"
    "v_smfmac_f32_32x32x32_bf16 v[80:95], v[216:219], v[160:167], v124\n\t"
    "v_exp_f32 v182, v182\n\t"
    "v_exp_f32 v183, v183\n\t"
    "v_exp_f32 v184, v184\n\t"
    "v_smfmac_f32_16x16x64_bf16 v[112:115], v[120:123], v[160:167], v124\n\t"
    "ds_read_b128 v[200:203], %[rb] offset:4096\n\t"
    "v_exp_f32 v185, v185\n\t"
    "v_exp_f32 v186, v186\n\t"
    "v_smfmac_f32_32x32x32_bf16 v[80:95], v[220:223], v[168:175], v124\n\t"
    "v_exp_f32 v187, v187\n\t"
    "v_exp_f32 v188, v188\n\t"
    "v_exp_f32 v189, v189\n\t"
    "v_smfmac_f32_16x16x64_bf16 v[112:115], v[120:123], v[168:175], v124\n\t"
    "ds_read_b128 v[204:207], %[rb] offset:5120\n\t"
    "v_exp_f32 v190, v190\n\t"
    "v_exp_f32 v191, v191\n\t"
    "v_mfma_f32_32x32x16_bf16 v[144:159], v[192:195], v[40:43], v[64:79]\n\t"
    "ds_read_b64_tr_b16 a[180:181], v229 offset:49152\n\t"
    "v_exp_f32 v128, v128\n\t"
    "v_exp_f32 v129, v129\n\t"
    "v_exp_f32 v130, v130\n\t"
    "v_mfma_f32_32x32x16_bf16 v[144:159], v[196:199], v[44:47], v[144:159]\n\t"
    "v_exp_f32 v131, v131\n\t"
    "v_exp_f32 v132, v132\n\t"
    "v_exp_f32 v133, v133\n\t"
    "v_smfmac_f32_32x32x32_bf16 v[96:111], v[216:219], v[176:183], v124\n\t"
    "buffer_load_dwordx4 %[vg], s[40:43], s52 offen offset:2048 sc1 lds\n\t"
    "buffer_load_dwordx4 %[vg], s[40:43], s52 offen offset:3072 sc1 lds\n\t"
    "v_exp_f32 v134, v134\n\t"
    "v_exp_f32 v135, v135\n\t"
    "v_exp_f32 v136, v136\n\t"
    "v_smfmac_f32_16x16x64_bf16 v[116:119], v[120:123], v[176:183], v124\n\t"
    "ds_read_b128 v[216:219], %[rb] offset:6144\n\t"
    "v_exp_f32 v137, v137\n\t"
    "v_exp_f32 v138, v138\n\t"
    "v_smfmac_f32_32x32x32_bf16 v[96:111], v[220:223], v[184:191], v124\n\t"
    "s_waitcnt lgkmcnt(2)\n\t"
    "global_load_dwordx4 v[250:253], v248, s[62:63]\n\t"
    "v_exp_f32 v139, v139\n\t"
    "v_exp_f32 v140, v140\n\t"
    "v_exp_f32 v141, v141\n\t"
    "v_smfmac_f32_16x16x64_bf16 v[116:119], v[120:123], v[184:191], v124\n\t"
    "ds_read_b128 v[220:223], %[rb] offset:7168\n\t"
    "v_exp_f32 v142, v142\n\t"
    "v_exp_f32 v143, v143\n\t"
    "v_mfma_f32_32x32x16_bf16 v[160:175], v[200:203], v[32:35], v[48:63]\n\t"
    "ds_read_b64_tr_b16 a[182:183], v229 offset:51200\n\t"
    "v_exp_f32 v144, v144\n\t"
    "v_exp_f32 v145, v145\n\t"
    "v_exp_f32 v146, v146\n\t"
    "v_mfma_f32_32x32x16_bf16 v[160:175], v[204:207], v[36:39], v[160:175]\n\t"
    "v_exp_f32 v147, v147\n\t"
    "v_exp_f32 v148, v148\n\t"
    "v_exp_f32 v149, v149\n\t"
    "v_smfmac_f32_32x32x32_bf16 v[80:95], v[208:211], v[128:135], v124\n\t"
    "v_exp_f32 v150, v150\n\t"
    "v_exp_f32 v151, v151\n\t"
    "v_exp_f32 v152, v152\n\t"
    "v_smfmac_f32_16x16x64_bf16 v[112:115], v[120:123], v[128:135], v124\n\t"
    "ds_read_b128 v[192:195], %[rb] offset:8192\n\t"
    "v_exp_f32 v153, v153\n\t"
    "v_exp_f32 v154, v154\n\t"
    "v_smfmac_f32_32x32x32_bf16 v[80:95], v[212:215], v[136:143], v124\n\t"
    "v_exp_f32 v155, v155\n\t"
    "v_exp_f32 v156, v156\n\t"
    "v_exp_f32 v157, v157\n\t"
    "v_smfmac_f32_16x16x64_bf16 v[112:115], v[120:123], v[136:143], v124\n\t"
    "ds_read_b128 v[196:199], %[rb] offset:9216\n\t"
    "v_exp_f32 v158, v158\n\t"
    "v_exp_f32 v159, v159\n\t"
    "v_mfma_f32_32x32x16_bf16 v[176:191], v[200:203], v[40:43], v[64:79]\n\t"
    "ds_read_b64_tr_b16 a[184:185], v228 offset:53248\n\t"
    "s_add_u32 m0, s54, 98304\n\t"
    "v_exp_f32 v160, v160\n\t"
    "v_exp_f32 v161, v161\n\t"
    "v_exp_f32 v162, v162\n\t"
    "v_mfma_f32_32x32x16_bf16 v[176:191], v[204:207], v[44:47], v[176:191]\n\t"
    "s_mov_b32 s53, 1572864\n\t"
    "v_exp_f32 v163, v163\n\t"
    "v_exp_f32 v164, v164\n\t"
    "v_exp_f32 v165, v165\n\t"
    "v_smfmac_f32_32x32x32_bf16 v[96:111], v[208:211], v[144:151], v124\n\t"
    "buffer_load_dwordx4 v224, s[44:47], s53 offen offset:0 lds\n\t"
    "buffer_load_dwordx4 v225, s[44:47], s53 offen offset:1024 lds\n\t"
    "v_exp_f32 v166, v166\n\t"
    "v_exp_f32 v167, v167\n\t"
    "v_exp_f32 v168, v168\n\t"
    "v_smfmac_f32_16x16x64_bf16 v[116:119], v[120:123], v[144:151], v124\n\t"
    "ds_read_b128 v[208:211], %[rb] offset:10240\n\t"
    "v_exp_f32 v169, v169\n\t"
    "v_exp_f32 v170, v170\n\t"
    "v_smfmac_f32_32x32x32_bf16 v[96:111], v[212:215], v[152:159], v124\n\t"
    "s_waitcnt lgkmcnt(2)\n\t"
    "v_exp_f32 v171, v171\n\t"
    "v_exp_f32 v172, v172\n\t"
    "v_exp_f32 v173, v173\n\t"
    "v_smfmac_f32_16x16x64_bf16 v[116:119], v[120:123], v[152:159], v124\n\t"
    "ds_read_b128 v[212:215], %[rb] offset:11264\n\t"
    "v_exp_f32 v174, v174\n\t"
    "v_exp_f32 v175, v175\n\t"
    "v_mfma_f32_32x32x16_bf16 v[128:143], v[192:195], v[32:35], v[48:63]\n\t"
    "ds_read_b64_tr_b16 a[186:187], v228 offset:55296\n\t"
    "v_exp_f32 v176, v176\n\t"
    "v_exp_f32 v177, v177\n\t"
    "v_exp_f32 v178, v178\n\t"
    "v_mfma_f32_32x32x16_bf16 v[128:143], v[196:199], v[36:39], v[128:143]\n\t"
    "v_exp_f32 v179, v179\n\t"
    "v_exp_f32 v180, v180\n\t"
    "v_exp_f32 v181, v181\n\t"
    "v_smfmac_f32_32x32x32_bf16 v[80:95], v[216:219], v[160:167], v124\n\t"
    "v_exp_f32 v182, v182\n\t"
    "v_exp_f32 v183, v183\n\t"
    "v_exp_f32 v184, v184\n\t"
    "v_smfmac_f32_16x16x64_bf16 v[112:115], v[120:123], v[160:167], v124\n\t"
    "ds_read_b128 v[200:203], %[rb] offset:12288\n\t"
    "v_exp_f32 v185, v185\n\t"
    "v_exp_f32 v186, v186\n\t"
    "v_smfmac_f32_32x32x32_bf16 v[80:95], v[220:223], v[168:175], v124\n\t"
    "v_exp_f32 v187, v187\n\t"
    "v_exp_f32 v188, v188\n\t"
    "v_exp_f32 v189, v189\n\t"
    "v_smfmac_f32_16x16x64_bf16 v[112:115], v[120:123], v[168:175], v124\n\t"
    "ds_read_b128 v[204:207], %[rb] offset:13312\n\t"
    "v_exp_f32 v190, v190\n\t"
    "v_exp_f32 v191, v191\n\t"
    "v_mfma_f32_32x32x16_bf16 v[144:159], v[192:195], v[40:43], v[64:79]\n\t"
    "ds_read_b64_tr_b16 a[188:189], v229 offset:53248\n\t"
    "ds_read_b64_tr_b16 a[190:191], v229 offset:55296\n\t"
    "v_exp_f32 v128, v128\n\t"
    "v_exp_f32 v129, v129\n\t"
    "v_exp_f32 v130, v130\n\t"
    "v_mfma_f32_32x32x16_bf16 v[144:159], v[196:199], v[44:47], v[144:159]\n\t"
    "v_exp_f32 v131, v131\n\t"
    "v_exp_f32 v132, v132\n\t"
    "v_exp_f32 v133, v133\n\t"
    "v_smfmac_f32_32x32x32_bf16 v[96:111], v[216:219], v[176:183], v124\n\t"
    "buffer_load_dwordx4 v226, s[44:47], s53 offen offset:2048 lds\n\t"
    "buffer_load_dwordx4 v227, s[44:47], s53 offen offset:3072 lds\n\t"
    "v_exp_f32 v134, v134\n\t"
    "v_exp_f32 v135, v135\n\t"
    "v_exp_f32 v136, v136\n\t"
    "v_smfmac_f32_16x16x64_bf16 v[116:119], v[120:123], v[176:183], v124\n\t"
    "ds_read_b128 v[216:219], %[rb] offset:14336\n\t"
    "v_exp_f32 v137, v137\n\t"
    "v_exp_f32 v138, v138\n\t"
    "v_smfmac_f32_32x32x32_bf16 v[96:111], v[220:223], v[184:191], v124\n\t"
    "s_waitcnt lgkmcnt(3)\n\t"
    "v_exp_f32 v139, v139\n\t"
    "v_exp_f32 v140, v140\n\t"
    "v_exp_f32 v141, v141\n\t"
    "v_smfmac_f32_16x16x64_bf16 v[116:119], v[120:123], v[184:191], v124\n\t"
    "ds_read_b128 v[220:223], %[rb] offset:15360\n\t"
    "v_exp_f32 v142, v142\n\t"
    "v_exp_f32 v143, v143\n\t"
    "v_mfma_f32_32x32x16_bf16 v[160:175], v[200:203], v[32:35], v[48:63]\n\t"
    "v_exp_f32 v144, v144\n\t"
    "v_exp_f32 v145, v145\n\t"
    "v_exp_f32 v146, v146\n\t"
    "v_mfma_f32_32x32x16_bf16 v[160:175], v[204:207], v[36:39], v[160:175]\n\t"
    "v_exp_f32 v147, v147\n\t"
    "v_exp_f32 v148, v148\n\t"
    "v_exp_f32 v149, v149\n\t"
    "v_smfmac_f32_32x32x32_bf16 v[80:95], v[208:211], v[128:135], v124\n\t"
    "s_waitcnt vmcnt(17) lgkmcnt(0)\n\t"
    "s_barrier\n\t"
    "v_exp_f32 v150, v150\n\t"
    "v_exp_f32 v151, v151\n\t"
    "v_exp_f32 v152, v152\n\t"
    "v_smfmac_f32_16x16x64_bf16 v[112:115], v[120:123], v[128:135], v124\n\t"
    "ds_read_b128 v[192:195], %[rb] offset:16384\n\t"
    "v_exp_f32 v153, v153\n\t"
    "v_exp_f32 v154, v154\n\t"
    "v_smfmac_f32_32x32x32_bf16 v[80:95], v[212:215], v[136:143], v124\n\t"
    "v_exp_f32 v155, v155\n\t"
    "v_exp_f32 v156, v156\n\t"
    "v_exp_f32 v157, v157\n\t"
    "v_smfmac_f32_16x16x64_bf16 v[112:115], v[120:123], v[136:143], v124\n\t"
    "ds_read_b128 v[196:199], %[rb] offset:17408\n\t"
    "v_exp_f32 v158, v158\n\t"
    "v_exp_f32 v159, v159\n\t"
    "v_mfma_f32_32x32x16_bf16 v[176:191], v[200:203], v[40:43], v[64:79]\n\t"
    "ds_read_b64_tr_b16 a[192:193], v228 offset:0\n\t"
    "v_exp_f32 v160, v160\n\t"
    "v_exp_f32 v161, v161\n\t"
    "v_exp_f32 v162, v162\n\t"
    "v_mfma_f32_32x32x16_bf16 v[176:191], v[204:207], v[44:47], v[176:191]\n\t"
    "v_exp_f32 v163, v163\n\t"
    "v_exp_f32 v164, v164\n\t"
    "v_exp_f32 v165, v165\n\t"
    "v_smfmac_f32_32x32x32_bf16 v[96:111], v[208:211], v[144:151], v124\n\t"
    "v_exp_f32 v166, v166\n\t"
    "v_exp_f32 v167, v167\n\t"
    "v_exp_f32 v168, v168\n\t"
    "v_smfmac_f32_16x16x64_bf16 v[116:119], v[120:123], v[144:151], v124\n\t"
    "ds_read_b128 v[208:211], %[rb] offset:18432\n\t"
    "v_exp_f32 v169, v169\n\t"
    "v_exp_f32 v170, v170\n\t"
    "v_smfmac_f32_32x32x32_bf16 v[96:111], v[212:215], v[152:159], v124\n\t"
    "s_waitcnt lgkmcnt(2)\n\t"
    "v_exp_f32 v171, v171\n\t"
    "v_exp_f32 v172, v172\n\t"
    "v_exp_f32 v173, v173\n\t"
    "v_smfmac_f32_16x16x64_bf16 v[116:119], v[120:123], v[152:159], v124\n\t"
    "ds_read_b128 v[212:215], %[rb] offset:19456\n\t"
    "v_exp_f32 v174, v174\n\t"
    "v_exp_f32 v175, v175\n\t"
    "v_mfma_f32_32x32x16_bf16 v[128:143], v[192:195], v[32:35], v[48:63]\n\t"
    "ds_read_b64_tr_b16 a[194:195], v228 offset:2048\n\t"
    "s_add_u32 m0, s54, 114688\n\t"
    "v_exp_f32 v176, v176\n\t"
    "v_exp_f32 v177, v177\n\t"
    "v_exp_f32 v178, v178\n\t"
    "v_mfma_f32_32x32x16_bf16 v[128:143], v[196:199], v[36:39], v[128:143]\n\t"
    "s_mov_b32 s53, 1835008\n\t"
    "v_exp_f32 v179, v179\n\t"
    "v_exp_f32 v180, v180\n\t"
    "v_exp_f32 v181, v181\n\t"
    "v_smfmac_f32_32x32x32_bf16 v[80:95], v[216:219], v[160:167], v124\n\t"
    "buffer_load_dwordx4 v224, s[44:47], s53 offen offset:0 lds\n\t"
    "buffer_load_dwordx4 v225, s[44:47], s53 offen offset:1024 lds\n\t"
    "v_exp_f32 v182, v182\n\t"
    "v_exp_f32 v183, v183\n\t"
    "v_exp_f32 v184, v184\n\t"
    "v_smfmac_f32_16x16x64_bf16 v[112:115], v[120:123], v[160:167], v124\n\t"
    "ds_read_b128 v[200:203], %[rb] offset:20480\n\t"
    "v_exp_f32 v185, v185\n\t"
    "v_exp_f32 v186, v186\n\t"
    "v_smfmac_f32_32x32x32_bf16 v[80:95], v[220:223], v[168:175], v124\n\t"
    "v_exp_f32 v187, v187\n\t"
    "v_exp_f32 v188, v188\n\t"
    "v_exp_f32 v189, v189\n\t"
    "v_smfmac_f32_16x16x64_bf16 v[112:115], v[120:123], v[168:175], v124\n\t"
    "ds_read_b128 v[204:207], %[rb] offset:21504\n\t"
    "v_exp_f32 v190, v190\n\t"
    "v_exp_f32 v191, v191\n\t"
    "v_mfma_f32_32x32x16_bf16 v[144:159], v[192:195], v[40:43], v[64:79]\n\t"
    "ds_read_b64_tr_b16 a[196:197], v229 offset:0\n\t"
    "v_exp_f32 v128, v128\n\t"
    "v_exp_f32 v129, v129\n\t"
    "v_exp_f32 v130, v130\n\t"
    "v_mfma_f32_32x32x16_bf16 v[144:159], v[196:199], v[44:47], v[144:159]\n\t"
    "v_exp_f32 v131, v131\n\t"
    "v_exp_f32 v132, v132\n\t"
    "v_exp_f32 v133, v133\n\t"
    "v_smfmac_f32_32x32x32_bf16 v[96:111], v[216:219], v[176:183], v124\n\t"
    "buffer_load_dwordx4 v226, s[44:47], s53 offen offset:2048 lds\n\t"
    "buffer_load_dwordx4 v227, s[44:47], s53 offen offset:3072 lds\n\t"
    "v_exp_f32 v134, v134\n\t"
    "v_exp_f32 v135, v135\n\t"
    "v_exp_f32 v136, v136\n\t"
    "v_smfmac_f32_16x16x64_bf16 v[116:119], v[120:123], v[176:183], v124\n\t"
    "ds_read_b128 v[216:219], %[rb] offset:22528\n\t"
    "v_exp_f32 v137, v137\n\t"
    "v_exp_f32 v138, v138\n\t"
    "v_smfmac_f32_32x32x32_bf16 v[96:111], v[220:223], v[184:191], v124\n\t"
    "s_waitcnt lgkmcnt(2)\n\t"
    "v_exp_f32 v139, v139\n\t"
    "v_exp_f32 v140, v140\n\t"
    "v_exp_f32 v141, v141\n\t"
    "v_smfmac_f32_16x16x64_bf16 v[116:119], v[120:123], v[184:191], v124\n\t"
    "ds_read_b128 v[220:223], %[rb] offset:23552\n\t"
    "v_exp_f32 v142, v142\n\t"
    "v_exp_f32 v143, v143\n\t"
    "v_mfma_f32_32x32x16_bf16 v[160:175], v[200:203], v[32:35], v[48:63]\n\t"
    "ds_read_b64_tr_b16 a[198:199], v229 offset:2048\n\t"
    "s_add_u32 m0, s59, 131072\n\t"
    "v_exp_f32 v144, v144\n\t"
    "v_exp_f32 v145, v145\n\t"
    "v_exp_f32 v146, v146\n\t"
    "v_mfma_f32_32x32x16_bf16 v[160:175], v[204:207], v[36:39], v[160:175]\n\t"
    "s_mov_b32 s58, 4194304\n\t"
    "v_exp_f32 v147, v147\n\t"
    "v_exp_f32 v148, v148\n\t"
    "v_exp_f32 v149, v149\n\t"
    "v_smfmac_f32_32x32x32_bf16 v[80:95], v[208:211], v[128:135], v124\n\t"
    "buffer_load_dwordx4 v244, s[48:51], s58 offen offset:0 sc1 lds\n\t"
    "buffer_load_dwordx4 v244, s[48:51], s58 offen offset:1024 sc1 lds\n\t"
    "v_exp_f32 v150, v150\n\t"
    "v_exp_f32 v151, v151\n\t"
    "v_exp_f32 v152, v152\n\t"
    "v_smfmac_f32_16x16x64_bf16 v[112:115], v[120:123], v[128:135], v124\n\t"
    "ds_read_b128 v[192:195], %[rb] offset:24576\n\t"
    "v_exp_f32 v153, v153\n\t"
    "v_exp_f32 v154, v154\n\t"
    "v_smfmac_f32_32x32x32_bf16 v[80:95], v[212:215], v[136:143], v124\n\t"
    "v_exp_f32 v155, v155\n\t"
    "v_exp_f32 v156, v156\n\t"
    "v_exp_f32 v157, v157\n\t"
    "v_smfmac_f32_16x16x64_bf16 v[112:115], v[120:123], v[136:143], v124\n\t"
    "ds_read_b128 v[196:199], %[rb] offset:25600\n\t"
    "v_exp_f32 v158, v158\n\t"
    "v_exp_f32 v159, v159\n\t"
    "v_mfma_f32_32x32x16_bf16 v[176:191], v[200:203], v[40:43], v[64:79]\n\t"
    "ds_read_b64_tr_b16 a[200:201], v228 offset:4096\n\t"
    "v_exp_f32 v160, v160\n\t"
    "v_exp_f32 v161, v161\n\t"
    "v_exp_f32 v162, v162\n\t"
    "v_mfma_f32_32x32x16_bf16 v[176:191], v[204:207], v[44:47], v[176:191]\n\t"
    "v_exp_f32 v163, v163\n\t"
    "v_exp_f32 v164, v164\n\t"
    "v_exp_f32 v165, v165\n\t"
    "v_smfmac_f32_32x32x32_bf16 v[96:111], v[208:211], v[144:151], v124\n\t"
    "buffer_load_dwordx4 v245, s[48:51], s58 offen offset:2048 sc1 lds\n\t"
    "buffer_load_dwordx4 v245, s[48:51], s58 offen offset:3072 sc1 lds\n\t"
    "v_exp_f32 v166, v166\n\t"
    "v_exp_f32 v167, v167\n\t"
    "v_exp_f32 v168, v168\n\t"
    "v_smfmac_f32_16x16x64_bf16 v[116:119], v[120:123], v[144:151], v124\n\t"
    "ds_read_b128 v[208:211], %[rb] offset:26624\n\t"
    "v_exp_f32 v169, v169\n\t"
    "v_exp_f32 v170, v170\n\t"
    "v_smfmac_f32_32x32x32_bf16 v[96:111], v[212:215], v[152:159], v124\n\t"
    "s_waitcnt lgkmcnt(2)\n\t"
    "v_exp_f32 v171, v171\n\t"
    "v_exp_f32 v172, v172\n\t"
    "v_exp_f32 v173, v173\n\t"
    "v_smfmac_f32_16x16x64_bf16 v[116:119], v[120:123], v[152:159], v124\n\t"
    "ds_read_b128 v[212:215], %[rb] offset:27648\n\t"
    "v_exp_f32 v174, v174\n\t"
    "v_exp_f32 v175, v175\n\t"
    "v_mfma_f32_32x32x16_bf16 v[128:143], v[192:195], v[32:35], v[48:63]\n\t"
    "ds_read_b64_tr_b16 a[202:203], v228 offset:6144\n\t"
    "s_add_u32 m0, s59, 135168\n\t"
    "v_exp_f32 v176, v176\n\t"
    "v_exp_f32 v177, v177\n\t"
    "v_exp_f32 v178, v178\n\t"
    "v_mfma_f32_32x32x16_bf16 v[128:143], v[196:199], v[36:39], v[128:143]\n\t"
    "s_mov_b32 s58, 4194304\n\t"
    "v_exp_f32 v179, v179\n\t"
    "v_exp_f32 v180, v180\n\t"
    "v_exp_f32 v181, v181\n\t"
    "v_smfmac_f32_32x32x32_bf16 v[80:95], v[216:219], v[160:167], v124\n\t"
    "buffer_load_dwordx4 v246, s[48:51], s58 offen offset:0 sc1 lds\n\t"
    "buffer_load_dwordx4 v246, s[48:51], s58 offen offset:1024 sc1 lds\n\t"
    "v_exp_f32 v182, v182\n\t"
    "v_exp_f32 v183, v183\n\t"
    "v_exp_f32 v184, v184\n\t"
    "v_smfmac_f32_16x16x64_bf16 v[112:115], v[120:123], v[160:167], v124\n\t"
    "ds_read_b128 v[200:203], %[rb] offset:28672\n\t"
    "v_exp_f32 v185, v185\n\t"
    "v_exp_f32 v186, v186\n\t"
    "v_smfmac_f32_32x32x32_bf16 v[80:95], v[220:223], v[168:175], v124\n\t"
    "v_exp_f32 v187, v187\n\t"
    "v_exp_f32 v188, v188\n\t"
    "v_exp_f32 v189, v189\n\t"
    "v_smfmac_f32_16x16x64_bf16 v[112:115], v[120:123], v[168:175], v124\n\t"
    "ds_read_b128 v[204:207], %[rb] offset:29696\n\t"
    "v_exp_f32 v190, v190\n\t"
    "v_exp_f32 v191, v191\n\t"
    "v_mfma_f32_32x32x16_bf16 v[144:159], v[192:195], v[40:43], v[64:79]\n\t"
    "ds_read_b64_tr_b16 a[204:205], v229 offset:4096\n\t"
    "ds_read_b64_tr_b16 a[206:207], v229 offset:6144\n\t"
    "v_exp_f32 v128, v128\n\t"
    "v_exp_f32 v129, v129\n\t"
    "v_exp_f32 v130, v130\n\t"
    "v_mfma_f32_32x32x16_bf16 v[144:159], v[196:199], v[44:47], v[144:159]\n\t"
    "v_exp_f32 v131, v131\n\t"
    "v_exp_f32 v132, v132\n\t"
    "v_exp_f32 v133, v133\n\t"
    "v_smfmac_f32_32x32x32_bf16 v[96:111], v[216:219], v[176:183], v124\n\t"
    "buffer_load_dwordx4 v247, s[48:51], s58 offen offset:2048 sc1 lds\n\t"
    "buffer_load_dwordx4 v247, s[48:51], s58 offen offset:3072 sc1 lds\n\t"
    "v_exp_f32 v134, v134\n\t"
    "v_exp_f32 v135, v135\n\t"
    "v_exp_f32 v136, v136\n\t"
    "v_smfmac_f32_16x16x64_bf16 v[116:119], v[120:123], v[176:183], v124\n\t"
    "ds_read_b128 v[216:219], %[rb] offset:30720\n\t"
    "v_exp_f32 v137, v137\n\t"
    "v_exp_f32 v138, v138\n\t"
    "v_smfmac_f32_32x32x32_bf16 v[96:111], v[220:223], v[184:191], v124\n\t"
    "s_waitcnt lgkmcnt(3)\n\t"
    "v_exp_f32 v139, v139\n\t"
    "v_exp_f32 v140, v140\n\t"
    "v_exp_f32 v141, v141\n\t"
    "v_smfmac_f32_16x16x64_bf16 v[116:119], v[120:123], v[184:191], v124\n\t"
    "ds_read_b128 v[220:223], %[rb] offset:31744\n\t"
    "v_exp_f32 v142, v142\n\t"
    "v_exp_f32 v143, v143\n\t"
    "v_mfma_f32_32x32x16_bf16 v[160:175], v[200:203], v[32:35], v[48:63]\n\t"
    "v_exp_f32 v144, v144\n\t"
    "v_exp_f32 v145, v145\n\t"
    "v_exp_f32 v146, v146\n\t"
    "v_mfma_f32_32x32x16_bf16 v[160:175], v[204:207], v[36:39], v[160:175]\n\t"
    "v_exp_f32 v147, v147\n\t"
    "v_exp_f32 v148, v148\n\t"
    "v_exp_f32 v149, v149\n\t"
    "v_smfmac_f32_32x32x32_bf16 v[80:95], v[208:211], v[128:135], v124\n\t"
    "s_waitcnt vmcnt(21) lgkmcnt(0)\n\t"
    "s_barrier\n\t"
    "v_exp_f32 v150, v150\n\t"
    "v_exp_f32 v151, v151\n\t"
    "v_exp_f32 v152, v152\n\t"
    "v_smfmac_f32_16x16x64_bf16 v[112:115], v[120:123], v[128:135], v124\n\t"
    "ds_read_b128 v[192:195], %[rb] offset:32768\n\t"
    "v_exp_f32 v153, v153\n\t"
    "v_exp_f32 v154, v154\n\t"
    "v_smfmac_f32_32x32x32_bf16 v[80:95], v[212:215], v[136:143], v124\n\t"
    "v_exp_f32 v155, v155\n\t"
    "v_exp_f32 v156, v156\n\t"
    "v_exp_f32 v157, v157\n\t"
    "v_smfmac_f32_16x16x64_bf16 v[112:115], v[120:123], v[136:143], v124\n\t"
    "ds_read_b128 v[196:199], %[rb] offset:33792\n\t"
    "v_exp_f32 v158, v158\n\t"
    "v_exp_f32 v159, v159\n\t"
    "v_mfma_f32_32x32x16_bf16 v[176:191], v[200:203], v[40:43], v[64:79]\n\t"
    "ds_read_b64_tr_b16 a[208:209], v228 offset:16384\n\t"
    "s_add_u32 m0, s59, 0\n\t"
    "v_exp_f32 v160, v160\n\t"
    "v_exp_f32 v161, v161\n\t"
    "v_exp_f32 v162, v162\n\t"
    "v_mfma_f32_32x32x16_bf16 v[176:191], v[204:207], v[44:47], v[176:191]\n\t"
    "s_mov_b32 s58, 4202496\n\t"
    "v_exp_f32 v163, v163\n\t"
    "v_exp_f32 v164, v164\n\t"
    "v_exp_f32 v165, v165\n\t"
    "v_smfmac_f32_32x32x32_bf16 v[96:111], v[208:211], v[144:151], v124\n\t"
    "buffer_load_dwordx4 v244, s[48:51], s58 offen offset:0 sc1 lds\n\t"
    "buffer_load_dwordx4 v244, s[48:51], s58 offen offset:1024 sc1 lds\n\t"
    "v_exp_f32 v166, v166\n\t"
    "v_exp_f32 v167, v167\n\t"
    "v_exp_f32 v168, v168\n\t"
    "v_smfmac_f32_16x16x64_bf16 v[116:119], v[120:123], v[144:151], v124\n\t"
    "ds_read_b128 v[208:211], %[rb] offset:34816\n\t"
    "v_exp_f32 v169, v169\n\t"
    "v_exp_f32 v170, v170\n\t"
    "v_smfmac_f32_32x32x32_bf16 v[96:111], v[212:215], v[152:159], v124\n\t"
    "s_waitcnt lgkmcnt(2)\n\t"
    "v_exp_f32 v171, v171\n\t"
    "v_exp_f32 v172, v172\n\t"
    "v_exp_f32 v173, v173\n\t"
    "v_smfmac_f32_16x16x64_bf16 v[116:119], v[120:123], v[152:159], v124\n\t"
    "ds_read_b128 v[212:215], %[rb] offset:35840\n\t"
    "v_exp_f32 v174, v174\n\t"
    "v_exp_f32 v175, v175\n\t"
    "v_mfma_f32_32x32x16_bf16 v[128:143], v[192:195], v[32:35], v[48:63]\n\t"
    "ds_read_b64_tr_b16 a[210:211], v228 offset:18432\n\t"
    "v_exp_f32 v176, v176\n\t"
    "v_exp_f32 v177, v177\n\t"
    "v_exp_f32 v178, v178\n\t"
    "v_mfma_f32_32x32x16_bf16 v[128:143], v[196:199], v[36:39], v[128:143]\n\t"
    "v_exp_f32 v179, v179\n\t"
    "v_exp_f32 v180, v180\n\t"
    "v_exp_f32 v181, v181\n\t"
    "v_smfmac_f32_32x32x32_bf16 v[80:95], v[216:219], v[160:167], v124\n\t"
    "v_exp_f32 v182, v182\n\t"
    "v_exp_f32 v183, v183\n\t"
    "v_exp_f32 v184, v184\n\t"
    "v_smfmac_f32_16x16x64_bf16 v[112:115], v[120:123], v[160:167], v124\n\t"
    "ds_read_b128 v[200:203], %[rb] offset:36864\n\t"
    "v_exp_f32 v185, v185\n\t"
    "v_exp_f32 v186, v186\n\t"
    "v_smfmac_f32_32x32x32_bf16 v[80:95], v[220:223], v[168:175], v124\n\t"
    "v_exp_f32 v187, v187\n\t"
    "v_exp_f32 v188, v188\n\t"
    "v_exp_f32 v189, v189\n\t"
    "v_smfmac_f32_16x16x64_bf16 v[112:115], v[120:123], v[168:175], v124\n\t"
    "ds_read_b128 v[204:207], %[rb] offset:37888\n\t"
    "v_exp_f32 v190, v190\n\t"
    "v_exp_f32 v191, v191\n\t"
    "v_mfma_f32_32x32x16_bf16 v[144:159], v[192:195], v[40:43], v[64:79]\n\t"
    "ds_read_b64_tr_b16 a[212:213], v229 offset:16384\n\t"
    "v_exp_f32 v128, v128\n\t"
    "v_exp_f32 v129, v129\n\t"
    "v_exp_f32 v130, v130\n\t"
    "v_mfma_f32_32x32x16_bf16 v[144:159], v[196:199], v[44:47], v[144:159]\n\t"
    "v_exp_f32 v131, v131\n\t"
    "v_exp_f32 v132, v132\n\t"
    "v_exp_f32 v133, v133\n\t"
    "v_smfmac_f32_32x32x32_bf16 v[96:111], v[216:219], v[176:183], v124\n\t"
    "buffer_load_dwordx4 v245, s[48:51], s58 offen offset:2048 sc1 lds\n\t"
    "buffer_load_dwordx4 v245, s[48:51], s58 offen offset:3072 sc1 lds\n\t"
    "v_exp_f32 v134, v134\n\t"
    "v_exp_f32 v135, v135\n\t"
    "v_exp_f32 v136, v136\n\t"
    "v_smfmac_f32_16x16x64_bf16 v[116:119], v[120:123], v[176:183], v124\n\t"
    "ds_read_b128 v[216:219], %[rb] offset:38912\n\t"
    "v_exp_f32 v137, v137\n\t"
    "v_exp_f32 v138, v138\n\t"
    "v_smfmac_f32_32x32x32_bf16 v[96:111], v[220:223], v[184:191], v124\n\t"
    "s_waitcnt lgkmcnt(2)\n\t"
    "v_exp_f32 v139, v139\n\t"
    "v_exp_f32 v140, v140\n\t"
    "v_exp_f32 v141, v141\n\t"
    "v_smfmac_f32_16x16x64_bf16 v[116:119], v[120:123], v[184:191], v124\n\t"
    "ds_read_b128 v[220:223], %[rb] offset:39936\n\t"
    "v_exp_f32 v142, v142\n\t"
    "v_exp_f32 v143, v143\n\t"
    "v_mfma_f32_32x32x16_bf16 v[160:175], v[200:203], v[32:35], v[48:63]\n\t"
    "ds_read_b64_tr_b16 a[214:215], v229 offset:18432\n\t"
    "v_exp_f32 v144, v144\n\t"
    "v_exp_f32 v145, v145\n\t"
    "v_exp_f32 v146, v146\n\t"
    "v_mfma_f32_32x32x16_bf16 v[160:175], v[204:207], v[36:39], v[160:175]\n\t"
    "v_exp_f32 v147, v147\n\t"
    "v_exp_f32 v148, v148\n\t"
    "v_exp_f32 v149, v149\n\t"
    "v_smfmac_f32_32x32x32_bf16 v[80:95], v[208:211], v[128:135], v124\n\t"
    "v_exp_f32 v150, v150\n\t"
    "v_exp_f32 v151, v151\n\t"
    "v_exp_f32 v152, v152\n\t"
    "v_smfmac_f32_16x16x64_bf16 v[112:115], v[120:123], v[128:135], v124\n\t"
    "ds_read_b128 v[192:195], %[rb] offset:40960\n\t"
    "v_exp_f32 v153, v153\n\t"
    "v_exp_f32 v154, v154\n\t"
    "v_smfmac_f32_32x32x32_bf16 v[80:95], v[212:215], v[136:143], v124\n\t"
    "v_exp_f32 v155, v155\n\t"
    "v_exp_f32 v156, v156\n\t"
    "v_exp_f32 v157, v157\n\t"
    "v_smfmac_f32_16x16x64_bf16 v[112:115], v[120:123], v[136:143], v124\n\t"
    "ds_read_b128 v[196:199], %[rb] offset:41984\n\t"
    "v_exp_f32 v158, v158\n\t"
    "v_exp_f32 v159, v159\n\t"
    "v_mfma_f32_32x32x16_bf16 v[176:191], v[200:203], v[40:43], v[64:79]\n\t"
    "ds_read_b64_tr_b16 a[216:217], v228 offset:20480\n\t"
    "s_add_u32 m0, s59, 4096\n\t"
    "v_exp_f32 v160, v160\n\t"
    "v_exp_f32 v161, v161\n\t"
    "v_exp_f32 v162, v162\n\t"
    "v_mfma_f32_32x32x16_bf16 v[176:191], v[204:207], v[44:47], v[176:191]\n\t"
    "s_mov_b32 s58, 4202496\n\t"
    "v_exp_f32 v163, v163\n\t"
    "v_exp_f32 v164, v164\n\t"
    "v_exp_f32 v165, v165\n\t"
    "v_smfmac_f32_32x32x32_bf16 v[96:111], v[208:211], v[144:151], v124\n\t"
    "buffer_load_dwordx4 v246, s[48:51], s58 offen offset:0 sc1 lds\n\t"
    "buffer_load_dwordx4 v246, s[48:51], s58 offen offset:1024 sc1 lds\n\t"
    "v_exp_f32 v166, v166\n\t"
    "v_exp_f32 v167, v167\n\t"
    "v_exp_f32 v168, v168\n\t"
    "v_smfmac_f32_16x16x64_bf16 v[116:119], v[120:123], v[144:151], v124\n\t"
    "ds_read_b128 v[208:211], %[rb] offset:43008\n\t"
    "v_exp_f32 v169, v169\n\t"
    "v_exp_f32 v170, v170\n\t"
    "v_smfmac_f32_32x32x32_bf16 v[96:111], v[212:215], v[152:159], v124\n\t"
    "s_waitcnt lgkmcnt(2)\n\t"
    "v_exp_f32 v171, v171\n\t"
    "v_exp_f32 v172, v172\n\t"
    "v_exp_f32 v173, v173\n\t"
    "v_smfmac_f32_16x16x64_bf16 v[116:119], v[120:123], v[152:159], v124\n\t"
    "ds_read_b128 v[212:215], %[rb] offset:44032\n\t"
    "v_exp_f32 v174, v174\n\t"
    "v_exp_f32 v175, v175\n\t"
    "v_mfma_f32_32x32x16_bf16 v[128:143], v[192:195], v[32:35], v[48:63]\n\t"
    "ds_read_b64_tr_b16 a[218:219], v228 offset:22528\n\t"
    "v_exp_f32 v176, v176\n\t"
    "v_exp_f32 v177, v177\n\t"
    "v_exp_f32 v178, v178\n\t"
    "v_mfma_f32_32x32x16_bf16 v[128:143], v[196:199], v[36:39], v[128:143]\n\t"
    "v_exp_f32 v179, v179\n\t"
    "v_exp_f32 v180, v180\n\t"
    "v_exp_f32 v181, v181\n\t"
    "v_smfmac_f32_32x32x32_bf16 v[80:95], v[216:219], v[160:167], v124\n\t"
    "v_exp_f32 v182, v182\n\t"
    "v_exp_f32 v183, v183\n\t"
    "v_exp_f32 v184, v184\n\t"
    "v_smfmac_f32_16x16x64_bf16 v[112:115], v[120:123], v[160:167], v124\n\t"
    "ds_read_b128 v[200:203], %[rb] offset:45056\n\t"
    "v_exp_f32 v185, v185\n\t"
    "v_exp_f32 v186, v186\n\t"
    "v_smfmac_f32_32x32x32_bf16 v[80:95], v[220:223], v[168:175], v124\n\t"
    "v_exp_f32 v187, v187\n\t"
    "v_exp_f32 v188, v188\n\t"
    "v_exp_f32 v189, v189\n\t"
    "v_smfmac_f32_16x16x64_bf16 v[112:115], v[120:123], v[168:175], v124\n\t"
    "ds_read_b128 v[204:207], %[rb] offset:46080\n\t"
    "v_exp_f32 v190, v190\n\t"
    "v_exp_f32 v191, v191\n\t"
    "v_mfma_f32_32x32x16_bf16 v[144:159], v[192:195], v[40:43], v[64:79]\n\t"
    "ds_read_b64_tr_b16 a[220:221], v229 offset:20480\n\t"
    "ds_read_b64_tr_b16 a[222:223], v229 offset:22528\n\t"
    "v_exp_f32 v128, v128\n\t"
    "v_exp_f32 v129, v129\n\t"
    "v_exp_f32 v130, v130\n\t"
    "v_mfma_f32_32x32x16_bf16 v[144:159], v[196:199], v[44:47], v[144:159]\n\t"
    "v_exp_f32 v131, v131\n\t"
    "v_exp_f32 v132, v132\n\t"
    "v_exp_f32 v133, v133\n\t"
    "v_smfmac_f32_32x32x32_bf16 v[96:111], v[216:219], v[176:183], v124\n\t"
    "buffer_load_dwordx4 v247, s[48:51], s58 offen offset:2048 sc1 lds\n\t"
    "buffer_load_dwordx4 v247, s[48:51], s58 offen offset:3072 sc1 lds\n\t"
    "v_exp_f32 v134, v134\n\t"
    "v_exp_f32 v135, v135\n\t"
    "v_exp_f32 v136, v136\n\t"
    "v_smfmac_f32_16x16x64_bf16 v[116:119], v[120:123], v[176:183], v124\n\t"
    "ds_read_b128 v[216:219], %[rb] offset:47104\n\t"
    "v_exp_f32 v137, v137\n\t"
    "v_exp_f32 v138, v138\n\t"
    "v_smfmac_f32_32x32x32_bf16 v[96:111], v[220:223], v[184:191], v124\n\t"
    "s_waitcnt lgkmcnt(3)\n\t"
    "v_exp_f32 v139, v139\n\t"
    "v_exp_f32 v140, v140\n\t"
    "v_exp_f32 v141, v141\n\t"
    "v_smfmac_f32_16x16x64_bf16 v[116:119], v[120:123], v[184:191], v124\n\t"
    "ds_read_b128 v[220:223], %[rb] offset:48128\n\t"
    "v_exp_f32 v142, v142\n\t"
    "v_exp_f32 v143, v143\n\t"
    "v_mfma_f32_32x32x16_bf16 v[160:175], v[200:203], v[32:35], v[48:63]\n\t"
    "v_exp_f32 v144, v144\n\t"
    "v_exp_f32 v145, v145\n\t"
    "v_exp_f32 v146, v146\n\t"
    "v_mfma_f32_32x32x16_bf16 v[160:175], v[204:207], v[36:39], v[160:175]\n\t"
    "v_exp_f32 v147, v147\n\t"
    "v_exp_f32 v148, v148\n\t"
    "v_exp_f32 v149, v149\n\t"
    "v_smfmac_f32_32x32x32_bf16 v[80:95], v[208:211], v[128:135], v124\n\t"
    "s_waitcnt vmcnt(20) lgkmcnt(0)\n\t"
    "s_barrier\n\t"
    "v_exp_f32 v150, v150\n\t"
    "v_exp_f32 v151, v151\n\t"
    "v_exp_f32 v152, v152\n\t"
    "v_smfmac_f32_16x16x64_bf16 v[112:115], v[120:123], v[128:135], v124\n\t"
    "ds_read_b128 v[192:195], %[rb] offset:49152\n\t"
    "v_exp_f32 v153, v153\n\t"
    "v_exp_f32 v154, v154\n\t"
    "v_smfmac_f32_32x32x32_bf16 v[80:95], v[212:215], v[136:143], v124\n\t"
    "v_exp_f32 v155, v155\n\t"
    "v_exp_f32 v156, v156\n\t"
    "v_exp_f32 v157, v157\n\t"
    "v_smfmac_f32_16x16x64_bf16 v[112:115], v[120:123], v[136:143], v124\n\t"
    "ds_read_b128 v[196:199], %[rb] offset:50176\n\t"
    "v_exp_f32 v158, v158\n\t"
    "v_exp_f32 v159, v159\n\t"
    "v_mfma_f32_32x32x16_bf16 v[176:191], v[200:203], v[40:43], v[64:79]\n\t"
    "ds_read_b64_tr_b16 a[224:225], v228 offset:32768\n\t"
    "s_add_u32 m0, s59, 65536\n\t"
    "v_exp_f32 v160, v160\n\t"
    "v_exp_f32 v161, v161\n\t"
    "v_exp_f32 v162, v162\n\t"
    "v_mfma_f32_32x32x16_bf16 v[176:191], v[204:207], v[44:47], v[176:191]\n\t"
    "s_mov_b32 s58, 4210688\n\t"
    "v_exp_f32 v163, v163\n\t"
    "v_exp_f32 v164, v164\n\t"
    "v_exp_f32 v165, v165\n\t"
    "v_smfmac_f32_32x32x32_bf16 v[96:111], v[208:211], v[144:151], v124\n\t"
    "buffer_load_dwordx4 v244, s[48:51], s58 offen offset:0 sc1 lds\n\t"
    "buffer_load_dwordx4 v244, s[48:51], s58 offen offset:1024 sc1 lds\n\t"
    "v_exp_f32 v166, v166\n\t"
    "v_exp_f32 v167, v167\n\t"
    "v_exp_f32 v168, v168\n\t"
    "v_smfmac_f32_16x16x64_bf16 v[116:119], v[120:123], v[144:151], v124\n\t"
    "ds_read_b128 v[208:211], %[rb] offset:51200\n\t"
    "v_exp_f32 v169, v169\n\t"
    "v_exp_f32 v170, v170\n\t"
    "v_smfmac_f32_32x32x32_bf16 v[96:111], v[212:215], v[152:159], v124\n\t"
    "s_waitcnt lgkmcnt(2)\n\t"
    "v_exp_f32 v171, v171\n\t"
    "v_exp_f32 v172, v172\n\t"
    "v_exp_f32 v173, v173\n\t"
    "v_smfmac_f32_16x16x64_bf16 v[116:119], v[120:123], v[152:159], v124\n\t"
    "ds_read_b128 v[212:215], %[rb] offset:52224\n\t"
    "v_exp_f32 v174, v174\n\t"
    "v_exp_f32 v175, v175\n\t"
    "v_mfma_f32_32x32x16_bf16 v[128:143], v[192:195], v[32:35], v[48:63]\n\t"
    "ds_read_b64_tr_b16 a[226:227], v228 offset:34816\n\t"
    "v_exp_f32 v176, v176\n\t"
    "v_exp_f32 v177, v177\n\t"
    "v_exp_f32 v178, v178\n\t"
    "v_mfma_f32_32x32x16_bf16 v[128:143], v[196:199], v[36:39], v[128:143]\n\t"
    "v_exp_f32 v179, v179\n\t"
    "v_exp_f32 v180, v180\n\t"
    "v_exp_f32 v181, v181\n\t"
    "v_smfmac_f32_32x32x32_bf16 v[80:95], v[216:219], v[160:167], v124\n\t"
    "v_exp_f32 v182, v182\n\t"
    "v_exp_f32 v183, v183\n\t"
    "v_exp_f32 v184, v184\n\t"
    "v_smfmac_f32_16x16x64_bf16 v[112:115], v[120:123], v[160:167], v124\n\t"
    "ds_read_b128 v[200:203], %[rb] offset:53248\n\t"
    "v_exp_f32 v185, v185\n\t"
    "v_exp_f32 v186, v186\n\t"
    "v_smfmac_f32_32x32x32_bf16 v[80:95], v[220:223], v[168:175], v124\n\t"
    "v_exp_f32 v187, v187\n\t"
    "v_exp_f32 v188, v188\n\t"
    "v_exp_f32 v189, v189\n\t"
    "v_smfmac_f32_16x16x64_bf16 v[112:115], v[120:123], v[168:175], v124\n\t"
    "ds_read_b128 v[204:207], %[rb] offset:54272\n\t"
    "v_exp_f32 v190, v190\n\t"
    "v_exp_f32 v191, v191\n\t"
    "v_mfma_f32_32x32x16_bf16 v[144:159], v[192:195], v[40:43], v[64:79]\n\t"
    "ds_read_b64_tr_b16 a[228:229], v229 offset:32768\n\t"
    "v_exp_f32 v128, v128\n\t"
    "v_exp_f32 v129, v129\n\t"
    "v_exp_f32 v130, v130\n\t"
    "v_mfma_f32_32x32x16_bf16 v[144:159], v[196:199], v[44:47], v[144:159]\n\t"
    "v_exp_f32 v131, v131\n\t"
    "v_exp_f32 v132, v132\n\t"
    "v_exp_f32 v133, v133\n\t"
    "v_smfmac_f32_32x32x32_bf16 v[96:111], v[216:219], v[176:183], v124\n\t"
    "buffer_load_dwordx4 v245, s[48:51], s58 offen offset:2048 sc1 lds\n\t"
    "buffer_load_dwordx4 v245, s[48:51], s58 offen offset:3072 sc1 lds\n\t"
    "v_exp_f32 v134, v134\n\t"
    "v_exp_f32 v135, v135\n\t"
    "v_exp_f32 v136, v136\n\t"
    "v_smfmac_f32_16x16x64_bf16 v[116:119], v[120:123], v[176:183], v124\n\t"
    "ds_read_b128 v[216:219], %[rb] offset:55296\n\t"
    "v_exp_f32 v137, v137\n\t"
    "v_exp_f32 v138, v138\n\t"
    "v_smfmac_f32_32x32x32_bf16 v[96:111], v[220:223], v[184:191], v124\n\t"
    "s_waitcnt lgkmcnt(2)\n\t"
    "global_load_dwordx4 v[250:253], v248, s[62:63]\n\t"
    "v_exp_f32 v139, v139\n\t"
    "v_exp_f32 v140, v140\n\t"
    "v_exp_f32 v141, v141\n\t"
    "v_smfmac_f32_16x16x64_bf16 v[116:119], v[120:123], v[184:191], v124\n\t"
    "ds_read_b128 v[220:223], %[rb] offset:56320\n\t"
    "v_exp_f32 v142, v142\n\t"
    "v_exp_f32 v143, v143\n\t"
    "v_mfma_f32_32x32x16_bf16 v[160:175], v[200:203], v[32:35], v[48:63]\n\t"
    "ds_read_b64_tr_b16 a[230:231], v229 offset:34816\n\t"
    "v_exp_f32 v144, v144\n\t"
    "v_exp_f32 v145, v145\n\t"
    "v_exp_f32 v146, v146\n\t"
    "v_mfma_f32_32x32x16_bf16 v[160:175], v[204:207], v[36:39], v[160:175]\n\t"
    "v_exp_f32 v147, v147\n\t"
    "v_exp_f32 v148, v148\n\t"
    "v_exp_f32 v149, v149\n\t"
    "v_smfmac_f32_32x32x32_bf16 v[80:95], v[208:211], v[128:135], v124\n\t"
    "v_exp_f32 v150, v150\n\t"
    "v_exp_f32 v151, v151\n\t"
    "v_exp_f32 v152, v152\n\t"
    "v_smfmac_f32_16x16x64_bf16 v[112:115], v[120:123], v[128:135], v124\n\t"
    "ds_read_b128 v[192:195], %[rb] offset:57344\n\t"
    "v_exp_f32 v153, v153\n\t"
    "v_exp_f32 v154, v154\n\t"
    "v_smfmac_f32_32x32x32_bf16 v[80:95], v[212:215], v[136:143], v124\n\t"
    "v_exp_f32 v155, v155\n\t"
    "v_exp_f32 v156, v156\n\t"
    "v_exp_f32 v157, v157\n\t"
    "v_smfmac_f32_16x16x64_bf16 v[112:115], v[120:123], v[136:143], v124\n\t"
    "ds_read_b128 v[196:199], %[rb] offset:58368\n\t"
    "v_exp_f32 v158, v158\n\t"
    "v_exp_f32 v159, v159\n\t"
    "v_mfma_f32_32x32x16_bf16 v[176:191], v[200:203], v[40:43], v[64:79]\n\t"
    "ds_read_b64_tr_b16 a[232:233], v228 offset:36864\n\t"
    "s_add_u32 m0, s59, 69632\n\t"
    "v_exp_f32 v160, v160\n\t"
    "v_exp_f32 v161, v161\n\t"
    "v_exp_f32 v162, v162\n\t"
    "v_mfma_f32_32x32x16_bf16 v[176:191], v[204:207], v[44:47], v[176:191]\n\t"
    "s_mov_b32 s58, 4210688\n\t"
    "v_exp_f32 v163, v163\n\t"
    "v_exp_f32 v164, v164\n\t"
    "v_exp_f32 v165, v165\n\t"
    "v_smfmac_f32_32x32x32_bf16 v[96:111], v[208:211], v[144:151], v124\n\t"
    "buffer_load_dwordx4 v246, s[48:51], s58 offen offset:0 sc1 lds\n\t"
    "buffer_load_dwordx4 v246, s[48:51], s58 offen offset:1024 sc1 lds\n\t"
    "v_exp_f32 v166, v166\n\t"
    "v_exp_f32 v167, v167\n\t"
    "v_exp_f32 v168, v168\n\t"
    "v_smfmac_f32_16x16x64_bf16 v[116:119], v[120:123], v[144:151], v124\n\t"
    "ds_read_b128 v[208:211], %[rb] offset:59392\n\t"
    "v_exp_f32 v169, v169\n\t"
    "v_exp_f32 v170, v170\n\t"
    "v_smfmac_f32_32x32x32_bf16 v[96:111], v[212:215], v[152:159], v124\n\t"
    "s_waitcnt lgkmcnt(2)\n\t"
    "v_exp_f32 v171, v171\n\t"
    "v_exp_f32 v172, v172\n\t"
    "v_exp_f32 v173, v173\n\t"
    "v_smfmac_f32_16x16x64_bf16 v[116:119], v[120:123], v[152:159], v124\n\t"
    "ds_read_b128 v[212:215], %[rb] offset:60416\n\t"
    "v_exp_f32 v174, v174\n\t"
    "v_exp_f32 v175, v175\n\t"
    "v_mfma_f32_32x32x16_bf16 v[128:143], v[192:195], v[32:35], v[48:63]\n\t"
    "ds_read_b64_tr_b16 a[234:235], v228 offset:38912\n\t"
    "v_exp_f32 v176, v176\n\t"
    "v_exp_f32 v177, v177\n\t"
    "v_exp_f32 v178, v178\n\t"
    "v_mfma_f32_32x32x16_bf16 v[128:143], v[196:199], v[36:39], v[128:143]\n\t"
    "v_exp_f32 v179, v179\n\t"
    "v_exp_f32 v180, v180\n\t"
    "v_exp_f32 v181, v181\n\t"
    "v_smfmac_f32_32x32x32_bf16 v[80:95], v[216:219], v[160:167], v124\n\t"
    "v_exp_f32 v182, v182\n\t"
    "v_exp_f32 v183, v183\n\t"
    "v_exp_f32 v184, v184\n\t"
    "v_smfmac_f32_16x16x64_bf16 v[112:115], v[120:123], v[160:167], v124\n\t"
    "ds_read_b128 v[200:203], %[rb] offset:61440\n\t"
    "v_exp_f32 v185, v185\n\t"
    "v_exp_f32 v186, v186\n\t"
    "v_smfmac_f32_32x32x32_bf16 v[80:95], v[220:223], v[168:175], v124\n\t"
    "v_exp_f32 v187, v187\n\t"
    "v_exp_f32 v188, v188\n\t"
    "v_exp_f32 v189, v189\n\t"
    "v_smfmac_f32_16x16x64_bf16 v[112:115], v[120:123], v[168:175], v124\n\t"
    "ds_read_b128 v[204:207], %[rb] offset:62464\n\t"
    "v_exp_f32 v190, v190\n\t"
    "v_exp_f32 v191, v191\n\t"
    "v_mfma_f32_32x32x16_bf16 v[144:159], v[192:195], v[40:43], v[64:79]\n\t"
    "ds_read_b64_tr_b16 a[236:237], v229 offset:36864\n\t"
    "ds_read_b64_tr_b16 a[238:239], v229 offset:38912\n\t"
    "v_exp_f32 v128, v128\n\t"
    "v_exp_f32 v129, v129\n\t"
    "v_exp_f32 v130, v130\n\t"
    "v_mfma_f32_32x32x16_bf16 v[144:159], v[196:199], v[44:47], v[144:159]\n\t"
    "v_exp_f32 v131, v131\n\t"
    "v_exp_f32 v132, v132\n\t"
    "v_exp_f32 v133, v133\n\t"
    "v_smfmac_f32_32x32x32_bf16 v[96:111], v[216:219], v[176:183], v124\n\t"
    "buffer_load_dwordx4 v247, s[48:51], s58 offen offset:2048 sc1 lds\n\t"
    "buffer_load_dwordx4 v247, s[48:51], s58 offen offset:3072 sc1 lds\n\t"
    "v_exp_f32 v134, v134\n\t"
    "v_exp_f32 v135, v135\n\t"
    "v_exp_f32 v136, v136\n\t"
    "v_smfmac_f32_16x16x64_bf16 v[116:119], v[120:123], v[176:183], v124\n\t"
    "ds_read_b128 v[216:219], %[rb] offset:63488\n\t"
    "v_exp_f32 v137, v137\n\t"
    "v_exp_f32 v138, v138\n\t"
    "v_smfmac_f32_32x32x32_bf16 v[96:111], v[220:223], v[184:191], v124\n\t"
    "s_waitcnt lgkmcnt(3)\n\t"
    "v_exp_f32 v139, v139\n\t"
    "v_exp_f32 v140, v140\n\t"
    "v_exp_f32 v141, v141\n\t"
    "v_smfmac_f32_16x16x64_bf16 v[116:119], v[120:123], v[184:191], v124\n\t"
    "ds_read_b128 v[220:223], %[rb] offset:64512\n\t"
    "v_exp_f32 v142, v142\n\t"
    "v_exp_f32 v143, v143\n\t"
    "v_mfma_f32_32x32x16_bf16 v[160:175], v[200:203], v[32:35], v[48:63]\n\t"
    "v_exp_f32 v144, v144\n\t"
    "v_exp_f32 v145, v145\n\t"
    "v_exp_f32 v146, v146\n\t"
    "v_mfma_f32_32x32x16_bf16 v[160:175], v[204:207], v[36:39], v[160:175]\n\t"
    "v_exp_f32 v147, v147\n\t"
    "v_exp_f32 v148, v148\n\t"
    "v_exp_f32 v149, v149\n\t"
    "v_smfmac_f32_32x32x32_bf16 v[80:95], v[208:211], v[128:135], v124\n\t"
    "s_waitcnt vmcnt(25) lgkmcnt(0)\n\t"
    "s_barrier\n\t"
    "v_exp_f32 v150, v150\n\t"
    "v_exp_f32 v151, v151\n\t"
    "v_exp_f32 v152, v152\n\t"
    "v_smfmac_f32_16x16x64_bf16 v[112:115], v[120:123], v[128:135], v124\n\t"
    "v_exp_f32 v153, v153\n\t"
    "v_exp_f32 v154, v154\n\t"
    "v_smfmac_f32_32x32x32_bf16 v[80:95], v[212:215], v[136:143], v124\n\t"
    "v_exp_f32 v155, v155\n\t"
    "v_exp_f32 v156, v156\n\t"
    "v_exp_f32 v157, v157\n\t"
    "v_smfmac_f32_16x16x64_bf16 v[112:115], v[120:123], v[136:143], v124\n\t"
    "v_exp_f32 v158, v158\n\t"
    "v_exp_f32 v159, v159\n\t"
    "v_mfma_f32_32x32x16_bf16 v[176:191], v[200:203], v[40:43], v[64:79]\n\t"
    "ds_read_b64_tr_b16 a[240:241], v228 offset:49152\n\t"
    "ds_read_b64_tr_b16 a[242:243], v228 offset:51200\n\t"
    "ds_read_b64_tr_b16 a[244:245], v229 offset:49152\n\t"
    "ds_read_b64_tr_b16 a[246:247], v229 offset:51200\n\t"
    "v_exp_f32 v160, v160\n\t"
    "v_exp_f32 v161, v161\n\t"
    "v_exp_f32 v162, v162\n\t"
    "v_mfma_f32_32x32x16_bf16 v[176:191], v[204:207], v[44:47], v[176:191]\n\t"
    "s_add_u32 m0, s59, 32768\n\t"
    "s_mov_b32 s58, 4218880\n\t"
    "s_nop 0\n\t"
    "buffer_load_dwordx4 v244, s[48:51], s58 offen offset:0 sc1 lds\n\t"
    "v_exp_f32 v163, v163\n\t"
    "v_exp_f32 v164, v164\n\t"
    "v_exp_f32 v165, v165\n\t"
    "v_smfmac_f32_32x32x32_bf16 v[96:111], v[208:211], v[144:151], v124\n\t"
    "buffer_load_dwordx4 v244, s[48:51], s58 offen offset:1024 sc1 lds\n\t"
    "v_exp_f32 v166, v166\n\t"
    "v_exp_f32 v167, v167\n\t"
    "v_exp_f32 v168, v168\n\t"
    "v_smfmac_f32_16x16x64_bf16 v[116:119], v[120:123], v[144:151], v124\n\t"
    "v_exp_f32 v169, v169\n\t"
    "v_exp_f32 v170, v170\n\t"
    "v_smfmac_f32_32x32x32_bf16 v[96:111], v[212:215], v[152:159], v124\n\t"
    "buffer_load_dwordx4 v245, s[48:51], s58 offen offset:2048 sc1 lds\n\t"
    "v_exp_f32 v171, v171\n\t"
    "v_exp_f32 v172, v172\n\t"
    "v_exp_f32 v173, v173\n\t"
    "v_smfmac_f32_16x16x64_bf16 v[116:119], v[120:123], v[152:159], v124\n\t"
    "buffer_load_dwordx4 v245, s[48:51], s58 offen offset:3072 sc1 lds\n\t"
    "v_exp_f32 v174, v174\n\t"
    "v_exp_f32 v175, v175\n\t"
    "ds_read_b64_tr_b16 a[248:249], v228 offset:53248\n\t"
    "ds_read_b64_tr_b16 a[250:251], v228 offset:55296\n\t"
    "ds_read_b64_tr_b16 a[252:253], v229 offset:53248\n\t"
    "ds_read_b64_tr_b16 a[254:255], v229 offset:55296\n\t"
    "v_exp_f32 v176, v176\n\t"
    "v_exp_f32 v177, v177\n\t"
    "v_exp_f32 v178, v178\n\t"
    "s_add_u32 m0, s59, 36864\n\t"
    "s_mov_b32 s58, 4218880\n\t"
    "s_nop 0\n\t"
    "buffer_load_dwordx4 v246, s[48:51], s58 offen offset:0 sc1 lds\n\t"
    "v_exp_f32 v179, v179\n\t"
    "v_exp_f32 v180, v180\n\t"
    "v_exp_f32 v181, v181\n\t"
    "v_smfmac_f32_32x32x32_bf16 v[80:95], v[216:219], v[160:167], v124\n\t"
    "buffer_load_dwordx4 v246, s[48:51], s58 offen offset:1024 sc1 lds\n\t"
    "v_exp_f32 v182, v182\n\t"
    "v_exp_f32 v183, v183\n\t"
    "v_exp_f32 v184, v184\n\t"
    "v_smfmac_f32_16x16x64_bf16 v[112:115], v[120:123], v[160:167], v124\n\t"
    "v_exp_f32 v185, v185\n\t"
    "v_exp_f32 v186, v186\n\t"
    "v_smfmac_f32_32x32x32_bf16 v[80:95], v[220:223], v[168:175], v124\n\t"
    "buffer_load_dwordx4 v247, s[48:51], s58 offen offset:2048 sc1 lds\n\t"
    "v_exp_f32 v187, v187\n\t"
    "v_exp_f32 v188, v188\n\t"
    "v_exp_f32 v189, v189\n\t"
    "v_smfmac_f32_16x16x64_bf16 v[112:115], v[120:123], v[168:175], v124\n\t"
    "buffer_load_dwordx4 v247, s[48:51], s58 offen offset:3072 sc1 lds\n\t"
    "v_exp_f32 v190, v190\n\t"
    "v_exp_f32 v191, v191\n\t"
    "v_smfmac_f32_32x32x32_bf16 v[96:111], v[216:219], v[176:183], v124\n\t"
    "v_smfmac_f32_16x16x64_bf16 v[116:119], v[120:123], v[176:183], v124\n\t"
    "v_smfmac_f32_32x32x32_bf16 v[96:111], v[220:223], v[184:191], v124\n\t"
    "v_smfmac_f32_16x16x64_bf16 v[116:119], v[120:123], v[184:191], v124\n\t"
    "s_waitcnt vmcnt(25) lgkmcnt(0)\n\t"
    "s_barrier\n\t"
    "s_mov_b32 s56, 0\n\t"
    "FIN_%=:\n"
    "v_rcp_f32 v125, v112\n\t"
    "s_nop 3\n\t"
    "v_rcp_f32 v126, v116\n\t"
    "v_mul_f32 v80, v80, v125\n\t"
    "v_mul_f32 v81, v81, v125\n\t"
    "v_mul_f32 v82, v82, v125\n\t"
    "v_mul_f32 v83, v83, v125\n\t"
    "v_mul_f32 v88, v88, v125\n\t"
    "v_mul_f32 v89, v89, v125\n\t"
    "v_mul_f32 v90, v90, v125\n\t"
    "v_mul_f32 v91, v91, v125\n\t"
    "v_cvt_pk_bf16_f32 v160, v80, v81\n\t"
    "v_cvt_pk_bf16_f32 v161, v82, v83\n\t"
    "v_cvt_pk_bf16_f32 v162, v88, v89\n\t"
    "v_cvt_pk_bf16_f32 v163, v90, v91\n\t"
    "s_mov_b32 s52, 0\n\t"
    "buffer_store_dwordx4 v[160:163], %[vos], %[sws], s52 offen offset:0 sc1\n\t"
    "v_mul_f32 v84, v84, v125\n\t"
    "v_mul_f32 v85, v85, v125\n\t"
    "v_mul_f32 v86, v86, v125\n\t"
    "v_mul_f32 v87, v87, v125\n\t"
    "v_mul_f32 v92, v92, v125\n\t"
    "v_mul_f32 v93, v93, v125\n\t"
    "v_mul_f32 v94, v94, v125\n\t"
    "v_mul_f32 v95, v95, v125\n\t"
    "v_cvt_pk_bf16_f32 v164, v84, v85\n\t"
    "v_cvt_pk_bf16_f32 v165, v86, v87\n\t"
    "v_cvt_pk_bf16_f32 v166, v92, v93\n\t"
    "v_cvt_pk_bf16_f32 v167, v94, v95\n\t"
    "buffer_store_dwordx4 v[164:167], %[vos], %[sws], s52 offen offset:512 sc1\n\t"
    "v_mul_f32 v96, v96, v126\n\t"
    "v_mul_f32 v97, v97, v126\n\t"
    "v_mul_f32 v98, v98, v126\n\t"
    "v_mul_f32 v99, v99, v126\n\t"
    "v_mul_f32 v104, v104, v126\n\t"
    "v_mul_f32 v105, v105, v126\n\t"
    "v_mul_f32 v106, v106, v126\n\t"
    "v_mul_f32 v107, v107, v126\n\t"
    "v_cvt_pk_bf16_f32 v168, v96, v97\n\t"
    "v_cvt_pk_bf16_f32 v169, v98, v99\n\t"
    "v_cvt_pk_bf16_f32 v170, v104, v105\n\t"
    "v_cvt_pk_bf16_f32 v171, v106, v107\n\t"
    "s_mov_b32 s52, 65536\n\t"
    "buffer_store_dwordx4 v[168:171], %[vos], %[sws], s52 offen offset:0 sc1\n\t"
    "v_mul_f32 v100, v100, v126\n\t"
    "v_mul_f32 v101, v101, v126\n\t"
    "v_mul_f32 v102, v102, v126\n\t"
    "v_mul_f32 v103, v103, v126\n\t"
    "v_mul_f32 v108, v108, v126\n\t"
    "v_mul_f32 v109, v109, v126\n\t"
    "v_mul_f32 v110, v110, v126\n\t"
    "v_mul_f32 v111, v111, v126\n\t"
    "v_cvt_pk_bf16_f32 v172, v100, v101\n\t"
    "v_cvt_pk_bf16_f32 v173, v102, v103\n\t"
    "v_cvt_pk_bf16_f32 v174, v108, v109\n\t"
    "v_cvt_pk_bf16_f32 v175, v110, v111\n\t"
    "buffer_store_dwordx4 v[172:175], %[vos], %[sws], s52 offen offset:512 sc1\n\t"
    "; ---- guard: row sums <= 2^110 and every accumulator finite, else the rows are redone against their true maxima\n\t"
    "v_mul_f32 v125, 0, v80\n\t"
    "v_mul_f32 v126, 0, v81\n\t"
    "v_fma_f32 v125, v82, 0, v125\n\t"
    "v_fma_f32 v126, v83, 0, v126\n\t"
    "v_fma_f32 v125, v84, 0, v125\n\t"
    "v_fma_f32 v126, v85, 0, v126\n\t"
    "v_fma_f32 v125, v86, 0, v125\n\t"
    "v_fma_f32 v126, v87, 0, v126\n\t"
    "v_fma_f32 v125, v88, 0, v125\n\t"
    "v_fma_f32 v126, v89, 0, v126\n\t"
    "v_fma_f32 v125, v90, 0, v125\n\t"
    "v_fma_f32 v126, v91, 0, v126\n\t"
    "v_fma_f32 v125, v92, 0, v125\n\t"
    "v_fma_f32 v126, v93, 0, v126\n\t"
    "v_fma_f32 v125, v94, 0, v125\n\t"
    "v_fma_f32 v126, v95, 0, v126\n\t"
    "v_fma_f32 v125, v96, 0, v125\n\t"
    "v_fma_f32 v126, v97, 0, v126\n\t"
    "v_fma_f32 v125, v98, 0, v125\n\t"
    "v_fma_f32 v126, v99, 0, v126\n\t"
    "v_fma_f32 v125, v100, 0, v125\n\t"
    "v_fma_f32 v126, v101, 0, v126\n\t"
    "v_fma_f32 v125, v102, 0, v125\n\t"
    "v_fma_f32 v126, v103, 0, v126\n\t"
    "v_fma_f32 v125, v104, 0, v125\n\t"
    "v_fma_f32 v126, v105, 0, v126\n\t"
    "v_fma_f32 v125, v106, 0, v125\n\t"
    "v_fma_f32 v126, v107, 0, v126\n\t"
    "v_fma_f32 v125, v108, 0, v125\n\t"
    "v_fma_f32 v126, v109, 0, v126\n\t"
    "v_fma_f32 v125, v110, 0, v125\n\t"
    "v_fma_f32 v126, v111, 0, v126\n\t"
    "v_add_f32 v125, v125, v126\n\t"
    "v_add_f32 v126, v112, v116\n\t"
    "v_add_f32 v125, v125, v126\n\t"
    "s_mov_b32 s60, 0x76800000\n\t"
    "v_cmp_nle_f32 vcc, v125, s60\n\t"
    "s_cmp_lg_u32 s56, 0\n\t"
    "s_cbranch_scc1 GO3_%=\n\t"
    "s_nop 2\n\t"
    "s_cbranch_vccnz SLOW_%=\n\t"
    "GO3_%=:\n"
    "v_add_u32 v236, 65536, %[ord]\n\t"
    "v_add_u32 v237, 131072, %[ord]\n\t"
    "v_mov_b32 v235, %[ord]\n\t"
    "v_add_u32 v238, s59, %[rb]\n\t"
    "v_add_u32 v238, 131072, v238\n\t"
    "v_xor_b32 v239, 16384, v238\n\t"
    "; ---- output projection: branch B, then branch A\n\t"
    "ds_read_b128 v[96:99], v237 offset:0\n\t"
    "ds_read_b128 v[100:103], v237 offset:4096\n\t"
    "ds_read_b128 v[104:107], v237 offset:8192\n\t"
    "ds_read_b128 v[108:111], v237 offset:12288\n\t"
    "ds_read_b128 v[112:115], v237 offset:1024\n\t"
    "ds_read_b128 v[116:119], v237 offset:5120\n\t"
    "ds_read_b128 v[120:123], v237 offset:9216\n\t"
    "ds_read_b128 v[124:127], v237 offset:13312\n\t"
    "s_waitcnt lgkmcnt(0)\n\t"
    "v_mfma_f32_16x16x32_bf16 v[192:195], a[0:3], v[96:99], 0\n\t"
    "ds_read_b128 v[128:131], v237 offset:2048\n\t"
    "v_mfma_f32_16x16x32_bf16 v[196:199], a[4:7], v[96:99], 0\n\t"
    "ds_read_b128 v[132:135], v237 offset:6144\n\t"
    "v_mfma_f32_16x16x32_bf16 v[200:203], a[0:3], v[100:103], 0\n\t"
    "ds_read_b128 v[136:139], v237 offset:10240\n\t"
    "v_mfma_f32_16x16x32_bf16 v[204:207], a[4:7], v[100:103], 0\n\t"
    "ds_read_b128 v[140:143], v237 offset:14336\n\t"
    "v_mfma_f32_16x16x32_bf16 v[208:211], a[0:3], v[104:107], 0\n\t"
    "ds_read_b128 v[144:147], v237 offset:3072\n\t"
    "v_mfma_f32_16x16x32_bf16 v[212:215], a[4:7], v[104:107], 0\n\t"
    "ds_read_b128 v[148:151], v237 offset:7168\n\t"
    "v_mfma_f32_16x16x32_bf16 v[216:219], a[0:3], v[108:111], 0\n\t"
    "ds_read_b128 v[152:155], v237 offset:11264\n\t"
    "v_mfma_f32_16x16x32_bf16 v[220:223], a[4:7], v[108:111], 0\n\t"
    "ds_read_b128 v[156:159], v237 offset:15360\n\t"
    "s_add_u32 m0, s59, 98304\n\t"
    "s_mov_b32 s58, 4194304\n\t"
    "v_xor_b32 v244, 0x20000, v244\n\t"
    "v_xor_b32 v245, 0x20000, v245\n\t"
    "v_xor_b32 v246, 0x20000, v246\n\t"
    "v_xor_b32 v247, 0x20000, v247\n\t"
    "v_mfma_f32_16x16x32_bf16 v[192:195], a[8:11], v[112:115], v[192:195]\n\t"
    "buffer_load_dwordx4 v244, s[48:51], s58 offen offset:0 sc1 lds\n\t"
    "v_mfma_f32_16x16x32_bf16 v[196:199], a[12:15], v[112:115], v[196:199]\n\t"
    "v_mfma_f32_16x16x32_bf16 v[200:203], a[8:11], v[116:119], v[200:203]\n\t"
    "buffer_load_dwordx4 v244, s[48:51], s58 offen offset:1024 sc1 lds\n\t"
    "v_mfma_f32_16x16x32_bf16 v[204:207], a[12:15], v[116:119], v[204:207]\n\t"
    "v_mfma_f32_16x16x32_bf16 v[208:211], a[8:11], v[120:123], v[208:211]\n\t"
    "buffer_load_dwordx4 v245, s[48:51], s58 offen offset:2048 sc1 lds\n\t"
    "v_mfma_f32_16x16x32_bf16 v[212:215], a[12:15], v[120:123], v[212:215]\n\t"
    "v_mfma_f32_16x16x32_bf16 v[216:219], a[8:11], v[124:127], v[216:219]\n\t"
    "buffer_load_dwordx4 v245, s[48:51], s58 offen offset:3072 sc1 lds\n\t"
    "v_mfma_f32_16x16x32_bf16 v[220:223], a[12:15], v[124:127], v[220:223]\n\t"
    "s_waitcnt lgkmcnt(0)\n\t"
    "s_waitcnt vmcnt(4)\n\t"
    "s_barrier\n\t"
    "s_cmp_lg_u32 %[wid], 0\n\t"
    "s_cbranch_scc1 NOPUB_%=\n\t"
    "s_mov_b64 exec, 1\n\t"
    "global_store_dword %[vf2], %[tag], %[pf] sc1\n\t"
    "s_mov_b64 exec, -1\n\t"
    "NOPUB_%=:\n"
    "v_mfma_f32_16x16x32_bf16 v[192:195], a[16:19], v[128:131], v[192:195]\n\t"
    "ds_read_b128 v[96:99], v235 offset:0\n\t"
    "v_mfma_f32_16x16x32_bf16 v[196:199], a[20:23], v[128:131], v[196:199]\n\t"
    "ds_read_b128 v[100:103], v235 offset:4096\n\t"
    "v_mfma_f32_16x16x32_bf16 v[200:203], a[16:19], v[132:135], v[200:203]\n\t"
    "ds_read_b128 v[104:107], v235 offset:8192\n\t"
    "v_mfma_f32_16x16x32_bf16 v[204:207], a[20:23], v[132:135], v[204:207]\n\t"
    "ds_read_b128 v[108:111], v235 offset:12288\n\t"
    "v_mfma_f32_16x16x32_bf16 v[208:211], a[16:19], v[136:139], v[208:211]\n\t"
    "ds_read_b128 v[112:115], v235 offset:1024\n\t"
    "v_mfma_f32_16x16x32_bf16 v[212:215], a[20:23], v[136:139], v[212:215]\n\t"
    "ds_read_b128 v[116:119], v235 offset:5120\n\t"
    "v_mfma_f32_16x16x32_bf16 v[216:219], a[16:19], v[140:143], v[216:219]\n\t"
    "ds_read_b128 v[120:123], v235 offset:9216\n\t"
    "v_mfma_f32_16x16x32_bf16 v[220:223], a[20:23], v[140:143], v[220:223]\n\t"
    "ds_read_b128 v[124:127], v235 offset:13312\n\t"
    "s_add_u32 m0, s59, 102400\n\t"
    "s_mov_b32 s58, 4194304\n\t"
    "v_mfma_f32_16x16x32_bf16 v[192:195], a[24:27], v[144:147], v[192:195]\n\t"
    "buffer_load_dwordx4 v246, s[48:51], s58 offen offset:0 sc1 lds\n\t"
    "v_mfma_f32_16x16x32_bf16 v[196:199], a[28:31], v[144:147], v[196:199]\n\t"
    "v_mfma_f32_16x16x32_bf16 v[200:203], a[24:27], v[148:151], v[200:203]\n\t"
    "buffer_load_dwordx4 v246, s[48:51], s58 offen offset:1024 sc1 lds\n\t"
    "v_mfma_f32_16x16x32_bf16 v[204:207], a[28:31], v[148:151], v[204:207]\n\t"
    "v_mfma_f32_16x16x32_bf16 v[208:211], a[24:27], v[152:155], v[208:211]\n\t"
    "buffer_load_dwordx4 v247, s[48:51], s58 offen offset:2048 sc1 lds\n\t"
    "v_mfma_f32_16x16x32_bf16 v[212:215], a[28:31], v[152:155], v[212:215]\n\t"
    "v_mfma_f32_16x16x32_bf16 v[216:219], a[24:27], v[156:159], v[216:219]\n\t"
    "buffer_load_dwordx4 v247, s[48:51], s58 offen offset:3072 sc1 lds\n\t"
    "v_mfma_f32_16x16x32_bf16 v[220:223], a[28:31], v[156:159], v[220:223]\n\t"
    "s_waitcnt lgkmcnt(0)\n\t"
    "v_mfma_f32_16x16x32_bf16 v[192:195], a[32:35], v[96:99], v[192:195]\n\t"
    "ds_read_b128 v[128:131], v235 offset:2048\n\t"
    "v_mfma_f32_16x16x32_bf16 v[196:199], a[36:39], v[96:99], v[196:199]\n\t"
    "ds_read_b128 v[132:135], v235 offset:6144\n\t"
    "v_mfma_f32_16x16x32_bf16 v[200:203], a[32:35], v[100:103], v[200:203]\n\t"
    "ds_read_b128 v[136:139], v235 offset:10240\n\t"
    "v_mfma_f32_16x16x32_bf16 v[204:207], a[36:39], v[100:103], v[204:207]\n\t"
    "ds_read_b128 v[140:143], v235 offset:14336\n\t"
    "v_mfma_f32_16x16x32_bf16 v[208:211], a[32:35], v[104:107], v[208:211]\n\t"
    "ds_read_b128 v[144:147], v235 offset:3072\n\t"
    "v_mfma_f32_16x16x32_bf16 v[212:215], a[36:39], v[104:107], v[212:215]\n\t"
    "ds_read_b128 v[148:151], v235 offset:7168\n\t"
    "v_mfma_f32_16x16x32_bf16 v[216:219], a[32:35], v[108:111], v[216:219]\n\t"
    "ds_read_b128 v[152:155], v235 offset:11264\n\t"
    "v_mfma_f32_16x16x32_bf16 v[220:223], a[36:39], v[108:111], v[220:223]\n\t"
    "ds_read_b128 v[156:159], v235 offset:15360\n\t"
    "v_mfma_f32_16x16x32_bf16 v[192:195], a[40:43], v[112:115], v[192:195]\n\t"
    "v_mfma_f32_16x16x32_bf16 v[196:199], a[44:47], v[112:115], v[196:199]\n\t"
    "v_mfma_f32_16x16x32_bf16 v[200:203], a[40:43], v[116:119], v[200:203]\n\t"
    "v_mfma_f32_16x16x32_bf16 v[204:207], a[44:47], v[116:119], v[204:207]\n\t"
    "v_mfma_f32_16x16x32_bf16 v[208:211], a[40:43], v[120:123], v[208:211]\n\t"
    "v_mfma_f32_16x16x32_bf16 v[212:215], a[44:47], v[120:123], v[212:215]\n\t"
    "v_mfma_f32_16x16x32_bf16 v[216:219], a[40:43], v[124:127], v[216:219]\n\t"
    "v_mfma_f32_16x16x32_bf16 v[220:223], a[44:47], v[124:127], v[220:223]\n\t"
    "s_waitcnt lgkmcnt(0)\n\t"
    "s_barrier\n\t"
    "v_mfma_f32_16x16x32_bf16 v[192:195], a[48:51], v[128:131], v[192:195]\n\t"
    "ds_read_b128 v[96:99], v236 offset:0\n\t"
    "v_mfma_f32_16x16x32_bf16 v[196:199], a[52:55], v[128:131], v[196:199]\n\t"
    "ds_read_b128 v[100:103], v236 offset:4096\n\t"
    "v_mfma_f32_16x16x32_bf16 v[200:203], a[48:51], v[132:135], v[200:203]\n\t"
    "ds_read_b128 v[104:107], v236 offset:8192\n\t"
    "v_mfma_f32_16x16x32_bf16 v[204:207], a[52:55], v[132:135], v[204:207]\n\t"
    "ds_read_b128 v[108:111], v236 offset:12288\n\t"
    "v_mfma_f32_16x16x32_bf16 v[208:211], a[48:51], v[136:139], v[208:211]\n\t"
    "ds_read_b128 v[112:115], v236 offset:1024\n\t"
    "v_mfma_f32_16x16x32_bf16 v[212:215], a[52:55], v[136:139], v[212:215]\n\t"
    "ds_read_b128 v[116:119], v236 offset:5120\n\t"
    "v_mfma_f32_16x16x32_bf16 v[216:219], a[48:51], v[140:143], v[216:219]\n\t"
    "ds_read_b128 v[120:123], v236 offset:9216\n\t"
    "v_mfma_f32_16x16x32_bf16 v[220:223], a[52:55], v[140:143], v[220:223]\n\t"
    "ds_read_b128 v[124:127], v236 offset:13312\n\t"
    "s_add_u32 m0, s59, 0\n\t"
    "s_mov_b32 s58, 4202496\n\t"
    "v_mfma_f32_16x16x32_bf16 v[192:195], a[56:59], v[144:147], v[192:195]\n\t"
    "buffer_load_dwordx4 v244, s[48:51], s58 offen offset:0 sc1 lds\n\t"
    "v_mfma_f32_16x16x32_bf16 v[196:199], a[60:63], v[144:147], v[196:199]\n\t"
    "v_mfma_f32_16x16x32_bf16 v[200:203], a[56:59], v[148:151], v[200:203]\n\t"
    "buffer_load_dwordx4 v244, s[48:51], s58 offen offset:1024 sc1 lds\n\t"
    "v_mfma_f32_16x16x32_bf16 v[204:207], a[60:63], v[148:151], v[204:207]\n\t"
    "v_mfma_f32_16x16x32_bf16 v[208:211], a[56:59], v[152:155], v[208:211]\n\t"
    "buffer_load_dwordx4 v245, s[48:51], s58 offen offset:2048 sc1 lds\n\t"
    "v_mfma_f32_16x16x32_bf16 v[212:215], a[60:63], v[152:155], v[212:215]\n\t"
    "v_mfma_f32_16x16x32_bf16 v[216:219], a[56:59], v[156:159], v[216:219]\n\t"
    "buffer_load_dwordx4 v245, s[48:51], s58 offen offset:3072 sc1 lds\n\t"
    "v_mfma_f32_16x16x32_bf16 v[220:223], a[60:63], v[156:159], v[220:223]\n\t"
    "s_waitcnt lgkmcnt(0)\n\t"
    "v_mfma_f32_16x16x32_bf16 v[192:195], a[64:67], v[96:99], v[192:195]\n\t"
    "ds_read_b128 v[128:131], v236 offset:2048\n\t"
    "v_mfma_f32_16x16x32_bf16 v[196:199], a[68:71], v[96:99], v[196:199]\n\t"
    "ds_read_b128 v[132:135], v236 offset:6144\n\t"
    "v_mfma_f32_16x16x32_bf16 v[200:203], a[64:67], v[100:103], v[200:203]\n\t"
    "ds_read_b128 v[136:139], v236 offset:10240\n\t"
    "v_mfma_f32_16x16x32_bf16 v[204:207], a[68:71], v[100:103], v[204:207]\n\t"
    "ds_read_b128 v[140:143], v236 offset:14336\n\t"
    "v_mfma_f32_16x16x32_bf16 v[208:211], a[64:67], v[104:107], v[208:211]\n\t"
    "ds_read_b128 v[144:147], v236 offset:3072\n\t"
    "v_mfma_f32_16x16x32_bf16 v[212:215], a[68:71], v[104:107], v[212:215]\n\t"
    "ds_read_b128 v[148:151], v236 offset:7168\n\t"
    "v_mfma_f32_16x16x32_bf16 v[216:219], a[64:67], v[108:111], v[216:219]\n\t"
    "ds_read_b128 v[152:155], v236 offset:11264\n\t"
    "v_mfma_f32_16x16x32_bf16 v[220:223], a[68:71], v[108:111], v[220:223]\n\t"
    "ds_read_b128 v[156:159], v236 offset:15360\n\t"
    "s_add_u32 m0, s59, 4096\n\t"
    "s_mov_b32 s58, 4202496\n\t"
    "v_mfma_f32_16x16x32_bf16 v[192:195], a[72:75], v[112:115], v[192:195]\n\t"
    "buffer_load_dwordx4 v246, s[48:51], s58 offen offset:0 sc1 lds\n\t"
    "v_mfma_f32_16x16x32_bf16 v[196:199], a[76:79], v[112:115], v[196:199]\n\t"
    "v_mfma_f32_16x16x32_bf16 v[200:203], a[72:75], v[116:119], v[200:203]\n\t"
    "buffer_load_dwordx4 v246, s[48:51], s58 offen offset:1024 sc1 lds\n\t"
    "v_mfma_f32_16x16x32_bf16 v[204:207], a[76:79], v[116:119], v[204:207]\n\t"
    "v_mfma_f32_16x16x32_bf16 v[208:211], a[72:75], v[120:123], v[208:211]\n\t"
    "buffer_load_dwordx4 v247, s[48:51], s58 offen offset:2048 sc1 lds\n\t"
    "v_mfma_f32_16x16x32_bf16 v[212:215], a[76:79], v[120:123], v[212:215]\n\t"
    "v_mfma_f32_16x16x32_bf16 v[216:219], a[72:75], v[124:127], v[216:219]\n\t"
    "buffer_load_dwordx4 v247, s[48:51], s58 offen offset:3072 sc1 lds\n\t"
    "v_mfma_f32_16x16x32_bf16 v[220:223], a[76:79], v[124:127], v[220:223]\n\t"
    "s_waitcnt lgkmcnt(0)\n\t"
    "s_barrier\n\t"
    "v_mfma_f32_16x16x32_bf16 v[192:195], a[80:83], v[128:131], v[192:195]\n\t"
    "ds_read_b128 v[96:99], v235 offset:32768\n\t"
    "v_mfma_f32_16x16x32_bf16 v[196:199], a[84:87], v[128:131], v[196:199]\n\t"
    "ds_read_b128 v[100:103], v235 offset:36864\n\t"
    "v_mfma_f32_16x16x32_bf16 v[200:203], a[80:83], v[132:135], v[200:203]\n\t"
    "ds_read_b128 v[104:107], v235 offset:40960\n\t"
    "v_mfma_f32_16x16x32_bf16 v[204:207], a[84:87], v[132:135], v[204:207]\n\t"
    "ds_read_b128 v[108:111], v235 offset:45056\n\t"
    "v_mfma_f32_16x16x32_bf16 v[208:211], a[80:83], v[136:139], v[208:211]\n\t"
    "ds_read_b128 v[112:115], v235 offset:33792\n\t"
    "v_mfma_f32_16x16x32_bf16 v[212:215], a[84:87], v[136:139], v[212:215]\n\t"
    "ds_read_b128 v[116:119], v235 offset:37888\n\t"
    "v_mfma_f32_16x16x32_bf16 v[216:219], a[80:83], v[140:143], v[216:219]\n\t"
    "ds_read_b128 v[120:123], v235 offset:41984\n\t"
    "v_mfma_f32_16x16x32_bf16 v[220:223], a[84:87], v[140:143], v[220:223]\n\t"
    "ds_read_b128 v[124:127], v235 offset:46080\n\t"
    "s_add_u32 m0, s59, 65536\n\t"
    "s_mov_b32 s58, 4210688\n\t"
    "v_mfma_f32_16x16x32_bf16 v[192:195], a[88:91], v[144:147], v[192:195]\n\t"
    "buffer_load_dwordx4 v244, s[48:51], s58 offen offset:0 sc1 lds\n\t"
    "v_mfma_f32_16x16x32_bf16 v[196:199], a[92:95], v[144:147], v[196:199]\n\t"
    "v_mfma_f32_16x16x32_bf16 v[200:203], a[88:91], v[148:151], v[200:203]\n\t"
    "buffer_load_dwordx4 v244, s[48:51], s58 offen offset:1024 sc1 lds\n\t"
    "v_mfma_f32_16x16x32_bf16 v[204:207], a[92:95], v[148:151], v[204:207]\n\t"
    "v_mfma_f32_16x16x32_bf16 v[208:211], a[88:91], v[152:155], v[208:211]\n\t"
    "buffer_load_dwordx4 v245, s[48:51], s58 offen offset:2048 sc1 lds\n\t"
    "v_mfma_f32_16x16x32_bf16 v[212:215], a[92:95], v[152:155], v[212:215]\n\t"
    "v_mfma_f32_16x16x32_bf16 v[216:219], a[88:91], v[156:159], v[216:219]\n\t"
    "buffer_load_dwordx4 v245, s[48:51], s58 offen offset:3072 sc1 lds\n\t"
    "v_mfma_f32_16x16x32_bf16 v[220:223], a[92:95], v[156:159], v[220:223]\n\t"
    "s_waitcnt lgkmcnt(0)\n\t"
    "v_mfma_f32_16x16x32_bf16 v[192:195], a[96:99], v[96:99], v[192:195]\n\t"
    "ds_read_b128 v[128:131], v235 offset:34816\n\t"
    "v_mfma_f32_16x16x32_bf16 v[196:199], a[100:103], v[96:99], v[196:199]\n\t"
    "ds_read_b128 v[132:135], v235 offset:38912\n\t"
    "v_mfma_f32_16x16x32_bf16 v[200:203], a[96:99], v[100:103], v[200:203]\n\t"
    "ds_read_b128 v[136:139], v235 offset:43008\n\t"
    "v_mfma_f32_16x16x32_bf16 v[204:207], a[100:103], v[100:103], v[204:207]\n\t"
    "ds_read_b128 v[140:143], v235 offset:47104\n\t"
    "v_mfma_f32_16x16x32_bf16 v[208:211], a[96:99], v[104:107], v[208:211]\n\t"
    "ds_read_b128 v[144:147], v235 offset:35840\n\t"
    "v_mfma_f32_16x16x32_bf16 v[212:215], a[100:103], v[104:107], v[212:215]\n\t"
    "ds_read_b128 v[148:151], v235 offset:39936\n\t"
    "v_mfma_f32_16x16x32_bf16 v[216:219], a[96:99], v[108:111], v[216:219]\n\t"
    "ds_read_b128 v[152:155], v235 offset:44032\n\t"
    "v_mfma_f32_16x16x32_bf16 v[220:223], a[100:103], v[108:111], v[220:223]\n\t"
    "ds_read_b128 v[156:159], v235 offset:48128\n\t"
    "s_add_u32 m0, s59, 69632\n\t"
    "s_mov_b32 s58, 4210688\n\t"
    "v_mfma_f32_16x16x32_bf16 v[192:195], a[104:107], v[112:115], v[192:195]\n\t"
    "buffer_load_dwordx4 v246, s[48:51], s58 offen offset:0 sc1 lds\n\t"
    "v_mfma_f32_16x16x32_bf16 v[196:199], a[108:111], v[112:115], v[196:199]\n\t"
    "v_mfma_f32_16x16x32_bf16 v[200:203], a[104:107], v[116:119], v[200:203]\n\t"
    "buffer_load_dwordx4 v246, s[48:51], s58 offen offset:1024 sc1 lds\n\t"
    "v_mfma_f32_16x16x32_bf16 v[204:207], a[108:111], v[116:119], v[204:207]\n\t"
    "v_mfma_f32_16x16x32_bf16 v[208:211], a[104:107], v[120:123], v[208:211]\n\t"
    "buffer_load_dwordx4 v247, s[48:51], s58 offen offset:2048 sc1 lds\n\t"
    "v_mfma_f32_16x16x32_bf16 v[212:215], a[108:111], v[120:123], v[212:215]\n\t"
    "v_mfma_f32_16x16x32_bf16 v[216:219], a[104:107], v[124:127], v[216:219]\n\t"
    "buffer_load_dwordx4 v247, s[48:51], s58 offen offset:3072 sc1 lds\n\t"
    "v_mfma_f32_16x16x32_bf16 v[220:223], a[108:111], v[124:127], v[220:223]\n\t"
    "s_waitcnt lgkmcnt(0)\n\t"
    "s_waitcnt vmcnt(16)\n\t"
    "s_barrier\n\t"
    "v_mfma_f32_16x16x32_bf16 v[192:195], a[112:115], v[128:131], v[192:195]\n\t"
    "ds_read_b128 v[96:99], v236 offset:32768\n\t"
    "v_mfma_f32_16x16x32_bf16 v[196:199], a[116:119], v[128:131], v[196:199]\n\t"
    "ds_read_b128 v[100:103], v236 offset:36864\n\t"
    "v_mfma_f32_16x16x32_bf16 v[200:203], a[112:115], v[132:135], v[200:203]\n\t"
    "ds_read_b128 v[104:107], v236 offset:40960\n\t"
    "v_mfma_f32_16x16x32_bf16 v[204:207], a[116:119], v[132:135], v[204:207]\n\t"
    "ds_read_b128 v[108:111], v236 offset:45056\n\t"
    "v_mfma_f32_16x16x32_bf16 v[208:211], a[112:115], v[136:139], v[208:211]\n\t"
    "ds_read_b128 v[112:115], v236 offset:33792\n\t"
    "v_mfma_f32_16x16x32_bf16 v[212:215], a[116:119], v[136:139], v[212:215]\n\t"
    "ds_read_b128 v[116:119], v236 offset:37888\n\t"
    "v_mfma_f32_16x16x32_bf16 v[216:219], a[112:115], v[140:143], v[216:219]\n\t"
    "ds_read_b128 v[120:123], v236 offset:41984\n\t"
    "v_mfma_f32_16x16x32_bf16 v[220:223], a[116:119], v[140:143], v[220:223]\n\t"
    "ds_read_b128 v[124:127], v236 offset:46080\n\t"
    "s_add_u32 m0, s59, 32768\n\t"
    "s_mov_b32 s58, 4218880\n\t"
    "v_mfma_f32_16x16x32_bf16 v[192:195], a[120:123], v[144:147], v[192:195]\n\t"
    "buffer_load_dwordx4 v244, s[48:51], s58 offen offset:0 sc1 lds\n\t"
    "v_mfma_f32_16x16x32_bf16 v[196:199], a[124:127], v[144:147], v[196:199]\n\t"
    "v_mfma_f32_16x16x32_bf16 v[200:203], a[120:123], v[148:151], v[200:203]\n\t"
    "buffer_load_dwordx4 v244, s[48:51], s58 offen offset:1024 sc1 lds\n\t"
    "v_mfma_f32_16x16x32_bf16 v[204:207], a[124:127], v[148:151], v[204:207]\n\t"
    "v_mfma_f32_16x16x32_bf16 v[208:211], a[120:123], v[152:155], v[208:211]\n\t"
    "buffer_load_dwordx4 v245, s[48:51], s58 offen offset:2048 sc1 lds\n\t"
    "v_mfma_f32_16x16x32_bf16 v[212:215], a[124:127], v[152:155], v[212:215]\n\t"
    "v_mfma_f32_16x16x32_bf16 v[216:219], a[120:123], v[156:159], v[216:219]\n\t"
    "buffer_load_dwordx4 v245, s[48:51], s58 offen offset:3072 sc1 lds\n\t"
    "v_mfma_f32_16x16x32_bf16 v[220:223], a[124:127], v[156:159], v[220:223]\n\t"
    "global_load_dword v232, %[vp2], %[pf] sc1\n\t"
    "s_waitcnt lgkmcnt(0)\n\t"
    "v_mfma_f32_16x16x32_bf16 v[160:163], a[0:3], v[96:99], 0\n\t"
    "ds_read_b128 v[128:131], v236 offset:34816\n\t"
    "v_mfma_f32_16x16x32_bf16 v[164:167], a[4:7], v[96:99], 0\n\t"
    "ds_read_b128 v[132:135], v236 offset:38912\n\t"
    "v_mfma_f32_16x16x32_bf16 v[168:171], a[0:3], v[100:103], 0\n\t"
    "ds_read_b128 v[136:139], v236 offset:43008\n\t"
    "v_mfma_f32_16x16x32_bf16 v[172:175], a[4:7], v[100:103], 0\n\t"
    "ds_read_b128 v[140:143], v236 offset:47104\n\t"
    "v_mfma_f32_16x16x32_bf16 v[176:179], a[0:3], v[104:107], 0\n\t"
    "ds_read_b128 v[144:147], v236 offset:35840\n\t"
    "v_mfma_f32_16x16x32_bf16 v[180:183], a[4:7], v[104:107], 0\n\t"
    "ds_read_b128 v[148:151], v236 offset:39936\n\t"
    "v_mfma_f32_16x16x32_bf16 v[184:187], a[0:3], v[108:111], 0\n\t"
    "ds_read_b128 v[152:155], v236 offset:44032\n\t"
    "v_mfma_f32_16x16x32_bf16 v[188:191], a[4:7], v[108:111], 0\n\t"
    "ds_read_b128 v[156:159], v236 offset:48128\n\t"
    "s_add_u32 m0, s59, 36864\n\t"
    "s_mov_b32 s58, 4218880\n\t"
    "v_mfma_f32_16x16x32_bf16 v[160:163], a[8:11], v[112:115], v[160:163]\n\t"
    "buffer_load_dwordx4 v246, s[48:51], s58 offen offset:0 sc1 lds\n\t"
    "v_mfma_f32_16x16x32_bf16 v[164:167], a[12:15], v[112:115], v[164:167]\n\t"
    "ds_write_b128 v238, v[192:195] offset:0\n\t"
    "v_mfma_f32_16x16x32_bf16 v[168:171], a[8:11], v[116:119], v[168:171]\n\t"
    "buffer_load_dwordx4 v246, s[48:51], s58 offen offset:1024 sc1 lds\n\t"
    "v_mfma_f32_16x16x32_bf16 v[172:175], a[12:15], v[116:119], v[172:175]\n\t"
    "v_mfma_f32_16x16x32_bf16 v[176:179], a[8:11], v[120:123], v[176:179]\n\t"
    "buffer_load_dwordx4 v247, s[48:51], s58 offen offset:2048 sc1 lds\n\t"
    "v_mfma_f32_16x16x32_bf16 v[180:183], a[12:15], v[120:123], v[180:183]\n\t"
    "ds_write_b128 v238, v[196:199] offset:1024\n\t"
    "v_mfma_f32_16x16x32_bf16 v[184:187], a[8:11], v[124:127], v[184:187]\n\t"
    "buffer_load_dwordx4 v247, s[48:51], s58 offen offset:3072 sc1 lds\n\t"
    "v_mfma_f32_16x16x32_bf16 v[188:191], a[12:15], v[124:127], v[188:191]\n\t"
    "s_waitcnt lgkmcnt(2)\n\t"
    "s_waitcnt vmcnt(4)\n\t"
    "s_mov_b32 s56, 0x40000\n\t"
    "W2P_%=:\n"
    "v_cmp_ne_u32 vcc, v232, %[tag]\n\t"
    "s_nop 4\n\t"
    "s_cbranch_vccz W2S_%=\n\t"
    "s_sleep 4\n\t"
    "global_load_dword v232, %[vp2], %[pf] sc1\n\t"
    "s_sub_u32 s56, s56, 1\n\t"
    "s_cmp_lg_u32 s56, 0\n\t"
    "s_waitcnt vmcnt(0)\n\t"
    "s_cbranch_scc1 W2P_%=\n\t"
    "W2S_%=:\n"
    "s_barrier\n\t"
    "v_mfma_f32_16x16x32_bf16 v[160:163], a[16:19], v[128:131], v[160:163]\n\t"
    "ds_read_b128 v[96:99], v235 offset:0\n\t"
    "v_mfma_f32_16x16x32_bf16 v[164:167], a[20:23], v[128:131], v[164:167]\n\t"
    "ds_read_b128 v[100:103], v235 offset:4096\n\t"
    "v_mfma_f32_16x16x32_bf16 v[168:171], a[16:19], v[132:135], v[168:171]\n\t"
    "ds_read_b128 v[104:107], v235 offset:8192\n\t"
    "v_mfma_f32_16x16x32_bf16 v[172:175], a[20:23], v[132:135], v[172:175]\n\t"
    "ds_read_b128 v[108:111], v235 offset:12288\n\t"
    "v_mfma_f32_16x16x32_bf16 v[176:179], a[16:19], v[136:139], v[176:179]\n\t"
    "ds_read_b128 v[112:115], v235 offset:1024\n\t"
    "v_mfma_f32_16x16x32_bf16 v[180:183], a[20:23], v[136:139], v[180:183]\n\t"
    "ds_read_b128 v[116:119], v235 offset:5120\n\t"
    "v_mfma_f32_16x16x32_bf16 v[184:187], a[16:19], v[140:143], v[184:187]\n\t"
    "ds_read_b128 v[120:123], v235 offset:9216\n\t"
    "v_mfma_f32_16x16x32_bf16 v[188:191], a[20:23], v[140:143], v[188:191]\n\t"
    "ds_read_b128 v[124:127], v235 offset:13312\n\t"
    "s_add_u32 m0, s59, 98304\n\t"
    "s_mov_b32 s58, 0\n\t"
    "v_xor_b32 v244, 0x20000, v244\n\t"
    "v_xor_b32 v245, 0x20000, v245\n\t"
    "v_xor_b32 v246, 0x20000, v246\n\t"
    "v_xor_b32 v247, 0x20000, v247\n\t"
    "v_mfma_f32_16x16x32_bf16 v[160:163], a[24:27], v[144:147], v[160:163]\n\t"
    "buffer_load_dwordx4 v244, s[48:51], s58 offen offset:0 sc1 lds\n\t"
    "v_mfma_f32_16x16x32_bf16 v[164:167], a[28:31], v[144:147], v[164:167]\n\t"
    "ds_write_b128 v238, v[200:203] offset:2048\n\t"
    "v_mfma_f32_16x16x32_bf16 v[168:171], a[24:27], v[148:151], v[168:171]\n\t"
    "buffer_load_dwordx4 v244, s[48:51], s58 offen offset:1024 sc1 lds\n\t"
    "v_mfma_f32_16x16x32_bf16 v[172:175], a[28:31], v[148:151], v[172:175]\n\t"
    "v_mfma_f32_16x16x32_bf16 v[176:179], a[24:27], v[152:155], v[176:179]\n\t"
    "buffer_load_dwordx4 v245, s[48:51], s58 offen offset:2048 sc1 lds\n\t"
    "v_mfma_f32_16x16x32_bf16 v[180:183], a[28:31], v[152:155], v[180:183]\n\t"
    "ds_write_b128 v238, v[204:207] offset:3072\n\t"
    "v_mfma_f32_16x16x32_bf16 v[184:187], a[24:27], v[156:159], v[184:187]\n\t"
    "buffer_load_dwordx4 v245, s[48:51], s58 offen offset:3072 sc1 lds\n\t"
    "v_mfma_f32_16x16x32_bf16 v[188:191], a[28:31], v[156:159], v[188:191]\n\t"
    "s_waitcnt lgkmcnt(2)\n\t"
    "v_mfma_f32_16x16x32_bf16 v[160:163], a[32:35], v[96:99], v[160:163]\n\t"
    "ds_read_b128 v[128:131], v235 offset:2048\n\t"
    "v_mfma_f32_16x16x32_bf16 v[164:167], a[36:39], v[96:99], v[164:167]\n\t"
    "ds_read_b128 v[132:135], v235 offset:6144\n\t"
    "v_mfma_f32_16x16x32_bf16 v[168:171], a[32:35], v[100:103], v[168:171]\n\t"
    "ds_read_b128 v[136:139], v235 offset:10240\n\t"
    "v_mfma_f32_16x16x32_bf16 v[172:175], a[36:39], v[100:103], v[172:175]\n\t"
    "ds_read_b128 v[140:143], v235 offset:14336\n\t"
    "v_mfma_f32_16x16x32_bf16 v[176:179], a[32:35], v[104:107], v[176:179]\n\t"
    "ds_read_b128 v[144:147], v235 offset:3072\n\t"
    "v_mfma_f32_16x16x32_bf16 v[180:183], a[36:39], v[104:107], v[180:183]\n\t"
    "ds_read_b128 v[148:151], v235 offset:7168\n\t"
    "v_mfma_f32_16x16x32_bf16 v[184:187], a[32:35], v[108:111], v[184:187]\n\t"
    "ds_read_b128 v[152:155], v235 offset:11264\n\t"
    "v_mfma_f32_16x16x32_bf16 v[188:191], a[36:39], v[108:111], v[188:191]\n\t"
    "ds_read_b128 v[156:159], v235 offset:15360\n\t"
    "s_add_u32 m0, s59, 102400\n\t"
    "s_mov_b32 s58, 0\n\t"
    "v_mfma_f32_16x16x32_bf16 v[160:163], a[40:43], v[112:115], v[160:163]\n\t"
    "buffer_load_dwordx4 v246, s[48:51], s58 offen offset:0 sc1 lds\n\t"
    "v_mfma_f32_16x16x32_bf16 v[164:167], a[44:47], v[112:115], v[164:167]\n\t"
    "ds_write_b128 v238, v[208:211] offset:4096\n\t"
    "v_mfma_f32_16x16x32_bf16 v[168:171], a[40:43], v[116:119], v[168:171]\n\t"
    "buffer_load_dwordx4 v246, s[48:51], s58 offen offset:1024 sc1 lds\n\t"
    "v_mfma_f32_16x16x32_bf16 v[172:175], a[44:47], v[116:119], v[172:175]\n\t"
    "v_mfma_f32_16x16x32_bf16 v[176:179], a[40:43], v[120:123], v[176:179]\n\t"
    "buffer_load_dwordx4 v247, s[48:51], s58 offen offset:2048 sc1 lds\n\t"
    "v_mfma_f32_16x16x32_bf16 v[180:183], a[44:47], v[120:123], v[180:183]\n\t"
    "ds_write_b128 v238, v[212:215] offset:5120\n\t"
    "v_mfma_f32_16x16x32_bf16 v[184:187], a[40:43], v[124:127], v[184:187]\n\t"
    "buffer_load_dwordx4 v247, s[48:51], s58 offen offset:3072 sc1 lds\n\t"
    "v_mfma_f32_16x16x32_bf16 v[188:191], a[44:47], v[124:127], v[188:191]\n\t"
    "s_waitcnt lgkmcnt(2)\n\t"
    "s_barrier\n\t"
    "v_mfma_f32_16x16x32_bf16 v[160:163], a[48:51], v[128:131], v[160:163]\n\t"
    "ds_read_b128 v[96:99], v236 offset:0\n\t"
    "v_mfma_f32_16x16x32_bf16 v[164:167], a[52:55], v[128:131], v[164:167]\n\t"
    "ds_read_b128 v[100:103], v236 offset:4096\n\t"
    "v_mfma_f32_16x16x32_bf16 v[168:171], a[48:51], v[132:135], v[168:171]\n\t"
    "ds_read_b128 v[104:107], v236 offset:8192\n\t"
    "v_mfma_f32_16x16x32_bf16 v[172:175], a[52:55], v[132:135], v[172:175]\n\t"
    "ds_read_b128 v[108:111], v236 offset:12288\n\t"
    "v_mfma_f32_16x16x32_bf16 v[176:179], a[48:51], v[136:139], v[176:179]\n\t"
    "ds_read_b128 v[112:115], v236 offset:1024\n\t"
    "v_mfma_f32_16x16x32_bf16 v[180:183], a[52:55], v[136:139], v[180:183]\n\t"
    "ds_read_b128 v[116:119], v236 offset:5120\n\t"
    "v_mfma_f32_16x16x32_bf16 v[184:187], a[48:51], v[140:143], v[184:187]\n\t"
    "ds_read_b128 v[120:123], v236 offset:9216\n\t"
    "v_mfma_f32_16x16x32_bf16 v[188:191], a[52:55], v[140:143], v[188:191]\n\t"
    "ds_read_b128 v[124:127], v236 offset:13312\n\t"
    "s_add_u32 m0, s59, 0\n\t"
    "s_mov_b32 s58, 8192\n\t"
    "v_mfma_f32_16x16x32_bf16 v[160:163], a[56:59], v[144:147], v[160:163]\n\t"
    "buffer_load_dwordx4 v244, s[48:51], s58 offen offset:0 sc1 lds\n\t"
    "v_mfma_f32_16x16x32_bf16 v[164:167], a[60:63], v[144:147], v[164:167]\n\t"
    "ds_write_b128 v238, v[216:219] offset:6144\n\t"
    "v_mfma_f32_16x16x32_bf16 v[168:171], a[56:59], v[148:151], v[168:171]\n\t"
    "buffer_load_dwordx4 v244, s[48:51], s58 offen offset:1024 sc1 lds\n\t"
    "v_mfma_f32_16x16x32_bf16 v[172:175], a[60:63], v[148:151], v[172:175]\n\t"
    "v_mfma_f32_16x16x32_bf16 v[176:179], a[56:59], v[152:155], v[176:179]\n\t"
    "buffer_load_dwordx4 v245, s[48:51], s58 offen offset:2048 sc1 lds\n\t"
    "v_mfma_f32_16x16x32_bf16 v[180:183], a[60:63], v[152:155], v[180:183]\n\t"
    "ds_write_b128 v238, v[220:223] offset:7168\n\t"
    "v_mfma_f32_16x16x32_bf16 v[184:187], a[56:59], v[156:159], v[184:187]\n\t"
    "buffer_load_dwordx4 v245, s[48:51], s58 offen offset:3072 sc1 lds\n\t"
    "v_mfma_f32_16x16x32_bf16 v[188:191], a[60:63], v[156:159], v[188:191]\n\t"
    "s_waitcnt lgkmcnt(2)\n\t"
    "v_mfma_f32_16x16x32_bf16 v[160:163], a[64:67], v[96:99], v[160:163]\n\t"
    "ds_read_b128 v[128:131], v236 offset:2048\n\t"
    "v_mfma_f32_16x16x32_bf16 v[164:167], a[68:71], v[96:99], v[164:167]\n\t"
    "ds_read_b128 v[132:135], v236 offset:6144\n\t"
    "v_mfma_f32_16x16x32_bf16 v[168:171], a[64:67], v[100:103], v[168:171]\n\t"
    "ds_read_b128 v[136:139], v236 offset:10240\n\t"
    "v_mfma_f32_16x16x32_bf16 v[172:175], a[68:71], v[100:103], v[172:175]\n\t"
    "ds_read_b128 v[140:143], v236 offset:14336\n\t"
    "v_mfma_f32_16x16x32_bf16 v[176:179], a[64:67], v[104:107], v[176:179]\n\t"
    "ds_read_b128 v[144:147], v236 offset:3072\n\t"
    "v_mfma_f32_16x16x32_bf16 v[180:183], a[68:71], v[104:107], v[180:183]\n\t"
    "ds_read_b128 v[148:151], v236 offset:7168\n\t"
    "v_mfma_f32_16x16x32_bf16 v[184:187], a[64:67], v[108:111], v[184:187]\n\t"
    "ds_read_b128 v[152:155], v236 offset:11264\n\t"
    "v_mfma_f32_16x16x32_bf16 v[188:191], a[68:71], v[108:111], v[188:191]\n\t"
    "ds_read_b128 v[156:159], v236 offset:15360\n\t"
    "s_add_u32 m0, s59, 4096\n\t"
    "s_mov_b32 s58, 8192\n\t"
    "v_mfma_f32_16x16x32_bf16 v[160:163], a[72:75], v[112:115], v[160:163]\n\t"
    "buffer_load_dwordx4 v246, s[48:51], s58 offen offset:0 sc1 lds\n\t"
    "v_mfma_f32_16x16x32_bf16 v[164:167], a[76:79], v[112:115], v[164:167]\n\t"
    "v_mfma_f32_16x16x32_bf16 v[168:171], a[72:75], v[116:119], v[168:171]\n\t"
    "buffer_load_dwordx4 v246, s[48:51], s58 offen offset:1024 sc1 lds\n\t"
    "v_mfma_f32_16x16x32_bf16 v[172:175], a[76:79], v[116:119], v[172:175]\n\t"
    "v_mfma_f32_16x16x32_bf16 v[176:179], a[72:75], v[120:123], v[176:179]\n\t"
    "buffer_load_dwordx4 v247, s[48:51], s58 offen offset:2048 sc1 lds\n\t"
    "v_mfma_f32_16x16x32_bf16 v[180:183], a[76:79], v[120:123], v[180:183]\n\t"
    "v_mfma_f32_16x16x32_bf16 v[184:187], a[72:75], v[124:127], v[184:187]\n\t"
    "buffer_load_dwordx4 v247, s[48:51], s58 offen offset:3072 sc1 lds\n\t"
    "v_mfma_f32_16x16x32_bf16 v[188:191], a[76:79], v[124:127], v[188:191]\n\t"
    "s_waitcnt lgkmcnt(0)\n\t"
    "s_waitcnt vmcnt(16)\n\t"
    "s_barrier\n\t"
    "v_mfma_f32_16x16x32_bf16 v[160:163], a[80:83], v[128:131], v[160:163]\n\t"
    "ds_read_b128 v[96:99], v235 offset:32768\n\t"
    "v_mfma_f32_16x16x32_bf16 v[164:167], a[84:87], v[128:131], v[164:167]\n\t"
    "ds_read_b128 v[100:103], v235 offset:36864\n\t"
    "v_mfma_f32_16x16x32_bf16 v[168:171], a[80:83], v[132:135], v[168:171]\n\t"
    "ds_read_b128 v[104:107], v235 offset:40960\n\t"
    "v_mfma_f32_16x16x32_bf16 v[172:175], a[84:87], v[132:135], v[172:175]\n\t"
    "ds_read_b128 v[108:111], v235 offset:45056\n\t"
    "v_mfma_f32_16x16x32_bf16 v[176:179], a[80:83], v[136:139], v[176:179]\n\t"
    "ds_read_b128 v[112:115], v235 offset:33792\n\t"
    "v_mfma_f32_16x16x32_bf16 v[180:183], a[84:87], v[136:139], v[180:183]\n\t"
    "ds_read_b128 v[116:119], v235 offset:37888\n\t"
    "v_mfma_f32_16x16x32_bf16 v[184:187], a[80:83], v[140:143], v[184:187]\n\t"
    "ds_read_b128 v[120:123], v235 offset:41984\n\t"
    "v_mfma_f32_16x16x32_bf16 v[188:191], a[84:87], v[140:143], v[188:191]\n\t"
    "ds_read_b128 v[124:127], v235 offset:46080\n\t"
    "s_add_u32 m0, s59, 65536\n\t"
    "s_mov_b32 s58, 16384\n\t"
    "v_mfma_f32_16x16x32_bf16 v[160:163], a[88:91], v[144:147], v[160:163]\n\t"
    "buffer_load_dwordx4 v244, s[48:51], s58 offen offset:0 sc1 lds\n\t"
    "v_mfma_f32_16x16x32_bf16 v[164:167], a[92:95], v[144:147], v[164:167]\n\t"
    "ds_read_b128 v[192:195], v239 offset:0\n\t"
    "v_mfma_f32_16x16x32_bf16 v[168:171], a[88:91], v[148:151], v[168:171]\n\t"
    "buffer_load_dwordx4 v244, s[48:51], s58 offen offset:1024 sc1 lds\n\t"
    "v_mfma_f32_16x16x32_bf16 v[172:175], a[92:95], v[148:151], v[172:175]\n\t"
    "ds_read_b128 v[196:199], v239 offset:1024\n\t"
    "v_mfma_f32_16x16x32_bf16 v[176:179], a[88:91], v[152:155], v[176:179]\n\t"
    "buffer_load_dwordx4 v245, s[48:51], s58 offen offset:2048 sc1 lds\n\t"
    "v_mfma_f32_16x16x32_bf16 v[180:183], a[92:95], v[152:155], v[180:183]\n\t"
    "ds_read_b128 v[200:203], v239 offset:2048\n\t"
    "v_mfma_f32_16x16x32_bf16 v[184:187], a[88:91], v[156:159], v[184:187]\n\t"
    "buffer_load_dwordx4 v245, s[48:51], s58 offen offset:3072 sc1 lds\n\t"
    "v_mfma_f32_16x16x32_bf16 v[188:191], a[92:95], v[156:159], v[188:191]\n\t"
    "ds_read_b128 v[204:207], v239 offset:3072\n\t"
    "s_waitcnt lgkmcnt(4)\n\t"
    "v_mfma_f32_16x16x32_bf16 v[160:163], a[96:99], v[96:99], v[160:163]\n\t"
    "ds_read_b128 v[128:131], v235 offset:34816\n\t"
    "v_mfma_f32_16x16x32_bf16 v[164:167], a[100:103], v[96:99], v[164:167]\n\t"
    "ds_read_b128 v[132:135], v235 offset:38912\n\t"
    "v_mfma_f32_16x16x32_bf16 v[168:171], a[96:99], v[100:103], v[168:171]\n\t"
    "ds_read_b128 v[136:139], v235 offset:43008\n\t"
    "v_mfma_f32_16x16x32_bf16 v[172:175], a[100:103], v[100:103], v[172:175]\n\t"
    "ds_read_b128 v[140:143], v235 offset:47104\n\t"
    "v_mfma_f32_16x16x32_bf16 v[176:179], a[96:99], v[104:107], v[176:179]\n\t"
    "ds_read_b128 v[144:147], v235 offset:35840\n\t"
    "v_mfma_f32_16x16x32_bf16 v[180:183], a[100:103], v[104:107], v[180:183]\n\t"
    "ds_read_b128 v[148:151], v235 offset:39936\n\t"
    "v_mfma_f32_16x16x32_bf16 v[184:187], a[96:99], v[108:111], v[184:187]\n\t"
    "ds_read_b128 v[152:155], v235 offset:44032\n\t"
    "v_mfma_f32_16x16x32_bf16 v[188:191], a[100:103], v[108:111], v[188:191]\n\t"
    "ds_read_b128 v[156:159], v235 offset:48128\n\t"
    "s_add_u32 m0, s59, 69632\n\t"
    "s_mov_b32 s58, 16384\n\t"
    "v_mfma_f32_16x16x32_bf16 v[160:163], a[104:107], v[112:115], v[160:163]\n\t"
    "buffer_load_dwordx4 v246, s[48:51], s58 offen offset:0 sc1 lds\n\t"
    "v_mfma_f32_16x16x32_bf16 v[164:167], a[108:111], v[112:115], v[164:167]\n\t"
    "ds_read_b128 v[208:211], v239 offset:4096\n\t"
    "v_mfma_f32_16x16x32_bf16 v[168:171], a[104:107], v[116:119], v[168:171]\n\t"
    "buffer_load_dwordx4 v246, s[48:51], s58 offen offset:1024 sc1 lds\n\t"
    "v_mfma_f32_16x16x32_bf16 v[172:175], a[108:111], v[116:119], v[172:175]\n\t"
    "ds_read_b128 v[212:215], v239 offset:5120\n\t"
    "v_mfma_f32_16x16x32_bf16 v[176:179], a[104:107], v[120:123], v[176:179]\n\t"
    "buffer_load_dwordx4 v247, s[48:51], s58 offen offset:2048 sc1 lds\n\t"
    "v_mfma_f32_16x16x32_bf16 v[180:183], a[108:111], v[120:123], v[180:183]\n\t"
    "ds_read_b128 v[216:219], v239 offset:6144\n\t"
    "v_mfma_f32_16x16x32_bf16 v[184:187], a[104:107], v[124:127], v[184:187]\n\t"
    "buffer_load_dwordx4 v247, s[48:51], s58 offen offset:3072 sc1 lds\n\t"
    "v_mfma_f32_16x16x32_bf16 v[188:191], a[108:111], v[124:127], v[188:191]\n\t"
    "ds_read_b128 v[220:223], v239 offset:7168\n\t"
    "s_waitcnt lgkmcnt(4)\n\t"
    "s_waitcnt vmcnt(16)\n\t"
    "s_barrier\n\t"
    "v_mfma_f32_16x16x32_bf16 v[160:163], a[112:115], v[128:131], v[160:163]\n\t"
    "ds_read_b128 v[96:99], v236 offset:32768\n\t"
    "v_mfma_f32_16x16x32_bf16 v[164:167], a[116:119], v[128:131], v[164:167]\n\t"
    "ds_read_b128 v[100:103], v236 offset:36864\n\t"
    "v_mfma_f32_16x16x32_bf16 v[168:171], a[112:115], v[132:135], v[168:171]\n\t"
    "ds_read_b128 v[104:107], v236 offset:40960\n\t"
    "v_mfma_f32_16x16x32_bf16 v[172:175], a[116:119], v[132:135], v[172:175]\n\t"
    "ds_read_b128 v[108:111], v236 offset:45056\n\t"
    "v_mfma_f32_16x16x32_bf16 v[176:179], a[112:115], v[136:139], v[176:179]\n\t"
    "ds_read_b128 v[112:115], v236 offset:33792\n\t"
    "v_mfma_f32_16x16x32_bf16 v[180:183], a[116:119], v[136:139], v[180:183]\n\t"
    "ds_read_b128 v[116:119], v236 offset:37888\n\t"
    "v_mfma_f32_16x16x32_bf16 v[184:187], a[112:115], v[140:143], v[184:187]\n\t"
    "ds_read_b128 v[120:123], v236 offset:41984\n\t"
    "v_mfma_f32_16x16x32_bf16 v[188:191], a[116:119], v[140:143], v[188:191]\n\t"
    "ds_read_b128 v[124:127], v236 offset:46080\n\t"
    "s_add_u32 m0, s59, 32768\n\t"
    "s_mov_b32 s58, 24576\n\t"
    "v_mfma_f32_16x16x32_bf16 v[160:163], a[120:123], v[144:147], v[160:163]\n\t"
    "buffer_load_dwordx4 v244, s[48:51], s58 offen offset:0 sc1 lds\n\t"
    "v_mfma_f32_16x16x32_bf16 v[164:167], a[124:127], v[144:147], v[164:167]\n\t"
    "v_mfma_f32_16x16x32_bf16 v[168:171], a[120:123], v[148:151], v[168:171]\n\t"
    "buffer_load_dwordx4 v244, s[48:51], s58 offen offset:1024 sc1 lds\n\t"
    "v_mfma_f32_16x16x32_bf16 v[172:175], a[124:127], v[148:151], v[172:175]\n\t"
    "v_mfma_f32_16x16x32_bf16 v[176:179], a[120:123], v[152:155], v[176:179]\n\t"
    "buffer_load_dwordx4 v245, s[48:51], s58 offen offset:2048 sc1 lds\n\t"
    "v_mfma_f32_16x16x32_bf16 v[180:183], a[124:127], v[152:155], v[180:183]\n\t"
    "v_mfma_f32_16x16x32_bf16 v[184:187], a[120:123], v[156:159], v[184:187]\n\t"
    "buffer_load_dwordx4 v245, s[48:51], s58 offen offset:3072 sc1 lds\n\t"
    "v_mfma_f32_16x16x32_bf16 v[188:191], a[124:127], v[156:159], v[188:191]\n\t"
    "s_waitcnt lgkmcnt(0)\n\t"
    "v_mfma_f32_16x16x32_bf16 v[64:67], a[128:131], v[96:99], 0\n\t"
    "ds_read_b128 v[128:131], v236 offset:34816\n\t"
    "v_mfma_f32_16x16x32_bf16 v[68:71], a[132:135], v[96:99], 0\n\t"
    "ds_read_b128 v[132:135], v236 offset:38912\n\t"
    "v_mfma_f32_16x16x32_bf16 v[72:75], a[128:131], v[100:103], 0\n\t"
    "ds_read_b128 v[136:139], v236 offset:43008\n\t"
    "v_mfma_f32_16x16x32_bf16 v[76:79], a[132:135], v[100:103], 0\n\t"
    "ds_read_b128 v[140:143], v236 offset:47104\n\t"
    "v_mfma_f32_16x16x32_bf16 v[80:83], a[128:131], v[104:107], 0\n\t"
    "ds_read_b128 v[144:147], v236 offset:35840\n\t"
    "v_mfma_f32_16x16x32_bf16 v[84:87], a[132:135], v[104:107], 0\n\t"
    "ds_read_b128 v[148:151], v236 offset:39936\n\t"
    "v_mfma_f32_16x16x32_bf16 v[88:91], a[128:131], v[108:111], 0\n\t"
    "ds_read_b128 v[152:155], v236 offset:44032\n\t"
    "v_mfma_f32_16x16x32_bf16 v[92:95], a[132:135], v[108:111], 0\n\t"
    "ds_read_b128 v[156:159], v236 offset:48128\n\t"
    "s_add_u32 m0, s59, 36864\n\t"
    "s_mov_b32 s58, 24576\n\t"
    "v_mfma_f32_16x16x32_bf16 v[64:67], a[136:139], v[112:115], v[64:67]\n\t"
    "buffer_load_dwordx4 v246, s[48:51], s58 offen offset:0 sc1 lds\n\t"
    "v_mfma_f32_16x16x32_bf16 v[68:71], a[140:143], v[112:115], v[68:71]\n\t"
    "v_add_f32 v160, v160, v192\n\t"
    "v_add_f32 v161, v161, v193\n\t"
    "v_mfma_f32_16x16x32_bf16 v[72:75], a[136:139], v[116:119], v[72:75]\n\t"
    "buffer_load_dwordx4 v246, s[48:51], s58 offen offset:1024 sc1 lds\n\t"
    "v_mfma_f32_16x16x32_bf16 v[76:79], a[140:143], v[116:119], v[76:79]\n\t"
    "v_add_f32 v162, v162, v194\n\t"
    "v_add_f32 v163, v163, v195\n\t"
    "v_mfma_f32_16x16x32_bf16 v[80:83], a[136:139], v[120:123], v[80:83]\n\t"
    "buffer_load_dwordx4 v247, s[48:51], s58 offen offset:2048 sc1 lds\n\t"
    "v_mfma_f32_16x16x32_bf16 v[84:87], a[140:143], v[120:123], v[84:87]\n\t"
    "v_add_f32 v164, v164, v196\n\t"
    "v_add_f32 v165, v165, v197\n\t"
    "v_mfma_f32_16x16x32_bf16 v[88:91], a[136:139], v[124:127], v[88:91]\n\t"
    "buffer_load_dwordx4 v247, s[48:51], s58 offen offset:3072 sc1 lds\n\t"
    "v_mfma_f32_16x16x32_bf16 v[92:95], a[140:143], v[124:127], v[92:95]\n\t"
    "v_add_f32 v166, v166, v198\n\t"
    "v_add_f32 v167, v167, v199\n\t"
    "s_waitcnt lgkmcnt(0)\n\t"
    "s_waitcnt vmcnt(16)\n\t"
    "s_barrier\n\t"
    "v_mfma_f32_16x16x32_bf16 v[64:67], a[144:147], v[128:131], v[64:67]\n\t"
    "ds_read_b128 v[96:99], v235 offset:0\n\t"
    "v_mfma_f32_16x16x32_bf16 v[68:71], a[148:151], v[128:131], v[68:71]\n\t"
    "ds_read_b128 v[100:103], v235 offset:4096\n\t"
    "v_mfma_f32_16x16x32_bf16 v[72:75], a[144:147], v[132:135], v[72:75]\n\t"
    "ds_read_b128 v[104:107], v235 offset:8192\n\t"
    "v_mfma_f32_16x16x32_bf16 v[76:79], a[148:151], v[132:135], v[76:79]\n\t"
    "ds_read_b128 v[108:111], v235 offset:12288\n\t"
    "v_mfma_f32_16x16x32_bf16 v[80:83], a[144:147], v[136:139], v[80:83]\n\t"
    "ds_read_b128 v[112:115], v235 offset:1024\n\t"
    "v_mfma_f32_16x16x32_bf16 v[84:87], a[148:151], v[136:139], v[84:87]\n\t"
    "ds_read_b128 v[116:119], v235 offset:5120\n\t"
    "v_mfma_f32_16x16x32_bf16 v[88:91], a[144:147], v[140:143], v[88:91]\n\t"
    "ds_read_b128 v[120:123], v235 offset:9216\n\t"
    "v_mfma_f32_16x16x32_bf16 v[92:95], a[148:151], v[140:143], v[92:95]\n\t"
    "ds_read_b128 v[124:127], v235 offset:13312\n\t"
    "s_add_u32 m0, s59, 98304\n\t"
    "s_mov_b32 s58, 0\n\t"
    "v_xor_b32 v244, 0x20000, v244\n\t"
    "v_xor_b32 v245, 0x20000, v245\n\t"
    "v_xor_b32 v246, 0x20000, v246\n\t"
    "v_xor_b32 v247, 0x20000, v247\n\t"
    "v_mfma_f32_16x16x32_bf16 v[64:67], a[152:155], v[144:147], v[64:67]\n\t"
    "buffer_load_dwordx4 v244, s[48:51], s58 offen offset:0 sc1 lds\n\t"
    "v_mfma_f32_16x16x32_bf16 v[68:71], a[156:159], v[144:147], v[68:71]\n\t"
    "v_cvt_pk_bf16_f32 v160, v160, v161\n\t"
    "v_cvt_pk_bf16_f32 v161, v162, v163\n\t"
    "v_mfma_f32_16x16x32_bf16 v[72:75], a[152:155], v[148:151], v[72:75]\n\t"
    "buffer_load_dwordx4 v244, s[48:51], s58 offen offset:1024 sc1 lds\n\t"
    "v_mfma_f32_16x16x32_bf16 v[76:79], a[156:159], v[148:151], v[76:79]\n\t"
    "v_cvt_pk_bf16_f32 v162, v164, v165\n\t"
    "v_cvt_pk_bf16_f32 v163, v166, v167\n\t"
    "v_mfma_f32_16x16x32_bf16 v[80:83], a[152:155], v[152:155], v[80:83]\n\t"
    "buffer_load_dwordx4 v245, s[48:51], s58 offen offset:2048 sc1 lds\n\t"
    "v_mfma_f32_16x16x32_bf16 v[84:87], a[156:159], v[152:155], v[84:87]\n\t"
    "s_mov_b32 s52, 2048\n\t"
    "v_mfma_f32_16x16x32_bf16 v[88:91], a[152:155], v[156:159], v[88:91]\n\t"
    "buffer_load_dwordx4 v245, s[48:51], s58 offen offset:3072 sc1 lds\n\t"
    "v_mfma_f32_16x16x32_bf16 v[92:95], a[156:159], v[156:159], v[92:95]\n\t"
    "v_permlane16_swap_b32 v160, v162\n\t"
    "s_waitcnt lgkmcnt(0)\n\t"
    "v_mfma_f32_16x16x32_bf16 v[64:67], a[160:163], v[96:99], v[64:67]\n\t"
    "ds_read_b128 v[128:131], v235 offset:2048\n\t"
    "v_mfma_f32_16x16x32_bf16 v[68:71], a[164:167], v[96:99], v[68:71]\n\t"
    "ds_read_b128 v[132:135], v235 offset:6144\n\t"
    "v_mfma_f32_16x16x32_bf16 v[72:75], a[160:163], v[100:103], v[72:75]\n\t"
    "ds_read_b128 v[136:139], v235 offset:10240\n\t"
    "v_mfma_f32_16x16x32_bf16 v[76:79], a[164:167], v[100:103], v[76:79]\n\t"
    "ds_read_b128 v[140:143], v235 offset:14336\n\t"
    "v_mfma_f32_16x16x32_bf16 v[80:83], a[160:163], v[104:107], v[80:83]\n\t"
    "ds_read_b128 v[144:147], v235 offset:3072\n\t"
    "v_mfma_f32_16x16x32_bf16 v[84:87], a[164:167], v[104:107], v[84:87]\n\t"
    "ds_read_b128 v[148:151], v235 offset:7168\n\t"
    "v_mfma_f32_16x16x32_bf16 v[88:91], a[160:163], v[108:111], v[88:91]\n\t"
    "ds_read_b128 v[152:155], v235 offset:11264\n\t"
    "v_mfma_f32_16x16x32_bf16 v[92:95], a[164:167], v[108:111], v[92:95]\n\t"
    "ds_read_b128 v[156:159], v235 offset:15360\n\t"
    "s_add_u32 m0, s59, 102400\n\t"
    "s_mov_b32 s58, 0\n\t"
    "v_mfma_f32_16x16x32_bf16 v[64:67], a[168:171], v[112:115], v[64:67]\n\t"
    "buffer_load_dwordx4 v246, s[48:51], s58 offen offset:0 sc1 lds\n\t"
    "v_mfma_f32_16x16x32_bf16 v[68:71], a[172:175], v[112:115], v[68:71]\n\t"
    "v_permlane16_swap_b32 v161, v163\n\t"
    "v_mfma_f32_16x16x32_bf16 v[72:75], a[168:171], v[116:119], v[72:75]\n\t"
    "buffer_load_dwordx4 v246, s[48:51], s58 offen offset:1024 sc1 lds\n\t"
    "v_mfma_f32_16x16x32_bf16 v[76:79], a[172:175], v[116:119], v[76:79]\n\t"
    "buffer_store_dwordx4 v[160:163], %[vout], %[so], s52 offen sc1\n\t"
    "v_mfma_f32_16x16x32_bf16 v[80:83], a[168:171], v[120:123], v[80:83]\n\t"
    "buffer_load_dwordx4 v247, s[48:51], s58 offen offset:2048 sc1 lds\n\t"
    "v_mfma_f32_16x16x32_bf16 v[84:87], a[172:175], v[120:123], v[84:87]\n\t"
    "v_add_f32 v168, v168, v200\n\t"
    "v_add_f32 v169, v169, v201\n\t"
    "v_mfma_f32_16x16x32_bf16 v[88:91], a[168:171], v[124:127], v[88:91]\n\t"
    "buffer_load_dwordx4 v247, s[48:51], s58 offen offset:3072 sc1 lds\n\t"
    "v_mfma_f32_16x16x32_bf16 v[92:95], a[172:175], v[124:127], v[92:95]\n\t"
    "v_add_f32 v170, v170, v202\n\t"
    "v_add_f32 v171, v171, v203\n\t"
    "s_waitcnt lgkmcnt(0)\n\t"
    "s_waitcnt vmcnt(17)\n\t"
    "s_barrier\n\t"
    "v_mfma_f32_16x16x32_bf16 v[64:67], a[176:179], v[128:131], v[64:67]\n\t"
    "ds_read_b128 v[96:99], v236 offset:0\n\t"
    "v_mfma_f32_16x16x32_bf16 v[68:71], a[180:183], v[128:131], v[68:71]\n\t"
    "ds_read_b128 v[100:103], v236 offset:4096\n\t"
    "v_mfma_f32_16x16x32_bf16 v[72:75], a[176:179], v[132:135], v[72:75]\n\t"
    "ds_read_b128 v[104:107], v236 offset:8192\n\t"
    "v_mfma_f32_16x16x32_bf16 v[76:79], a[180:183], v[132:135], v[76:79]\n\t"
    "ds_read_b128 v[108:111], v236 offset:12288\n\t"
    "v_mfma_f32_16x16x32_bf16 v[80:83], a[176:179], v[136:139], v[80:83]\n\t"
    "ds_read_b128 v[112:115], v236 offset:1024\n\t"
    "v_mfma_f32_16x16x32_bf16 v[84:87], a[180:183], v[136:139], v[84:87]\n\t"
    "ds_read_b128 v[116:119], v236 offset:5120\n\t"
    "v_mfma_f32_16x16x32_bf16 v[88:91], a[176:179], v[140:143], v[88:91]\n\t"
    "ds_read_b128 v[120:123], v236 offset:9216\n\t"
    "v_mfma_f32_16x16x32_bf16 v[92:95], a[180:183], v[140:143], v[92:95]\n\t"
    "ds_read_b128 v[124:127], v236 offset:13312\n\t"
    "s_add_u32 m0, s59, 0\n\t"
    "s_mov_b32 s58, 8192\n\t"
    "v_mfma_f32_16x16x32_bf16 v[64:67], a[184:187], v[144:147], v[64:67]\n\t"
    "buffer_load_dwordx4 v244, s[48:51], s58 offen offset:0 sc1 lds\n\t"
    "v_mfma_f32_16x16x32_bf16 v[68:71], a[188:191], v[144:147], v[68:71]\n\t"
    "v_add_f32 v172, v172, v204\n\t"
    "v_add_f32 v173, v173, v205\n\t"
    "v_mfma_f32_16x16x32_bf16 v[72:75], a[184:187], v[148:151], v[72:75]\n\t"
    "buffer_load_dwordx4 v244, s[48:51], s58 offen offset:1024 sc1 lds\n\t"
    "v_mfma_f32_16x16x32_bf16 v[76:79], a[188:191], v[148:151], v[76:79]\n\t"
    "v_add_f32 v174, v174, v206\n\t"
    "v_add_f32 v175, v175, v207\n\t"
    "v_mfma_f32_16x16x32_bf16 v[80:83], a[184:187], v[152:155], v[80:83]\n\t"
    "buffer_load_dwordx4 v245, s[48:51], s58 offen offset:2048 sc1 lds\n\t"
    "v_mfma_f32_16x16x32_bf16 v[84:87], a[188:191], v[152:155], v[84:87]\n\t"
    "v_cvt_pk_bf16_f32 v168, v168, v169\n\t"
    "v_cvt_pk_bf16_f32 v169, v170, v171\n\t"
    "v_mfma_f32_16x16x32_bf16 v[88:91], a[184:187], v[156:159], v[88:91]\n\t"
    "buffer_load_dwordx4 v245, s[48:51], s58 offen offset:3072 sc1 lds\n\t"
    "v_mfma_f32_16x16x32_bf16 v[92:95], a[188:191], v[156:159], v[92:95]\n\t"
    "v_cvt_pk_bf16_f32 v170, v172, v173\n\t"
    "v_cvt_pk_bf16_f32 v171, v174, v175\n\t"
    "s_waitcnt lgkmcnt(0)\n\t"
    "v_mfma_f32_16x16x32_bf16 v[64:67], a[192:195], v[96:99], v[64:67]\n\t"
    "ds_read_b128 v[128:131], v236 offset:2048\n\t"
    "v_mfma_f32_16x16x32_bf16 v[68:71], a[196:199], v[96:99], v[68:71]\n\t"
    "ds_read_b128 v[132:135], v236 offset:6144\n\t"
    "v_mfma_f32_16x16x32_bf16 v[72:75], a[192:195], v[100:103], v[72:75]\n\t"
    "ds_read_b128 v[136:139], v236 offset:10240\n\t"
    "v_mfma_f32_16x16x32_bf16 v[76:79], a[196:199], v[100:103], v[76:79]\n\t"
    "ds_read_b128 v[140:143], v236 offset:14336\n\t"
    "v_mfma_f32_16x16x32_bf16 v[80:83], a[192:195], v[104:107], v[80:83]\n\t"
    "ds_read_b128 v[144:147], v236 offset:3072\n\t"
    "v_mfma_f32_16x16x32_bf16 v[84:87], a[196:199], v[104:107], v[84:87]\n\t"
    "ds_read_b128 v[148:151], v236 offset:7168\n\t"
    "v_mfma_f32_16x16x32_bf16 v[88:91], a[192:195], v[108:111], v[88:91]\n\t"
    "ds_read_b128 v[152:155], v236 offset:11264\n\t"
    "v_mfma_f32_16x16x32_bf16 v[92:95], a[196:199], v[108:111], v[92:95]\n\t"
    "ds_read_b128 v[156:159], v236 offset:15360\n\t"
    "s_add_u32 m0, s59, 4096\n\t"
    "s_mov_b32 s58, 8192\n\t"
    "v_mfma_f32_16x16x32_bf16 v[64:67], a[200:203], v[112:115], v[64:67]\n\t"
    "buffer_load_dwordx4 v246, s[48:51], s58 offen offset:0 sc1 lds\n\t"
    "v_mfma_f32_16x16x32_bf16 v[68:71], a[204:207], v[112:115], v[68:71]\n\t"
    "s_mov_b32 s52, 67584\n\t"
    "v_mfma_f32_16x16x32_bf16 v[72:75], a[200:203], v[116:119], v[72:75]\n\t"
    "buffer_load_dwordx4 v246, s[48:51], s58 offen offset:1024 sc1 lds\n\t"
    "v_mfma_f32_16x16x32_bf16 v[76:79], a[204:207], v[116:119], v[76:79]\n\t"
    "v_permlane16_swap_b32 v168, v170\n\t"
    "v_mfma_f32_16x16x32_bf16 v[80:83], a[200:203], v[120:123], v[80:83]\n\t"
    "buffer_load_dwordx4 v247, s[48:51], s58 offen offset:2048 sc1 lds\n\t"
    "v_mfma_f32_16x16x32_bf16 v[84:87], a[204:207], v[120:123], v[84:87]\n\t"
    "v_permlane16_swap_b32 v169, v171\n\t"
    "v_mfma_f32_16x16x32_bf16 v[88:91], a[200:203], v[124:127], v[88:91]\n\t"
    "buffer_load_dwordx4 v247, s[48:51], s58 offen offset:3072 sc1 lds\n\t"
    "v_mfma_f32_16x16x32_bf16 v[92:95], a[204:207], v[124:127], v[92:95]\n\t"
    "buffer_store_dwordx4 v[168:171], %[vout], %[so], s52 offen sc1\n\t"
    "s_waitcnt lgkmcnt(0)\n\t"
    "s_waitcnt vmcnt(18)\n\t"
    "s_barrier\n\t"
    "v_mfma_f32_16x16x32_bf16 v[64:67], a[208:211], v[128:131], v[64:67]\n\t"
    "ds_read_b128 v[96:99], v235 offset:32768\n\t"
    "v_mfma_f32_16x16x32_bf16 v[68:71], a[212:215], v[128:131], v[68:71]\n\t"
    "ds_read_b128 v[100:103], v235 offset:36864\n\t"
    "v_mfma_f32_16x16x32_bf16 v[72:75], a[208:211], v[132:135], v[72:75]\n\t"
    "ds_read_b128 v[104:107], v235 offset:40960\n\t"
    "v_mfma_f32_16x16x32_bf16 v[76:79], a[212:215], v[132:135], v[76:79]\n\t"
    "ds_read_b128 v[108:111], v235 offset:45056\n\t"
    "v_mfma_f32_16x16x32_bf16 v[80:83], a[208:211], v[136:139], v[80:83]\n\t"
    "ds_read_b128 v[112:115], v235 offset:33792\n\t"
    "v_mfma_f32_16x16x32_bf16 v[84:87], a[212:215], v[136:139], v[84:87]\n\t"
    "ds_read_b128 v[116:119], v235 offset:37888\n\t"
    "v_mfma_f32_16x16x32_bf16 v[88:91], a[208:211], v[140:143], v[88:91]\n\t"
    "ds_read_b128 v[120:123], v235 offset:41984\n\t"
    "v_mfma_f32_16x16x32_bf16 v[92:95], a[212:215], v[140:143], v[92:95]\n\t"
    "ds_read_b128 v[124:127], v235 offset:46080\n\t"
    "s_add_u32 m0, s59, 65536\n\t"
    "s_mov_b32 s58, 16384\n\t"
    "v_mfma_f32_16x16x32_bf16 v[64:67], a[216:219], v[144:147], v[64:67]\n\t"
    "buffer_load_dwordx4 v244, s[48:51], s58 offen offset:0 sc1 lds\n\t"
    "v_mfma_f32_16x16x32_bf16 v[68:71], a[220:223], v[144:147], v[68:71]\n\t"
    "v_add_f32 v176, v176, v208\n\t"
    "v_add_f32 v177, v177, v209\n\t"
    "v_mfma_f32_16x16x32_bf16 v[72:75], a[216:219], v[148:151], v[72:75]\n\t"
    "buffer_load_dwordx4 v244, s[48:51], s58 offen offset:1024 sc1 lds\n\t"
    "v_mfma_f32_16x16x32_bf16 v[76:79], a[220:223], v[148:151], v[76:79]\n\t"
    "v_add_f32 v178, v178, v210\n\t"
    "v_add_f32 v179, v179, v211\n\t"
    "v_mfma_f32_16x16x32_bf16 v[80:83], a[216:219], v[152:155], v[80:83]\n\t"
    "buffer_load_dwordx4 v245, s[48:51], s58 offen offset:2048 sc1 lds\n\t"
    "v_mfma_f32_16x16x32_bf16 v[84:87], a[220:223], v[152:155], v[84:87]\n\t"
    "v_add_f32 v180, v180, v212\n\t"
    "v_add_f32 v181, v181, v213\n\t"
    "v_mfma_f32_16x16x32_bf16 v[88:91], a[216:219], v[156:159], v[88:91]\n\t"
    "buffer_load_dwordx4 v245, s[48:51], s58 offen offset:3072 sc1 lds\n\t"
    "v_mfma_f32_16x16x32_bf16 v[92:95], a[220:223], v[156:159], v[92:95]\n\t"
    "v_add_f32 v182, v182, v214\n\t"
    "v_add_f32 v183, v183, v215\n\t"
    "s_waitcnt lgkmcnt(0)\n\t"
    "v_mfma_f32_16x16x32_bf16 v[64:67], a[224:227], v[96:99], v[64:67]\n\t"
    "ds_read_b128 v[128:131], v235 offset:34816\n\t"
    "v_mfma_f32_16x16x32_bf16 v[68:71], a[228:231], v[96:99], v[68:71]\n\t"
    "ds_read_b128 v[132:135], v235 offset:38912\n\t"
    "v_mfma_f32_16x16x32_bf16 v[72:75], a[224:227], v[100:103], v[72:75]\n\t"
    "ds_read_b128 v[136:139], v235 offset:43008\n\t"
    "v_mfma_f32_16x16x32_bf16 v[76:79], a[228:231], v[100:103], v[76:79]\n\t"
    "ds_read_b128 v[140:143], v235 offset:47104\n\t"
    "v_mfma_f32_16x16x32_bf16 v[80:83], a[224:227], v[104:107], v[80:83]\n\t"
    "ds_read_b128 v[144:147], v235 offset:35840\n\t"
    "v_mfma_f32_16x16x32_bf16 v[84:87], a[228:231], v[104:107], v[84:87]\n\t"
    "ds_read_b128 v[148:151], v235 offset:39936\n\t"
    "v_mfma_f32_16x16x32_bf16 v[88:91], a[224:227], v[108:111], v[88:91]\n\t"
    "ds_read_b128 v[152:155], v235 offset:44032\n\t"
    "v_mfma_f32_16x16x32_bf16 v[92:95], a[228:231], v[108:111], v[92:95]\n\t"
    "ds_read_b128 v[156:159], v235 offset:48128\n\t"
    "s_add_u32 m0, s59, 69632\n\t"
    "s_mov_b32 s58, 16384\n\t"
    "v_mfma_f32_16x16x32_bf16 v[64:67], a[232:235], v[112:115], v[64:67]\n\t"
    "buffer_load_dwordx4 v246, s[48:51], s58 offen offset:0 sc1 lds\n\t"
    "v_mfma_f32_16x16x32_bf16 v[68:71], a[236:239], v[112:115], v[68:71]\n\t"
    "v_cvt_pk_bf16_f32 v176, v176, v177\n\t"
    "v_cvt_pk_bf16_f32 v177, v178, v179\n\t"
    "v_mfma_f32_16x16x32_bf16 v[72:75], a[232:235], v[116:119], v[72:75]\n\t"
    "buffer_load_dwordx4 v246, s[48:51], s58 offen offset:1024 sc1 lds\n\t"
    "v_mfma_f32_16x16x32_bf16 v[76:79], a[236:239], v[116:119], v[76:79]\n\t"
    "v_cvt_pk_bf16_f32 v178, v180, v181\n\t"
    "v_cvt_pk_bf16_f32 v179, v182, v183\n\t"
    "v_mfma_f32_16x16x32_bf16 v[80:83], a[232:235], v[120:123], v[80:83]\n\t"
    "buffer_load_dwordx4 v247, s[48:51], s58 offen offset:2048 sc1 lds\n\t"
    "v_mfma_f32_16x16x32_bf16 v[84:87], a[236:239], v[120:123], v[84:87]\n\t"
    "s_mov_b32 s52, 133120\n\t"
    "v_mfma_f32_16x16x32_bf16 v[88:91], a[232:235], v[124:127], v[88:91]\n\t"
    "buffer_load_dwordx4 v247, s[48:51], s58 offen offset:3072 sc1 lds\n\t"
    "v_mfma_f32_16x16x32_bf16 v[92:95], a[236:239], v[124:127], v[92:95]\n\t"
    "v_permlane16_swap_b32 v176, v178\n\t"
    "s_waitcnt lgkmcnt(0)\n\t"
    "s_waitcnt vmcnt(17)\n\t"
    "s_barrier\n\t"
    "v_mfma_f32_16x16x32_bf16 v[64:67], a[240:243], v[128:131], v[64:67]\n\t"
    "ds_read_b128 v[96:99], v236 offset:32768\n\t"
    "v_mfma_f32_16x16x32_bf16 v[68:71], a[244:247], v[128:131], v[68:71]\n\t"
    "ds_read_b128 v[100:103], v236 offset:36864\n\t"
    "v_mfma_f32_16x16x32_bf16 v[72:75], a[240:243], v[132:135], v[72:75]\n\t"
    "ds_read_b128 v[104:107], v236 offset:40960\n\t"
    "v_mfma_f32_16x16x32_bf16 v[76:79], a[244:247], v[132:135], v[76:79]\n\t"
    "ds_read_b128 v[108:111], v236 offset:45056\n\t"
    "v_mfma_f32_16x16x32_bf16 v[80:83], a[240:243], v[136:139], v[80:83]\n\t"
    "ds_read_b128 v[112:115], v236 offset:33792\n\t"
    "v_mfma_f32_16x16x32_bf16 v[84:87], a[244:247], v[136:139], v[84:87]\n\t"
    "ds_read_b128 v[116:119], v236 offset:37888\n\t"
    "v_mfma_f32_16x16x32_bf16 v[88:91], a[240:243], v[140:143], v[88:91]\n\t"
    "ds_read_b128 v[120:123], v236 offset:41984\n\t"
    "v_mfma_f32_16x16x32_bf16 v[92:95], a[244:247], v[140:143], v[92:95]\n\t"
    "ds_read_b128 v[124:127], v236 offset:46080\n\t"
    "s_add_u32 m0, s59, 32768\n\t"
    "s_mov_b32 s58, 24576\n\t"
    "v_mfma_f32_16x16x32_bf16 v[64:67], a[248:251], v[144:147], v[64:67]\n\t"
    "buffer_load_dwordx4 v244, s[48:51], s58 offen offset:0 sc1 lds\n\t"
    "v_mfma_f32_16x16x32_bf16 v[68:71], a[252:255], v[144:147], v[68:71]\n\t"
    "v_permlane16_swap_b32 v177, v179\n\t"
    "v_mfma_f32_16x16x32_bf16 v[72:75], a[248:251], v[148:151], v[72:75]\n\t"
    "buffer_load_dwordx4 v244, s[48:51], s58 offen offset:1024 sc1 lds\n\t"
    "v_mfma_f32_16x16x32_bf16 v[76:79], a[252:255], v[148:151], v[76:79]\n\t"
    "buffer_store_dwordx4 v[176:179], %[vout], %[so], s52 offen sc1\n\t"
    "v_mfma_f32_16x16x32_bf16 v[80:83], a[248:251], v[152:155], v[80:83]\n\t"
    "buffer_load_dwordx4 v245, s[48:51], s58 offen offset:2048 sc1 lds\n\t"
    "v_mfma_f32_16x16x32_bf16 v[84:87], a[252:255], v[152:155], v[84:87]\n\t"
    "v_add_f32 v184, v184, v216\n\t"
    "v_add_f32 v185, v185, v217\n\t"
    "v_mfma_f32_16x16x32_bf16 v[88:91], a[248:251], v[156:159], v[88:91]\n\t"
    "buffer_load_dwordx4 v245, s[48:51], s58 offen offset:3072 sc1 lds\n\t"
    "v_mfma_f32_16x16x32_bf16 v[92:95], a[252:255], v[156:159], v[92:95]\n\t"
    "v_add_f32 v186, v186, v218\n\t"
    "v_add_f32 v187, v187, v219\n\t"
    "s_waitcnt lgkmcnt(0)\n\t"
    "v_mfma_f32_16x16x32_bf16 v[32:35], a[128:131], v[96:99], 0\n\t"
    "ds_read_b128 v[128:131], v236 offset:34816\n\t"
    "v_mfma_f32_16x16x32_bf16 v[36:39], a[132:135], v[96:99], 0\n\t"
    "ds_read_b128 v[132:135], v236 offset:38912\n\t"
    "v_mfma_f32_16x16x32_bf16 v[40:43], a[128:131], v[100:103], 0\n\t"
    "ds_read_b128 v[136:139], v236 offset:43008\n\t"
    "v_mfma_f32_16x16x32_bf16 v[44:47], a[132:135], v[100:103], 0\n\t"
    "ds_read_b128 v[140:143], v236 offset:47104\n\t"
    "v_mfma_f32_16x16x32_bf16 v[48:51], a[128:131], v[104:107], 0\n\t"
    "ds_read_b128 v[144:147], v236 offset:35840\n\t"
    "v_mfma_f32_16x16x32_bf16 v[52:55], a[132:135], v[104:107], 0\n\t"
    "ds_read_b128 v[148:151], v236 offset:39936\n\t"
    "v_mfma_f32_16x16x32_bf16 v[56:59], a[128:131], v[108:111], 0\n\t"
    "ds_read_b128 v[152:155], v236 offset:44032\n\t"
    "v_mfma_f32_16x16x32_bf16 v[60:63], a[132:135], v[108:111], 0\n\t"
    "ds_read_b128 v[156:159], v236 offset:48128\n\t"
    "s_add_u32 m0, s59, 36864\n\t"
    "s_mov_b32 s58, 24576\n\t"
    "v_mfma_f32_16x16x32_bf16 v[32:35], a[136:139], v[112:115], v[32:35]\n\t"
    "buffer_load_dwordx4 v246, s[48:51], s58 offen offset:0 sc1 lds\n\t"
    "v_mfma_f32_16x16x32_bf16 v[36:39], a[140:143], v[112:115], v[36:39]\n\t"
    "ds_write_b128 v238, v[64:67] offset:0\n\t"
    "v_add_f32 v188, v188, v220\n\t"
    "v_mfma_f32_16x16x32_bf16 v[40:43], a[136:139], v[116:119], v[40:43]\n\t"
    "buffer_load_dwordx4 v246, s[48:51], s58 offen offset:1024 sc1 lds\n\t"
    "v_mfma_f32_16x16x32_bf16 v[44:47], a[140:143], v[116:119], v[44:47]\n\t"
    "v_add_f32 v189, v189, v221\n\t"
    "v_add_f32 v190, v190, v222\n\t"
    "v_mfma_f32_16x16x32_bf16 v[48:51], a[136:139], v[120:123], v[48:51]\n\t"
    "buffer_load_dwordx4 v247, s[48:51], s58 offen offset:2048 sc1 lds\n\t"
    "v_mfma_f32_16x16x32_bf16 v[52:55], a[140:143], v[120:123], v[52:55]\n\t"
    "ds_write_b128 v238, v[68:71] offset:1024\n\t"
    "v_add_f32 v191, v191, v223\n\t"
    "v_mfma_f32_16x16x32_bf16 v[56:59], a[136:139], v[124:127], v[56:59]\n\t"
    "buffer_load_dwordx4 v247, s[48:51], s58 offen offset:3072 sc1 lds\n\t"
    "v_mfma_f32_16x16x32_bf16 v[60:63], a[140:143], v[124:127], v[60:63]\n\t"
    "v_cvt_pk_bf16_f32 v184, v184, v185\n\t"
    "v_cvt_pk_bf16_f32 v185, v186, v187\n\t"
    "s_waitcnt lgkmcnt(2)\n\t"
    "s_waitcnt vmcnt(18)\n\t"
    "s_barrier\n\t"
    "v_mfma_f32_16x16x32_bf16 v[32:35], a[144:147], v[128:131], v[32:35]\n\t"
    "ds_read_b128 v[96:99], v235 offset:0\n\t"
    "v_mfma_f32_16x16x32_bf16 v[36:39], a[148:151], v[128:131], v[36:39]\n\t"
    "ds_read_b128 v[100:103], v235 offset:4096\n\t"
    "v_mfma_f32_16x16x32_bf16 v[40:43], a[144:147], v[132:135], v[40:43]\n\t"
    "ds_read_b128 v[104:107], v235 offset:8192\n\t"
    "v_mfma_f32_16x16x32_bf16 v[44:47], a[148:151], v[132:135], v[44:47]\n\t"
    "ds_read_b128 v[108:111], v235 offset:12288\n\t"
    "v_mfma_f32_16x16x32_bf16 v[48:51], a[144:147], v[136:139], v[48:51]\n\t"
    "ds_read_b128 v[112:115], v235 offset:1024\n\t"
    "v_mfma_f32_16x16x32_bf16 v[52:55], a[148:151], v[136:139], v[52:55]\n\t"
    "ds_read_b128 v[116:119], v235 offset:5120\n\t"
    "v_mfma_f32_16x16x32_bf16 v[56:59], a[144:147], v[140:143], v[56:59]\n\t"
    "ds_read_b128 v[120:123], v235 offset:9216\n\t"
    "v_mfma_f32_16x16x32_bf16 v[60:63], a[148:151], v[140:143], v[60:63]\n\t"
    "ds_read_b128 v[124:127], v235 offset:13312\n\t"
    "v_mfma_f32_16x16x32_bf16 v[32:35], a[152:155], v[144:147], v[32:35]\n\t"
    "v_cvt_pk_bf16_f32 v186, v188, v189\n\t"
    "v_cvt_pk_bf16_f32 v187, v190, v191\n\t"
    "v_mfma_f32_16x16x32_bf16 v[36:39], a[156:159], v[144:147], v[36:39]\n\t"
    "ds_write_b128 v238, v[72:75] offset:2048\n\t"
    "s_mov_b32 s52, 198656\n\t"
    "v_mfma_f32_16x16x32_bf16 v[40:43], a[152:155], v[148:151], v[40:43]\n\t"
    "v_permlane16_swap_b32 v184, v186\n\t"
    "v_mfma_f32_16x16x32_bf16 v[44:47], a[156:159], v[148:151], v[44:47]\n\t"
    "v_permlane16_swap_b32 v185, v187\n\t"
    "v_mfma_f32_16x16x32_bf16 v[48:51], a[152:155], v[152:155], v[48:51]\n\t"
    "buffer_store_dwordx4 v[184:187], %[vout], %[so], s52 offen sc1\n\t"
    "v_mfma_f32_16x16x32_bf16 v[52:55], a[156:159], v[152:155], v[52:55]\n\t"
    "ds_write_b128 v238, v[76:79] offset:3072\n\t"
    "v_mfma_f32_16x16x32_bf16 v[56:59], a[152:155], v[156:159], v[56:59]\n\t"
    "v_mfma_f32_16x16x32_bf16 v[60:63], a[156:159], v[156:159], v[60:63]\n\t"
    "s_waitcnt lgkmcnt(2)\n\t"
    "s_cmp_lg_u32 %[bump], 0\n\t"
    "s_cbranch_scc1 NOSMP_%=\n\t"
    "v_add_u32 v248, 0x2000, %[vsm]\n\t"
    "v_add_u32 v249, 0x4000, %[vsm]\n\t"
    "v_add_u32 v250, 0x6000, %[vsm]\n\t"
    "global_load_dword %[sm0], %[vsm], %[pf] sc1\n\t"
    "global_load_dword %[sm1], v248, %[pf] sc1\n\t"
    "global_load_dword %[sm2], v249, %[pf] sc1\n\t"
    "global_load_dword %[sm3], v250, %[pf] sc1\n\t"
    "NOSMP_%=:\n"
    "v_mfma_f32_16x16x32_bf16 v[32:35], a[160:163], v[96:99], v[32:35]\n\t"
    "ds_read_b128 v[128:131], v235 offset:2048\n\t"
    "v_mfma_f32_16x16x32_bf16 v[36:39], a[164:167], v[96:99], v[36:39]\n\t"
    "ds_read_b128 v[132:135], v235 offset:6144\n\t"
    "v_mfma_f32_16x16x32_bf16 v[40:43], a[160:163], v[100:103], v[40:43]\n\t"
    "ds_read_b128 v[136:139], v235 offset:10240\n\t"
    "v_mfma_f32_16x16x32_bf16 v[44:47], a[164:167], v[100:103], v[44:47]\n\t"
    "ds_read_b128 v[140:143], v235 offset:14336\n\t"
    "v_mfma_f32_16x16x32_bf16 v[48:51], a[160:163], v[104:107], v[48:51]\n\t"
    "ds_read_b128 v[144:147], v235 offset:3072\n\t"
    "v_mfma_f32_16x16x32_bf16 v[52:55], a[164:167], v[104:107], v[52:55]\n\t"
    "ds_read_b128 v[148:151], v235 offset:7168\n\t"
    "v_mfma_f32_16x16x32_bf16 v[56:59], a[160:163], v[108:111], v[56:59]\n\t"
    "ds_read_b128 v[152:155], v235 offset:11264\n\t"
    "v_mfma_f32_16x16x32_bf16 v[60:63], a[164:167], v[108:111], v[60:63]\n\t"
    "ds_read_b128 v[156:159], v235 offset:15360\n\t"
    "v_mfma_f32_16x16x32_bf16 v[32:35], a[168:171], v[112:115], v[32:35]\n\t"
    "v_mfma_f32_16x16x32_bf16 v[36:39], a[172:175], v[112:115], v[36:39]\n\t"
    "ds_write_b128 v238, v[80:83] offset:4096\n\t"
    "v_mfma_f32_16x16x32_bf16 v[40:43], a[168:171], v[116:119], v[40:43]\n\t"
    "v_mfma_f32_16x16x32_bf16 v[44:47], a[172:175], v[116:119], v[44:47]\n\t"
    "v_mfma_f32_16x16x32_bf16 v[48:51], a[168:171], v[120:123], v[48:51]\n\t"
    "v_mfma_f32_16x16x32_bf16 v[52:55], a[172:175], v[120:123], v[52:55]\n\t"
    "ds_write_b128 v238, v[84:87] offset:5120\n\t"
    "v_mfma_f32_16x16x32_bf16 v[56:59], a[168:171], v[124:127], v[56:59]\n\t"
    "v_mfma_f32_16x16x32_bf16 v[60:63], a[172:175], v[124:127], v[60:63]\n\t"
    "s_waitcnt lgkmcnt(2)\n\t"
    "s_waitcnt vmcnt(10)\n\t"
    "s_barrier\n\t"
    "v_mfma_f32_16x16x32_bf16 v[32:35], a[176:179], v[128:131], v[32:35]\n\t"
    "ds_read_b128 v[96:99], v236 offset:0\n\t"
    "v_mfma_f32_16x16x32_bf16 v[36:39], a[180:183], v[128:131], v[36:39]\n\t"
    "ds_read_b128 v[100:103], v236 offset:4096\n\t"
    "v_mfma_f32_16x16x32_bf16 v[40:43], a[176:179], v[132:135], v[40:43]\n\t"
    "ds_read_b128 v[104:107], v236 offset:8192\n\t"
    "v_mfma_f32_16x16x32_bf16 v[44:47], a[180:183], v[132:135], v[44:47]\n\t"
    "ds_read_b128 v[108:111], v236 offset:12288\n\t"
    "v_mfma_f32_16x16x32_bf16 v[48:51], a[176:179], v[136:139], v[48:51]\n\t"
    "ds_read_b128 v[112:115], v236 offset:1024\n\t"
    "v_mfma_f32_16x16x32_bf16 v[52:55], a[180:183], v[136:139], v[52:55]\n\t"
    "ds_read_b128 v[116:119], v236 offset:5120\n\t"
    "v_mfma_f32_16x16x32_bf16 v[56:59], a[176:179], v[140:143], v[56:59]\n\t"
    "ds_read_b128 v[120:123], v236 offset:9216\n\t"
    "v_mfma_f32_16x16x32_bf16 v[60:63], a[180:183], v[140:143], v[60:63]\n\t"
    "ds_read_b128 v[124:127], v236 offset:13312\n\t"
    "v_mfma_f32_16x16x32_bf16 v[32:35], a[184:187], v[144:147], v[32:35]\n\t"
    "v_mfma_f32_16x16x32_bf16 v[36:39], a[188:191], v[144:147], v[36:39]\n\t"
    "ds_write_b128 v238, v[88:91] offset:6144\n\t"
    "v_mfma_f32_16x16x32_bf16 v[40:43], a[184:187], v[148:151], v[40:43]\n\t"
    "v_mfma_f32_16x16x32_bf16 v[44:47], a[188:191], v[148:151], v[44:47]\n\t"
    "v_mfma_f32_16x16x32_bf16 v[48:51], a[184:187], v[152:155], v[48:51]\n\t"
    "v_mfma_f32_16x16x32_bf16 v[52:55], a[188:191], v[152:155], v[52:55]\n\t"
    "ds_write_b128 v238, v[92:95] offset:7168\n\t"
    "v_mfma_f32_16x16x32_bf16 v[56:59], a[184:187], v[156:159], v[56:59]\n\t"
    "v_mfma_f32_16x16x32_bf16 v[60:63], a[188:191], v[156:159], v[60:63]\n\t"
    "s_waitcnt lgkmcnt(2)\n\t"
    "v_mfma_f32_16x16x32_bf16 v[32:35], a[192:195], v[96:99], v[32:35]\n\t"
    "ds_read_b128 v[128:131], v236 offset:2048\n\t"
    "v_mfma_f32_16x16x32_bf16 v[36:39], a[196:199], v[96:99], v[36:39]\n\t"
    "ds_read_b128 v[132:135], v236 offset:6144\n\t"
    "v_mfma_f32_16x16x32_bf16 v[40:43], a[192:195], v[100:103], v[40:43]\n\t"
    "ds_read_b128 v[136:139], v236 offset:10240\n\t"
    "v_mfma_f32_16x16x32_bf16 v[44:47], a[196:199], v[100:103], v[44:47]\n\t"
    "ds_read_b128 v[140:143], v236 offset:14336\n\t"
    "v_mfma_f32_16x16x32_bf16 v[48:51], a[192:195], v[104:107], v[48:51]\n\t"
    "ds_read_b128 v[144:147], v236 offset:3072\n\t"
    "v_mfma_f32_16x16x32_bf16 v[52:55], a[196:199], v[104:107], v[52:55]\n\t"
    "ds_read_b128 v[148:151], v236 offset:7168\n\t"
    "v_mfma_f32_16x16x32_bf16 v[56:59], a[192:195], v[108:111], v[56:59]\n\t"
    "ds_read_b128 v[152:155], v236 offset:11264\n\t"
    "v_mfma_f32_16x16x32_bf16 v[60:63], a[196:199], v[108:111], v[60:63]\n\t"
    "ds_read_b128 v[156:159], v236 offset:15360\n\t"
    "v_mfma_f32_16x16x32_bf16 v[32:35], a[200:203], v[112:115], v[32:35]\n\t"
    "v_mfma_f32_16x16x32_bf16 v[36:39], a[204:207], v[112:115], v[36:39]\n\t"
    "v_mfma_f32_16x16x32_bf16 v[40:43], a[200:203], v[116:119], v[40:43]\n\t"
    "v_mfma_f32_16x16x32_bf16 v[44:47], a[204:207], v[116:119], v[44:47]\n\t"
    "v_mfma_f32_16x16x32_bf16 v[48:51], a[200:203], v[120:123], v[48:51]\n\t"
    "v_mfma_f32_16x16x32_bf16 v[52:55], a[204:207], v[120:123], v[52:55]\n\t"
    "v_mfma_f32_16x16x32_bf16 v[56:59], a[200:203], v[124:127], v[56:59]\n\t"
    "v_mfma_f32_16x16x32_bf16 v[60:63], a[204:207], v[124:127], v[60:63]\n\t"
    "s_waitcnt lgkmcnt(0)\n\t"
    "s_waitcnt vmcnt(1)\n\t"
    "s_barrier\n\t"
    "v_mfma_f32_16x16x32_bf16 v[32:35], a[208:211], v[128:131], v[32:35]\n\t"
    "ds_read_b128 v[96:99], v235 offset:32768\n\t"
    "v_mfma_f32_16x16x32_bf16 v[36:39], a[212:215], v[128:131], v[36:39]\n\t"
    "ds_read_b128 v[100:103], v235 offset:36864\n\t"
    "v_mfma_f32_16x16x32_bf16 v[40:43], a[208:211], v[132:135], v[40:43]\n\t"
    "ds_read_b128 v[104:107], v235 offset:40960\n\t"
    "v_mfma_f32_16x16x32_bf16 v[44:47], a[212:215], v[132:135], v[44:47]\n\t"
    "ds_read_b128 v[108:111], v235 offset:45056\n\t"
    "v_mfma_f32_16x16x32_bf16 v[48:51], a[208:211], v[136:139], v[48:51]\n\t"
    "ds_read_b128 v[112:115], v235 offset:33792\n\t"
    "v_mfma_f32_16x16x32_bf16 v[52:55], a[212:215], v[136:139], v[52:55]\n\t"
    "ds_read_b128 v[116:119], v235 offset:37888\n\t"
    "v_mfma_f32_16x16x32_bf16 v[56:59], a[208:211], v[140:143], v[56:59]\n\t"
    "ds_read_b128 v[120:123], v235 offset:41984\n\t"
    "v_mfma_f32_16x16x32_bf16 v[60:63], a[212:215], v[140:143], v[60:63]\n\t"
    "ds_read_b128 v[124:127], v235 offset:46080\n\t"
    "v_mfma_f32_16x16x32_bf16 v[32:35], a[216:219], v[144:147], v[32:35]\n\t"
    "v_mfma_f32_16x16x32_bf16 v[36:39], a[220:223], v[144:147], v[36:39]\n\t"
    "ds_read_b128 v[64:67], v239 offset:0\n\t"
    "v_mfma_f32_16x16x32_bf16 v[40:43], a[216:219], v[148:151], v[40:43]\n\t"
    "v_mfma_f32_16x16x32_bf16 v[44:47], a[220:223], v[148:151], v[44:47]\n\t"
    "ds_read_b128 v[68:71], v239 offset:1024\n\t"
    "v_mfma_f32_16x16x32_bf16 v[48:51], a[216:219], v[152:155], v[48:51]\n\t"
    "v_mfma_f32_16x16x32_bf16 v[52:55], a[220:223], v[152:155], v[52:55]\n\t"
    "ds_read_b128 v[72:75], v239 offset:2048\n\t"
    "v_mfma_f32_16x16x32_bf16 v[56:59], a[216:219], v[156:159], v[56:59]\n\t"
    "v_mfma_f32_16x16x32_bf16 v[60:63], a[220:223], v[156:159], v[60:63]\n\t"
    "ds_read_b128 v[76:79], v239 offset:3072\n\t"
    "s_waitcnt lgkmcnt(4)\n\t"
    "v_mfma_f32_16x16x32_bf16 v[32:35], a[224:227], v[96:99], v[32:35]\n\t"
    "ds_read_b128 v[128:131], v235 offset:34816\n\t"
    "v_mfma_f32_16x16x32_bf16 v[36:39], a[228:231], v[96:99], v[36:39]\n\t"
    "ds_read_b128 v[132:135], v235 offset:38912\n\t"
    "v_mfma_f32_16x16x32_bf16 v[40:43], a[224:227], v[100:103], v[40:43]\n\t"
    "ds_read_b128 v[136:139], v235 offset:43008\n\t"
    "v_mfma_f32_16x16x32_bf16 v[44:47], a[228:231], v[100:103], v[44:47]\n\t"
    "ds_read_b128 v[140:143], v235 offset:47104\n\t"
    "v_mfma_f32_16x16x32_bf16 v[48:51], a[224:227], v[104:107], v[48:51]\n\t"
    "ds_read_b128 v[144:147], v235 offset:35840\n\t"
    "v_mfma_f32_16x16x32_bf16 v[52:55], a[228:231], v[104:107], v[52:55]\n\t"
    "ds_read_b128 v[148:151], v235 offset:39936\n\t"
    "v_mfma_f32_16x16x32_bf16 v[56:59], a[224:227], v[108:111], v[56:59]\n\t"
    "ds_read_b128 v[152:155], v235 offset:44032\n\t"
    "v_mfma_f32_16x16x32_bf16 v[60:63], a[228:231], v[108:111], v[60:63]\n\t"
    "ds_read_b128 v[156:159], v235 offset:48128\n\t"
    "v_mfma_f32_16x16x32_bf16 v[32:35], a[232:235], v[112:115], v[32:35]\n\t"
    "v_mfma_f32_16x16x32_bf16 v[36:39], a[236:239], v[112:115], v[36:39]\n\t"
    "ds_read_b128 v[80:83], v239 offset:4096\n\t"
    "v_mfma_f32_16x16x32_bf16 v[40:43], a[232:235], v[116:119], v[40:43]\n\t"
    "v_mfma_f32_16x16x32_bf16 v[44:47], a[236:239], v[116:119], v[44:47]\n\t"
    "ds_read_b128 v[84:87], v239 offset:5120\n\t"
    "v_mfma_f32_16x16x32_bf16 v[48:51], a[232:235], v[120:123], v[48:51]\n\t"
    "v_mfma_f32_16x16x32_bf16 v[52:55], a[236:239], v[120:123], v[52:55]\n\t"
    "ds_read_b128 v[88:91], v239 offset:6144\n\t"
    "v_mfma_f32_16x16x32_bf16 v[56:59], a[232:235], v[124:127], v[56:59]\n\t"
    "v_mfma_f32_16x16x32_bf16 v[60:63], a[236:239], v[124:127], v[60:63]\n\t"
    "ds_read_b128 v[92:95], v239 offset:7168\n\t"
    "s_waitcnt lgkmcnt(4)\n\t"
    "v_mfma_f32_16x16x32_bf16 v[32:35], a[240:243], v[128:131], v[32:35]\n\t"
    "v_mfma_f32_16x16x32_bf16 v[36:39], a[244:247], v[128:131], v[36:39]\n\t"
    "v_mfma_f32_16x16x32_bf16 v[40:43], a[240:243], v[132:135], v[40:43]\n\t"
    "v_mfma_f32_16x16x32_bf16 v[44:47], a[244:247], v[132:135], v[44:47]\n\t"
    "v_mfma_f32_16x16x32_bf16 v[48:51], a[240:243], v[136:139], v[48:51]\n\t"
    "v_mfma_f32_16x16x32_bf16 v[52:55], a[244:247], v[136:139], v[52:55]\n\t"
    "v_mfma_f32_16x16x32_bf16 v[56:59], a[240:243], v[140:143], v[56:59]\n\t"
    "v_mfma_f32_16x16x32_bf16 v[60:63], a[244:247], v[140:143], v[60:63]\n\t"
    "v_mfma_f32_16x16x32_bf16 v[32:35], a[248:251], v[144:147], v[32:35]\n\t"
    "v_mfma_f32_16x16x32_bf16 v[36:39], a[252:255], v[144:147], v[36:39]\n\t"
    "v_mfma_f32_16x16x32_bf16 v[40:43], a[248:251], v[148:151], v[40:43]\n\t"
    "v_mfma_f32_16x16x32_bf16 v[44:47], a[252:255], v[148:151], v[44:47]\n\t"
    "v_mfma_f32_16x16x32_bf16 v[48:51], a[248:251], v[152:155], v[48:51]\n\t"
    "v_mfma_f32_16x16x32_bf16 v[52:55], a[252:255], v[152:155], v[52:55]\n\t"
    "v_mfma_f32_16x16x32_bf16 v[56:59], a[248:251], v[156:159], v[56:59]\n\t"
    "v_mfma_f32_16x16x32_bf16 v[60:63], a[252:255], v[156:159], v[60:63]\n\t"
    "s_waitcnt lgkmcnt(0)\n\t"
    "v_add_f32 v32, v32, v64\n\t"
    "v_add_f32 v33, v33, v65\n\t"
    "v_add_f32 v34, v34, v66\n\t"
    "v_add_f32 v35, v35, v67\n\t"
    "v_add_f32 v36, v36, v68\n\t"
    "v_add_f32 v37, v37, v69\n\t"
    "v_add_f32 v38, v38, v70\n\t"
    "v_add_f32 v39, v39, v71\n\t"
    "v_cvt_pk_bf16_f32 v32, v32, v33\n\t"
    "v_cvt_pk_bf16_f32 v33, v34, v35\n\t"
    "v_cvt_pk_bf16_f32 v34, v36, v37\n\t"
    "v_cvt_pk_bf16_f32 v35, v38, v39\n\t"
    "s_mov_b32 s52, 0\n\t"
    "v_permlane16_swap_b32 v32, v34\n\t"
    "v_permlane16_swap_b32 v33, v35\n\t"
    "buffer_store_dwordx4 v[32:35], %[vout], %[so], s52 offen sc1\n\t"
    "v_add_f32 v40, v40, v72\n\t"
    "v_add_f32 v41, v41, v73\n\t"
    "v_add_f32 v42, v42, v74\n\t"
    "v_add_f32 v43, v43, v75\n\t"
    "v_add_f32 v44, v44, v76\n\t"
    "v_add_f32 v45, v45, v77\n\t"
    "v_add_f32 v46, v46, v78\n\t"
    "v_add_f32 v47, v47, v79\n\t"
    "v_cvt_pk_bf16_f32 v40, v40, v41\n\t"
    "v_cvt_pk_bf16_f32 v41, v42, v43\n\t"
    "v_cvt_pk_bf16_f32 v42, v44, v45\n\t"
    "v_cvt_pk_bf16_f32 v43, v46, v47\n\t"
    "s_mov_b32 s52, 65536\n\t"
    "v_permlane16_swap_b32 v40, v42\n\t"
    "v_permlane16_swap_b32 v41, v43\n\t"
    "buffer_store_dwordx4 v[40:43], %[vout], %[so], s52 offen sc1\n\t"
    "v_add_f32 v48, v48, v80\n\t"
    "v_add_f32 v49, v49, v81\n\t"
    "v_add_f32 v50, v50, v82\n\t"
    "v_add_f32 v51, v51, v83\n\t"
    "v_add_f32 v52, v52, v84\n\t"
    "v_add_f32 v53, v53, v85\n\t"
    "v_add_f32 v54, v54, v86\n\t"
    "v_add_f32 v55, v55, v87\n\t"
    "v_cvt_pk_bf16_f32 v48, v48, v49\n\t"
    "v_cvt_pk_bf16_f32 v49, v50, v51\n\t"
    "v_cvt_pk_bf16_f32 v50, v52, v53\n\t"
    "v_cvt_pk_bf16_f32 v51, v54, v55\n\t"
    "s_mov_b32 s52, 131072\n\t"
    "v_permlane16_swap_b32 v48, v50\n\t"
    "v_permlane16_swap_b32 v49, v51\n\t"
    "buffer_store_dwordx4 v[48:51], %[vout], %[so], s52 offen sc1\n\t"
    "v_add_f32 v56, v56, v88\n\t"
    "v_add_f32 v57, v57, v89\n\t"
    "v_add_f32 v58, v58, v90\n\t"
    "v_add_f32 v59, v59, v91\n\t"
    "v_add_f32 v60, v60, v92\n\t"
    "v_add_f32 v61, v61, v93\n\t"
    "v_add_f32 v62, v62, v94\n\t"
    "v_add_f32 v63, v63, v95\n\t"
    "v_cvt_pk_bf16_f32 v56, v56, v57\n\t"
    "v_cvt_pk_bf16_f32 v57, v58, v59\n\t"
    "v_cvt_pk_bf16_f32 v58, v60, v61\n\t"
    "v_cvt_pk_bf16_f32 v59, v62, v63\n\t"
    "s_mov_b32 s52, 196608\n\t"
    "v_permlane16_swap_b32 v56, v58\n\t"
    "v_permlane16_swap_b32 v57, v59\n\t"
    "buffer_store_dwordx4 v[56:59], %[vout], %[so], s52 offen sc1\n\t"
    "s_waitcnt vmcnt(4)\n\t"
    "s_branch END_%=\n\t"
    "; ---- fallback: pass 1 takes the true row maxima over all 2048 keys, pass 2 redoes softmax(QK)V against them\n\t"
    "SLOW_%=:\n"
    "s_mov_b32 s56, 1\n\t"
    "v_mov_b32 v125, 0xff800000\n\t"
    "v_mov_b32 v126, 0xff800000\n\t"
    "s_mov_b32 s60, 0\n\t"
    "SLOW1_%=:\n"
    "buffer_load_dwordx4 v[192:195], %[rb], s[40:43], s60 offen offset:0 sc1\n\t"
    "buffer_load_dwordx4 v[196:199], %[rb], s[40:43], s60 offen offset:1024 sc1\n\t"
    "s_waitcnt vmcnt(0)\n\t"
    "v_mfma_f32_32x32x16_bf16 v[128:143], v[192:195], v[32:35], 0\n\t"
    "v_mfma_f32_32x32x16_bf16 v[128:143], v[196:199], v[36:39], v[128:143]\n\t"
    "v_mfma_f32_32x32x16_bf16 v[144:159], v[192:195], v[40:43], 0\n\t"
    "v_mfma_f32_32x32x16_bf16 v[144:159], v[196:199], v[44:47], v[144:159]\n\t"
    "s_nop 7\n\t"
    "s_nop 1\n\t"
    "v_max3_f32 v125, v125, v128, v129\n\t"
    "v_max3_f32 v125, v125, v130, v131\n\t"
    "v_max3_f32 v125, v125, v132, v133\n\t"
    "v_max3_f32 v125, v125, v134, v135\n\t"
    "v_max3_f32 v125, v125, v136, v137\n\t"
    "v_max3_f32 v125, v125, v138, v139\n\t"
    "v_max3_f32 v125, v125, v140, v141\n\t"
    "v_max3_f32 v125, v125, v142, v143\n\t"
    "v_max3_f32 v126, v126, v144, v145\n\t"
    "v_max3_f32 v126, v126, v146, v147\n\t"
    "v_max3_f32 v126, v126, v148, v149\n\t"
    "v_max3_f32 v126, v126, v150, v151\n\t"
    "v_max3_f32 v126, v126, v152, v153\n\t"
    "v_max3_f32 v126, v126, v154, v155\n\t"
    "v_max3_f32 v126, v126, v156, v157\n\t"
    "v_max3_f32 v126, v126, v158, v159\n\t"
    "s_add_u32 s60, s60, 4096\n\t"
    "s_cmp_lt_u32 s60, 0x40000\n\t"
    "s_nop 7\n\t"
    "s_cbranch_scc1 SLOW1_%=\n\t"
    "v_mov_b32 v127, v125\n\t"
    "s_nop 1\n\t"
    "v_permlane32_swap_b32 v125, v127\n\t"
    "v_max_f32 v125, v125, v127\n\t"
    "v_mov_b32 v127, v126\n\t"
    "s_nop 1\n\t"
    "v_permlane32_swap_b32 v126, v127\n\t"
    "v_max_f32 v126, v126, v127\n\t"
    "v_sub_f32 v48, 0, v125\n\t"
    "v_sub_f32 v64, 0, v126\n\t"
    "v_mov_b32 v80, 0\n\t"
    "v_mov_b32 v96, 0\n\t"
    "v_sub_f32 v49, 0, v125\n\t"
    "v_sub_f32 v65, 0, v126\n\t"
    "v_mov_b32 v81, 0\n\t"
    "v_mov_b32 v97, 0\n\t"
    "v_sub_f32 v50, 0, v125\n\t"
    "v_sub_f32 v66, 0, v126\n\t"
    "v_mov_b32 v82, 0\n\t"
    "v_mov_b32 v98, 0\n\t"
    "v_sub_f32 v51, 0, v125\n\t"
    "v_sub_f32 v67, 0, v126\n\t"
    "v_mov_b32 v83, 0\n\t"
    "v_mov_b32 v99, 0\n\t"
    "v_sub_f32 v52, 0, v125\n\t"
    "v_sub_f32 v68, 0, v126\n\t"
    "v_mov_b32 v84, 0\n\t"
    "v_mov_b32 v100, 0\n\t"
    "v_sub_f32 v53, 0, v125\n\t"
    "v_sub_f32 v69, 0, v126\n\t"
    "v_mov_b32 v85, 0\n\t"
    "v_mov_b32 v101, 0\n\t"
    "v_sub_f32 v54, 0, v125\n\t"
    "v_sub_f32 v70, 0, v126\n\t"
    "v_mov_b32 v86, 0\n\t"
    "v_mov_b32 v102, 0\n\t"
    "v_sub_f32 v55, 0, v125\n\t"
    "v_sub_f32 v71, 0, v126\n\t"
    "v_mov_b32 v87, 0\n\t"
    "v_mov_b32 v103, 0\n\t"
    "v_sub_f32 v56, 0, v125\n\t"
    "v_sub_f32 v72, 0, v126\n\t"
    "v_mov_b32 v88, 0\n\t"
    "v_mov_b32 v104, 0\n\t"
    "v_sub_f32 v57, 0, v125\n\t"
    "v_sub_f32 v73, 0, v126\n\t"
    "v_mov_b32 v89, 0\n\t"
    "v_mov_b32 v105, 0\n\t"
    "v_sub_f32 v58, 0, v125\n\t"
    "v_sub_f32 v74, 0, v126\n\t"
    "v_mov_b32 v90, 0\n\t"
    "v_mov_b32 v106, 0\n\t"
    "v_sub_f32 v59, 0, v125\n\t"
    "v_sub_f32 v75, 0, v126\n\t"
    "v_mov_b32 v91, 0\n\t"
    "v_mov_b32 v107, 0\n\t"
    "v_sub_f32 v60, 0, v125\n\t"
    "v_sub_f32 v76, 0, v126\n\t"
    "v_mov_b32 v92, 0\n\t"
    "v_mov_b32 v108, 0\n\t"
    "v_sub_f32 v61, 0, v125\n\t"
    "v_sub_f32 v77, 0, v126\n\t"
    "v_mov_b32 v93, 0\n\t"
    "v_mov_b32 v109, 0\n\t"
    "v_sub_f32 v62, 0, v125\n\t"
    "v_sub_f32 v78, 0, v126\n\t"
    "v_mov_b32 v94, 0\n\t"
    "v_mov_b32 v110, 0\n\t"
    "v_sub_f32 v63, 0, v125\n\t"
    "v_sub_f32 v79, 0, v126\n\t"
    "v_mov_b32 v95, 0\n\t"
    "v_mov_b32 v111, 0\n\t"
    "v_mov_b32 v112, 0\n\t"
    "v_mov_b32 v113, 0\n\t"
    "v_mov_b32 v114, 0\n\t"
    "v_mov_b32 v115, 0\n\t"
    "v_mov_b32 v116, 0\n\t"
    "v_mov_b32 v117, 0\n\t"
    "v_mov_b32 v118, 0\n\t"
    "v_mov_b32 v119, 0\n\t"
    "s_mov_b32 s60, 0\n\t"
    "SLOW2_%=:\n"
    "buffer_load_dwordx4 v[192:195], %[rb], s[40:43], s60 offen offset:0 sc1\n\t"
    "buffer_load_dwordx4 v[196:199], %[rb], s[40:43], s60 offen offset:1024 sc1\n\t"
    "buffer_load_dwordx4 v[208:211], %[rb], s[40:43], s60 offen offset:2048 sc1\n\t"
    "buffer_load_dwordx4 v[212:215], %[rb], s[40:43], s60 offen offset:3072 sc1\n\t"
    "s_waitcnt vmcnt(0)\n\t"
    "v_mfma_f32_32x32x16_bf16 v[128:143], v[192:195], v[32:35], v[48:63]\n\t"
    "v_mfma_f32_32x32x16_bf16 v[128:143], v[196:199], v[36:39], v[128:143]\n\t"
    "v_mfma_f32_32x32x16_bf16 v[144:159], v[192:195], v[40:43], v[64:79]\n\t"
    "v_mfma_f32_32x32x16_bf16 v[144:159], v[196:199], v[44:47], v[144:159]\n\t"
    "s_nop 7\n\t"
    "s_nop 1\n\t"
    "v_exp_f32 v128, v128\n\t"
    "v_exp_f32 v129, v129\n\t"
    "v_exp_f32 v130, v130\n\t"
    "v_exp_f32 v131, v131\n\t"
    "v_exp_f32 v132, v132\n\t"
    "v_exp_f32 v133, v133\n\t"
    "v_exp_f32 v134, v134\n\t"
    "v_exp_f32 v135, v135\n\t"
    "v_exp_f32 v136, v136\n\t"
    "v_exp_f32 v137, v137\n\t"
    "v_exp_f32 v138, v138\n\t"
    "v_exp_f32 v139, v139\n\t"
    "v_exp_f32 v140, v140\n\t"
    "v_exp_f32 v141, v141\n\t"
    "v_exp_f32 v142, v142\n\t"
    "v_exp_f32 v143, v143\n\t"
    "v_exp_f32 v144, v144\n\t"
    "v_exp_f32 v145, v145\n\t"
    "v_exp_f32 v146, v146\n\t"
    "v_exp_f32 v147, v147\n\t"
    "v_exp_f32 v148, v148\n\t"
    "v_exp_f32 v149, v149\n\t"
    "v_exp_f32 v150, v150\n\t"
    "v_exp_f32 v151, v151\n\t"
    "v_exp_f32 v152, v152\n\t"
    "v_exp_f32 v153, v153\n\t"
    "v_exp_f32 v154, v154\n\t"
    "v_exp_f32 v155, v155\n\t"
    "v_exp_f32 v156, v156\n\t"
    "v_exp_f32 v157, v157\n\t"
    "v_exp_f32 v158, v158\n\t"
    "v_exp_f32 v159, v159\n\t"
    "v_smfmac_f32_32x32x32_bf16 v[80:95], v[208:211], v[128:135], v124\n\t"
    "v_smfmac_f32_16x16x64_bf16 v[112:115], v[120:123], v[128:135], v124\n\t"
    "v_smfmac_f32_32x32x32_bf16 v[80:95], v[212:215], v[136:143], v124\n\t"
    "v_smfmac_f32_16x16x64_bf16 v[112:115], v[120:123], v[136:143], v124\n\t"
    "v_smfmac_f32_32x32x32_bf16 v[96:111], v[208:211], v[144:151], v124\n\t"
    "v_smfmac_f32_16x16x64_bf16 v[116:119], v[120:123], v[144:151], v124\n\t"
    "v_smfmac_f32_32x32x32_bf16 v[96:111], v[212:215], v[152:159], v124\n\t"
    "v_smfmac_f32_16x16x64_bf16 v[116:119], v[120:123], v[152:159], v124\n\t"
    "s_add_u32 s60, s60, 4096\n\t"
    "s_cmp_lt_u32 s60, 0x40000\n\t"
    "s_nop 7\n\t"
    "s_nop 7\n\t"
    "s_cbranch_scc1 SLOW2_%=\n\t"
    "s_branch FIN_%=\n\t"
    "END_%=:\n"
    ".Lfwd_end:\n"
      : [sm0] "=&v"(sm0), [sm1] "=&v"(sm1), [sm2] "=&v"(sm2), [sm3] "=&v"(sm3)
      : [pkv] "s"(pkv), [pwa] "s"(woa), [pwb] "s"(wob), [ord] "v"(ord), [po] "s"(po), [wid] "s"(wid), [tag] "v"(tag), [pf] "s"(pf), [vf1] "v"(vf1), "{a[240:255]}"(qfr), "{a[238:239]}"(ref),
        [vg] "v"(vg), [cpo] "v"(cpo), [krot] "s"(krot), [vw] "v"(vw), [wrd] "v"(wrd), [one0] "v"(one0), [one1] "v"(one1),
        [rb] "v"(rb), [vpb] "v"(vpb), [vp3] "v"(vp3), [bump] "s"(bump), [vsm] "v"(vsm), [vos] "v"(vos2), [sws] "s"(sws), [vo] "v"(vo), [vp2] "v"(vp2), [vf2] "v"(vf2),
        [vout] "v"(vout), [so] "s"(so)
      : "v32", "v33", "v34", "v35", "v36", "v37", "v38", "v39", "v40", "v41", "v42", "v43", "v44", "v45", "v46", "v47", "v48", "v49", "v50", "v51", "v52", "v53", "v54", "v55", "v56", "v57", "v58", "v59", "v60", "v61", "v62", "v63", "v64", "v65", "v66", "v67", "v68", "v69", "v70", "v71", "v72", "v73", "v74", "v75", "v76", "v77", "v78", "v79", "v80", "v81", "v82", "v83", "v84", "v85", "v86", "v87", "v88", "v89", "v90", "v91", "v92", "v93", "v94", "v95", "v96", "v97", "v98", "v99", "v100", "v101", "v102", "v103", "v104", "v105", "v106", "v107", "v108", "v109", "v110", "v111", "v112", "v113", "v114", "v115", "v116", "v117", "v118", "v119", "v120", "v121", "v122", "v123", "v124", "v125", "v126", "v127", "v128", "v129", "v130", "v131", "v132", "v133", "v134", "v135", "v136", "v137", "v138", "v139", "v140", "v141", "v142", "v143", "v144", "v145", "v146", "v147", "v148", "v149", "v150", "v151", "v152", "v153", "v154", "v155", "v156", "v157", "v158", "v159", "v160", "v161", "v162", "v163", "v164", "v165", "v166", "v167", "v168", "v169", "v170", "v171", "v172", "v173", "v174", "v175", "v176", "v177", "v178", "v179", "v180", "v181", "v182", "v183", "v184", "v185", "v186", "v187", "v188", "v189", "v190", "v191", "v192", "v193", "v194", "v195", "v196", "v197", "v198", "v199", "v200", "v201", "v202", "v203", "v204", "v205", "v206", "v207", "v208", "v209", "v210", "v211", "v212", "v213", "v214", "v215", "v216", "v217", "v218", "v219", "v220", "v221", "v222", "v223", "v224", "v225", "v226", "v227", "v228", "v229", "v230", "v231", "v232", "v233", "v234", "v235", "v236", "v237", "v238", "v239", "v240", "v241", "v242", "v243", "v244", "v245", "v246", "v247", "v248", "v249", "v250", "v251", "v252", "v253", "v254", "v255", "a0", "a1", "a2", "a3", "a4", "a5", "a6", "a7", "a8", "a9", "a10", "a11", "a12", "a13", "a14", "a15", "a16", "a17", "a18", "a19", "a20", "a21", "a22", "a23", "a24", "a25", "a26", "a27", "a28", "a29", "a30", "a31", "a32", "a33", "a34", "a35", "a36", "a37", "a38", "a39", "a40", "a41", "a42", "a43", "a44", "a45", "a46", "a47", "a48", "a49", "a50", "a51", "a52", "a53", "a54", "a55", "a56", "a57", "a58", "a59", "a60", "a61", "a62", "a63", "a64", "a65", "a66", "a67", "a68", "a69", "a70", "a71", "a72", "a73", "a74", "a75", "a76", "a77", "a78", "a79", "a80", "a81", "a82", "a83", "a84", "a85", "a86", "a87", "a88", "a89", "a90", "a91", "a92", "a93", "a94", "a95", "a96", "a97", "a98", "a99", "a100", "a101", "a102", "a103", "a104", "a105", "a106", "a107", "a108", "a109", "a110", "a111", "a112", "a113", "a114", "a115", "a116", "a117", "a118", "a119", "a120", "a121", "a122", "a123", "a124", "a125", "a126", "a127", "a128", "a129", "a130", "a131", "a132", "a133", "a134", "a135", "a136", "a137", "a138", "a139", "a140", "a141", "a142", "a143", "a144", "a145", "a146", "a147", "a148", "a149", "a150", "a151", "a152", "a153", "a154", "a155", "a156", "a157", "a158", "a159", "a160", "a161", "a162", "a163", "a164", "a165", "a166", "a167", "a168", "a169", "a170", "a171", "a172", "a173", "a174", "a175", "a176", "a177", "a178", "a179", "a180", "a181", "a182", "a183", "a184", "a185", "a186", "a187", "a188", "a189", "a190", "a191", "a192", "a193", "a194", "a195", "a196", "a197", "a198", "a199", "a200", "a201", "a202", "a203", "a204", "a205", "a206", "a207", "a208", "a209", "a210", "a211", "a212", "a213", "a214", "a215", "a216", "a217", "a218", "a219", "a220", "a221", "a222", "a223", "a224", "a225", "a226", "a227", "a228", "a229", "a230", "a231", "a232", "a233", "a234", "a235", "a236", "a237", "s40", "s41", "s42", "s43", "s44", "s45", "s46", "s47", "s48", "s49", "s50", "s51", "s52", "s53", "s54", "s55", "s56", "s57", "s58", "s59", "s60", "s61", "s62", "s63", "s92", "s93", "s94", "s95", "scc", "vcc", "memory");
  }
  if (bump == 0u) {
    const bool seen = __all(sm0 == tag && sm1 == tag && sm2 == tag && sm3 == tag);
    for (unsigned spins = 0; !seen && spins < (1u << 18); ++spins) {
      bool ok = true;
#pragma unroll
      for (unsigned i = 0; i < 4u; ++i)
        ok &= __hip_atomic_load((gu32*)(pf + F_OA + (2u * i + (l >> 5)) * 4096u + spread(l & 31u, FS2)), __ATOMIC_RELAXED, __HIP_MEMORY_SCOPE_AGENT) == tag;
      if (__all(ok)) break;
      __builtin_amdgcn_s_sleep(1);
    }
    if (l == 0u) __hip_atomic_store((gu32*)pf, tag, __ATOMIC_RELAXED, __HIP_MEMORY_SCOPE_AGENT);
  }
}

extern "C" void kernel_launch(void* const* d_in, const int* in_sizes, int n_in, void* d_out, int out_size, void* d_ws, size_t ws_size,
                              hipStream_t stream) {
  (void)in_sizes; (void)n_in; (void)out_size; (void)ws_size;
  fwd_kernel<<<256, 256, 0, stream>>>((const char*)d_in[0], (const char*)d_in[1], (const char*)d_in[2], (const char*)d_in[3],
                                      (const char*)d_in[4], (const char*)d_in[5], (char*)d_out, (char*)d_ws);
}
```
